```python
import torch

def _softmax(x, axis=-1):
    return torch.softmax(x, dim=axis)
def _gelu(x):
    return torch.nn.functional.gelu(x, approximate="tanh")
def _silu(x):
    return torch.nn.functional.silu(x)
def _sigmoid(x):
    return torch.sigmoid(x)
def _layernorm(x, axis=-1, eps=1e-5):
    m = x.mean(dim=axis, keepdim=True)
    v = x.var(dim=axis, keepdim=True, unbiased=False)
    return (x - m) / torch.sqrt(v + eps)
def _scatter_add(tgt, idx, src, axis=0):
    return tgt.clone().index_add_(axis, idx, src)
def _segment_sum(src, seg, n):
    out = torch.zeros((n,) + src.shape[1:], dtype=src.dtype)
    return out.index_add_(0, seg, src)
def _pad(x, pw):
    flat = tuple(v for pair in reversed(pw) for v in pair)
    return torch.nn.functional.pad(x, flat)

kernel_name = 'gated_mix__mha_block__dense_activation_relu__prompt'

def setup_inputs():

    _w = (lambda a: (lambda t: t.to(torch.bfloat16) if t.is_floating_point() else t)(torch.as_tensor(a)))
    def _A_setup():
        B, S, D, H = 1, 2048, 512, 32
        return {
            "x": _w((lambda *s: torch.randn(*s, dtype=torch.bfloat16) * 0.05)(B, S, D)),
            "wqkv": _w((lambda *s: torch.randn(*s, dtype=torch.bfloat16) * 0.8)(D, 3 * D)),
            "wo": _w((lambda *s: torch.randn(*s, dtype=torch.bfloat16) * 0.2)(D, D)),
        }
    _A_in = _A_setup()

    def _B_setup():
        B, S, D, Dff = 1, 2048, 512, 1792
        return {
            "x": _w((lambda *s: torch.randn(*s, dtype=torch.bfloat16) * 0.05)(B, S, D)),
            "w1": _w((lambda *s: torch.randn(*s, dtype=torch.bfloat16) * 0.05)(D, Dff)),
            "w2": _w((lambda *s: torch.randn(*s, dtype=torch.bfloat16) * 0.05)(Dff, D)),
        }
    _B_in = _B_setup()

    return {
        "A_x": _A_in["x"],
        "A_wqkv": _A_in["wqkv"],
        "A_wo": _A_in["wo"],
        "B_x": _B_in["x"],
        "B_w1": _B_in["w1"],
        "B_w2": _B_in["w2"],
        "gate_w": _w((lambda *s: torch.randn(*s, dtype=torch.bfloat16) * 0.05)(512) * 16.0),
    }

def reference(A_x, A_wqkv, A_wo, B_x, B_w1, B_w2, gate_w):
    def _A_ref(x, wqkv, wo):
        B, S, D = x.shape
        H = 32
        Dh = D // H
        qkv = (lambda x, *d: x.permute(*d))((x @ wqkv).reshape(B, S, 3, H, Dh), 2, 0, 3, 1, 4)
        q, k, v = qkv[0], qkv[1], qkv[2]
        scores = torch.einsum("bhqd,bhkd->bhqk", q, k) * (Dh ** -0.5)
        p = _softmax(scores, axis=-1)
        o = torch.einsum("bhqk,bhkd->bhqd", p, v)
        return (lambda x, *d: x.permute(*d))(o, 0, 2, 1, 3).reshape(B, S, D) @ wo

    def _B_ref(x, w1, w2):
        h = torch.einsum("bsd,df->bsf", x, w1)
        zero = torch.zeros_like(h)
        h = torch.maximum(h, zero)
        return torch.einsum("bsf,fd->bsd", h, w2)

    x = A_x
    g = _sigmoid((x * gate_w).sum(-1, keepdims=True))
    a = _A_ref(x, A_wqkv, A_wo)
    b = _B_ref(x, B_w1, B_w2)
    return g * a + (1 - g) * b
```

```cpp
#include <hip/hip_runtime.h>
#include <hip/hip_bf16.h>
#include <stdint.h>
#include <utility>

#define LAS __attribute__((address_space(3)))
#define GAS __attribute__((address_space(1)))
typedef unsigned u32x4 __attribute__((ext_vector_type(4)));
typedef unsigned u32x2 __attribute__((ext_vector_type(2)));
typedef float f32x4 __attribute__((ext_vector_type(4)));
typedef float f32x8 __attribute__((ext_vector_type(8)));
typedef float f32x16 __attribute__((ext_vector_type(16)));
typedef __bf16 bf16x8 __attribute__((ext_vector_type(8)));
typedef __bf16 bf16x16 __attribute__((ext_vector_type(16)));
typedef __bf16 bf16x2 __attribute__((ext_vector_type(2)));
typedef float f32x2 __attribute__((ext_vector_type(2)));
typedef short s16x4 __attribute__((ext_vector_type(4)));
typedef LAS char* ldsp;

#define GM_STAMPS 0

namespace gm {
constexpr int SEQ = 2048, DM = 512, NH = 32, DFF = 1792;
constexpr int WS_EPOCH = 0;
constexpr int WS_ACK = 4096;
constexpr int WS_KVF = 8192;
constexpr int WS_HF = 16384;
constexpr int WS_OF = 24576;
constexpr int WS_G = 32768;
constexpr int WS_KV = 1 << 20;
constexpr int WS_Z = WS_KV + (4 << 20);
constexpr int ZT = 144 * 1024;
constexpr int WS_END = WS_Z + 64 * ZT;
constexpr int WS_STAMP = 16 << 20;
constexpr int LDS_BYTES = 81920;
constexpr int XSTG = 16640;
constexpr int L_X = 0, L_W = 3 * XSTG;
constexpr int L_GI = L_W + 4 * 6144;
constexpr int L_MISC = L_GI + 4096;
constexpr int L_GX = L_MISC, L_CNT = L_MISC + 1024, L_TAG = L_MISC + 1024 + 64;
constexpr int TILEB = 2064, SLOTB = 4 * TILEB, RINGB = 4 * SLOTB;
constexpr unsigned SPIN_CAP = 1u << 14;
constexpr float QSCALE = 0.25f * 1.4426950408889634f, LOG2E = 1.4426950408889634f;
constexpr int AUX_SC1 = 16;

template <int B, int E, class F> __device__ __forceinline__ void static_for(F&& f) {
    if constexpr (B < E) { f(std::integral_constant<int, B>{}); static_for<B + 1, E>(f); }
}
#define SBAR() __builtin_amdgcn_sched_barrier(0)
#define PIN(x) asm volatile("" : "+v"(x))
#define MFMA(a, b, c) __builtin_amdgcn_mfma_f32_32x32x16_bf16(a, b, c, 0, 0, 0)

__device__ __forceinline__ u32x4 mk_rsrc(const void* p, unsigned bytes) {
    const uint64_t a = (uint64_t)p;
    u32x4 r = {(unsigned)a, (unsigned)(a >> 32) & 0xffffu, bytes, 0x00020000u};
    return r;
}
__device__ __forceinline__ void dma16(u32x4 rs, unsigned voff, unsigned soff, unsigned ldsdst) {
    unsigned sv;
    asm volatile("s_mov_b32 %0, m0\n\ts_mov_b32 m0, %4\n\ts_nop 0\n\tbuffer_load_dwordx4 %1, %2, %3 offen lds\n\ts_mov_b32 m0, %0"
                 : "=&s"(sv) : "v"(voff), "s"(rs), "s"(soff), "s"(ldsdst) : "memory");
}
template <int N> __device__ __forceinline__ void wait_bar() {
    asm volatile("s_waitcnt vmcnt(%0) lgkmcnt(0)\n\ts_barrier" ::"n"(N) : "memory");
}
__device__ __forceinline__ void ld2_sc1(u32x4& a, u32x4& b, u32x4 rs, unsigned voff, unsigned soff) {
    asm volatile("buffer_load_dwordx4 %0, %1, %2, %3 offen sc1" : "=v"(a) : "v"(voff), "s"(rs), "s"(soff));
    asm volatile("buffer_load_dwordx4 %0, %1, %2, %3 offen offset:1024 sc1" : "=v"(b) : "v"(voff), "s"(rs), "s"(soff));
}
template <int N> __device__ __forceinline__ void wait_bar2(u32x4& a, u32x4& b) {
    asm volatile("s_waitcnt vmcnt(%2) lgkmcnt(0)\n\ts_barrier" : "+v"(a), "+v"(b) : "n"(N) : "memory");
}
template <int N> __device__ __forceinline__ void wait2(u32x4& a, u32x4& b) {
    asm volatile("s_waitcnt vmcnt(%2)" : "+v"(a), "+v"(b) : "n"(N) : "memory");
}
__device__ __forceinline__ void drain() { asm volatile("s_waitcnt vmcnt(0)" ::: "memory"); }
__device__ __forceinline__ void wg_bar() { asm volatile("s_waitcnt lgkmcnt(0)\n\ts_barrier" ::: "memory"); }
__device__ __forceinline__ bf16x8 lds128(ldsp p) { return *(const LAS bf16x8*)p; }
__device__ __forceinline__ bf16x8 lds_tr2(ldsp p0, ldsp p1) {
    s16x4 a = __builtin_amdgcn_ds_read_tr16_b64_v4i16((LAS s16x4*)p0);
    s16x4 b = __builtin_amdgcn_ds_read_tr16_b64_v4i16((LAS s16x4*)p1);
    return __builtin_bit_cast(bf16x8, __builtin_shufflevector(a, b, 0, 1, 2, 3, 4, 5, 6, 7));
}
__device__ __forceinline__ unsigned pk2(float lo, float hi) {
    f32x2 v = {lo, hi};
    return __builtin_bit_cast(unsigned, __builtin_convertvector(v, bf16x2));
}
template <int O> __device__ __forceinline__ u32x4 pack8(const f32x16& a, float s) {
    u32x4 r = {pk2(a[O] * s, a[O + 1] * s), pk2(a[O + 2] * s, a[O + 3] * s), pk2(a[O + 4] * s, a[O + 5] * s), pk2(a[O + 6] * s, a[O + 7] * s)};
    return r;
}
template <int O> __device__ __forceinline__ u32x4 pack8(const f32x16& a) {
    u32x4 r = {pk2(a[O], a[O + 1]), pk2(a[O + 2], a[O + 3]), pk2(a[O + 4], a[O + 5]), pk2(a[O + 6], a[O + 7])};
    return r;
}
__device__ __forceinline__ unsigned ld_flag(__amdgpu_buffer_rsrc_t rs, int off) { return __builtin_amdgcn_raw_buffer_load_b32(rs, off, 0, AUX_SC1); }
__device__ __forceinline__ void st_flag(__amdgpu_buffer_rsrc_t rs, int off, unsigned v) { __builtin_amdgcn_raw_buffer_store_b32(v, rs, off, 0, AUX_SC1); }
__device__ __forceinline__ void wait_flags(__amdgpu_buffer_rsrc_t rs, int off, unsigned tag) {
#pragma unroll 1
    for (unsigned spins = 0; spins < SPIN_CAP; ++spins) {
        asm volatile("" ::: "memory");
        const unsigned v = ld_flag(rs, off);
        if (__all(v == tag)) break;
        __builtin_amdgcn_s_sleep(2);
    }
    __builtin_amdgcn_fence(__ATOMIC_ACQUIRE, "wavefront");
}
#define STAMP(i) do { } while (0)
#define CSTAMP(i) do { } while (0)

constexpr int gemma_wait(int j, int NWR, bool hasW) {
    int seq[64] = {}; int n = 0;
    seq[n++] = 100; seq[n++] = 100;
    if (hasW) { seq[n++] = 0; seq[n++] = 1; }
    seq[n++] = 101; seq[n++] = 101;
    if (hasW) { seq[n++] = 2; if (NWR == 4) seq[n++] = 3; }
    seq[n++] = 102; seq[n++] = 102;
    for (int i = 0; i < j; ++i) {
        if (hasW && i >= 1 && i + NWR - 1 <= 15) seq[n++] = i + NWR - 1;
        if ((i & 1) == 0 && i >= 2 && i / 2 + 2 <= 7) { seq[n++] = 100 + i / 2 + 2; seq[n++] = 100 + i / 2 + 2; }
    }
    int last = -1;
    for (int i = 0; i < n; ++i) if (seq[i] == 100 + j / 2 || seq[i] == j) last = i;
    return n - 1 - last;
}

template <int ROLE>
__device__ __forceinline__ void gemm_a(ldsp L, unsigned lds0, u32x4 rx, u32x4 rw, int row0, int colb, int w, int lane,
                                       f32x16& acc0, f32x16& acc1, unsigned gate_raw, unsigned& epoch_raw) {
    constexpr int NSEG = ROLE == 0 ? 3 : 4, NWR = ROLE == 0 ? 4 : 3, WU = NSEG * 2048, PITCH = ROLE == 0 ? 3072 : 3584;
    const int tt = w & 3, sel = w >> 2, r = lane & 31, hf = lane >> 5;
    const bool hasW = ROLE == 1 || w < 6;
    const unsigned xv = (unsigned)((row0 + 32 * (lane >> 4) + ((lane >> 3) & 1)) * 1024 + (lane & 7) * 16);
    const unsigned xs = (unsigned)(w * 4096), xd = lds0 + L_X + (unsigned)(w * 2080);
    unsigned wv;
    { const int blk = 4 * w + (lane >> 4), rw_ = (lane >> 2) & 3, pc = lane & 3;
      if (ROLE == 0) { const int kq = blk / 3, sg = blk % 3; wv = (unsigned)((4 * kq + rw_) * PITCH + sg * 1024 + colb + ((pc ^ (sg == 1 ? 2 : 0)) * 16)); }
      else wv = (unsigned)((4 * w + rw_) * PITCH + colb + (lane >> 4) * 64 + pc * 16); }
    const unsigned wd = lds0 + L_W + (unsigned)(w * 1024);
#define DX(s) do { dma16(rx, xv, xs + (s) * 128, xd + ((s) % 3) * XSTG); dma16(rx, xv, xs + (s) * 128 + 2048, xd + ((s) % 3) * XSTG + 1040); } while (0)
#define DW(u) do { if (hasW) dma16(rw, wv, (unsigned)((u) * 32 * PITCH), wd + ((u) % NWR) * WU); } while (0)
    DX(0); DW(0); DW(1); DX(1); DW(2); if (NWR == 4) DW(3); DX(2);
    if (ROLE == 0) {
        asm volatile("s_waitcnt vmcnt(6)" : "+v"(gate_raw), "+v"(epoch_raw));
        u32x2 gi = {gate_raw & 0xffffu, 0u};
        *(LAS u32x2*)(L + L_GI + (w * 64 + lane) * 8) = gi;
    }
    const ldsp xb = L + L_X + (r >> 1) * 1040 + (2 * tt + (r & 1)) * 128 + hf * 16;
    const int ch = (lane >> 4) & 1, q = (lane >> 2) & 3, p = lane & 3;
    ldsp wb0, wb1; int st1kq, st1u;
    if (ROLE == 0) {
        wb0 = L + L_W + (2 * hf * NSEG + ch) * 256 + q * 64 + (ch ? 1 - sel : sel) * 32 + p * 8;
        wb1 = ch ? L + L_GI + (8 * hf + q) * 8 : L + L_W + (2 * hf * NSEG + 2) * 256 + q * 64 + sel * 32 + p * 8;
        st1kq = ch ? 32 : NSEG * 256;
    } else {
        wb0 = L + L_W + (2 * hf * NSEG + 2 * sel) * 256 + q * 64 + ch * 32 + p * 8;
        wb1 = wb0 + 256; st1kq = NSEG * 256;
    }
    (void)st1u;
    bf16x8 xf[2][2], f0[2][2], f1[2][2];
    static_for<0, 17>([&](auto J) {
        constexpr int j = J;
        if constexpr (j < 16) {
            if (hasW) wait_bar<gemma_wait(j, NWR, true)>(); else wait_bar<gemma_wait(j, NWR, false)>();
            if constexpr (j >= 1 && j + NWR - 1 <= 15) DW(j + NWR - 1);
            if constexpr ((j & 1) == 0 && j >= 2 && j / 2 + 2 <= 7) DX(j / 2 + 2);
            constexpr int xo = ((j / 2) % 3) * XSTG + (j & 1) * 64, wo_ = (j % NWR) * WU;
            const int u1 = ROLE == 0 ? (ch ? j * 256 : wo_) : wo_;
#pragma unroll
            for (int k = 0; k < 2; ++k) {
                xf[j & 1][k] = lds128(xb + xo + k * 32);
                f0[j & 1][k] = lds_tr2(wb0 + wo_ + (4 * k) * NSEG * 256, wb0 + wo_ + (4 * k + 1) * NSEG * 256);
                f1[j & 1][k] = lds_tr2(wb1 + u1 + (4 * k) * st1kq, wb1 + u1 + (4 * k + 1) * st1kq);
            }
            SBAR();
        }
        if constexpr (j >= 1) {
            constexpr int b = (j - 1) & 1;
#pragma unroll
            for (int k = 0; k < 2; ++k) {
                acc0 = MFMA(f0[b][k], xf[b][k], acc0);
                if (ROLE == 0) acc1 = MFMA(xf[b][k], f1[b][k], acc1); else acc1 = MFMA(f1[b][k], xf[b][k], acc1);
            }
            SBAR();
        }
    });
#undef DX
#undef DW
}

__device__ __forceinline__ f32x16 smf(bf16x8 a, f32x8 b, f32x16 c) {
    return __builtin_amdgcn_smfmac_f32_32x32x32_bf16(a, __builtin_bit_cast(bf16x16, b), c, 0xDDDD, 0, 0);
}
__device__ __forceinline__ f32x8 lo8(const f32x16& s) { return __builtin_shufflevector(s, s, 0, 1, 2, 3, 4, 5, 6, 7); }
__device__ __forceinline__ f32x8 hi8(const f32x16& s) { return __builtin_shufflevector(s, s, 8, 9, 10, 11, 12, 13, 14, 15); }
__device__ __forceinline__ float rowmax16(const f32x16& s) {
    float a = __builtin_fmaxf(__builtin_fmaxf(s[0], s[1]), s[2]), b = __builtin_fmaxf(__builtin_fmaxf(s[3], s[4]), s[5]);
    a = __builtin_fmaxf(__builtin_fmaxf(a, s[6]), s[7]); b = __builtin_fmaxf(__builtin_fmaxf(b, s[8]), s[9]);
    a = __builtin_fmaxf(__builtin_fmaxf(a, s[10]), s[11]); b = __builtin_fmaxf(__builtin_fmaxf(b, s[12]), s[13]);
    a = __builtin_fmaxf(__builtin_fmaxf(a, s[14]), s[15]);
    return __builtin_fmaxf(a, b);
}
__device__ __forceinline__ float xhalf_max(float m) {
    auto rr = __builtin_amdgcn_permlane32_swap(__float_as_uint(m), __float_as_uint(m), false, false);
    return __builtin_fmaxf(__uint_as_float(rr[0]), __uint_as_float(rr[1]));
}
__device__ __forceinline__ f32x16 splat16(float v) { f32x16 r; for (int i = 0; i < 16; ++i) r[i] = v; return r; }
#define EXPR(S, a, b) do { _Pragma("unroll") for (int i_ = (a); i_ < (b); ++i_) S[i_] = __builtin_amdgcn_exp2f(S[i_]); PIN(S); SBAR(); } while (0)

#define A_EX(b, i) "v_exp_f32 v[" #b "+" #i "], v[" #b "+" #i "]\n\t"
#define A_QK(sp) "v_mfma_f32_32x32x16_bf16 v[" #sp ":" #sp "+15], %[kf], %[qf], %[cm]\n\t"
#define A_SMH(sp) "v_smfmac_f32_32x32x32_bf16 %[o], %[v1], v[" #sp "+8:" #sp "+15], %[ix]\n\t"
#define A_SML(sc) "v_smfmac_f32_32x32x32_bf16 %[o], %[v0], v[" #sc ":" #sc "+7], %[ix]\n\t"
#define A_RK(off) "ds_read_b128 %[kf], %[ak] offset:" off "\n\t"
#define A_R0(off) "ds_read_b128 %[v0], %[a0] offset:" off "\n\t"
#define A_R1(off) "ds_read_b128 %[v1], %[a1] offset:" off "\n\t"
#define A_LG(n) "s_waitcnt lgkmcnt(" #n ")\n\t"
#define A_SECT(sc, sp, SMH, R1, QK, RK, R0, XTRA) \
    A_EX(sc, 0) A_EX(sc, 1) A_EX(sc, 2) SMH R1 A_EX(sc, 3) A_EX(sc, 4) A_EX(sc, 5) QK RK \
    A_EX(sc, 6) A_EX(sc, 7) A_EX(sc, 8) A_EX(sc, 9) A_EX(sc, 10) A_LG(2) A_SML(sc) R0 XTRA \
    A_EX(sc, 11) A_EX(sc, 12) A_EX(sc, 13) A_EX(sc, 14) A_EX(sc, 15)
#define A_FULL(sc, sp, o0, o1, o2, XTRA) A_SECT(sc, sp, A_LG(1) A_SMH(sp), A_R1(o0), A_QK(sp), A_RK(o2), A_R0(o1), XTRA)
#define A_PUT(off) \
    "s_waitcnt vmcnt(0)\n\tds_write_b128 %[ap], %[pk] offset:" off "\n\tds_write_b128 %[ap], %[pv] offset:" off "+1024\n\t" \
    "s_mov_b64 exec, 1\n\tds_add_u32 %[ac], %[one]\n\ts_mov_b64 exec, -1\n\t"
#define A_FETCH(id) \
    "s_xor_b32 %[st], %[rb], %[nc]\n\ts_bitcmp1_b32 %[rd], %[st]\n\ts_cbranch_scc0 .Lpoll" #id "_%=\n" \
    ".Lpolled" #id "_%=:\n\ts_lshl_b32 %[st], %[st], 13\n\ts_add_u32 %[nc], %[nc], 1\n\t" \
    "buffer_load_dwordx4 %[pk], %[gs], %[rs], %[st] offen sc1\n\tbuffer_load_dwordx4 %[pv], %[gs], %[rs], %[st] offen offset:1024 sc1\n\t"
#define A_POLL(id) \
    ".Lpoll" #id "_%=:\n\tbuffer_load_dword %[tm], %[gf], %[rs], 0 offen sc1\n\ts_waitcnt vmcnt(0)\n\t" \
    "v_cmp_eq_u32 vcc, %[tag], %[tm]\n\ts_nop 1\n\ts_mov_b32 %[rd], vcc_lo\n\ts_bitcmp1_b32 %[rd], %[st]\n\ts_cbranch_scc1 .Lpolled" #id "_%=\n\t" \
    "s_sub_u32 %[sp], %[sp], 16\n\ts_cbranch_scc1 .Lgiveup_%=\n\ts_sleep 1\n\ts_branch .Lpoll" #id "_%=\n"
#define A_SYNC(id) \
    "v_readfirstlane_b32 %[st], %[tm]\n\ts_nop 0\n\ts_cmp_ge_u32 %[st], %[wt]\n\ts_cbranch_scc0 .Lsync" #id "_%=\n" \
    ".Lsynced" #id "_%=:\n\ts_add_u32 %[wt], %[wt], 4\n\t"
#define A_SYNCSLOW(id) \
    ".Lsync" #id "_%=:\n\tds_read_b32 %[tm], %[ac]\n\ts_waitcnt lgkmcnt(0)\n\tv_readfirstlane_b32 %[st], %[tm]\n\ts_nop 0\n\t" \
    "s_cmp_ge_u32 %[st], %[wt]\n\ts_cbranch_scc1 .Lsynced" #id "_%=\n\t" \
    "s_sub_u32 %[sp], %[sp], 1\n\ts_cbranch_scc1 .Lgiveup_%=\n\ts_sleep 1\n\ts_branch .Lsync" #id "_%=\n"
#define A_RC "ds_read_b32 %[tm], %[ac]\n\t"
#define T0(s) #s "*8256"
#define T1(s) #s "*8256+2064"
#define T2(s) #s "*8256+4128"
#define T3(s) #s "*8256+6192"
#define A_CHUNK(s, n, MID) \
    A_FULL(32, 48, T0(s), T1(s), T2(s), A_RC) A_FULL(48, 32, T1(s), T2(s), T3(s), "") MID \
    A_FULL(32, 48, T2(s), T3(s), T0(n), "") A_FULL(48, 32, T3(s), T0(n), T1(n), "")
#define A_TS(n) ""
#define A_LAST(lbl) "s_cmp_eq_u32 %[nc], 16\n\ts_cbranch_scc1 " lbl "_%=\n\t"
__device__ __forceinline__ void attn_loop(f32x16& o, const f32x16& cm, bf16x8 qf, unsigned a_k, unsigned a_v0, unsigned a_v1, unsigned a_put,
                                          unsigned a_cnt, unsigned g_flag, unsigned g_src, u32x4 rs, unsigned tag, int rb, char* ws) {
    u32x4 kf, v0, v1, pk, pv; unsigned tm, st, rd, nc, wt, sp;
    unsigned long long t0, t1, t2;
    asm volatile(
        "s_mov_b32 %[rd], 0\n\ts_mov_b32 %[nc], 1\n\ts_mov_b32 %[wt], 4\n\ts_mov_b32 %[sp], 0x40000\n\t"
        A_RK(T0(0)) A_R0(T0(0)) A_LG(1) A_QK(32) A_RK(T1(0)) "s_nop 11\n\t"
        A_SECT(32, 48, "", A_R1(T0(0)), A_LG(1) A_QK(48), A_RK(T2(0)), A_R0(T1(0)), "")
        A_FULL(48, 32, T1(0), T2(0), T3(0), "")
        A_SECT(32, 48, A_LG(1) A_SMH(48), A_R1(T2(0)), A_QK(48), A_LG(1), A_R0(T3(0)), "")
        A_SECT(48, 32, A_LG(1) A_SMH(32), A_R1(T3(0)), "", A_LG(1), "", "")
        A_LG(0) "s_nop 0\n\t" A_SMH(48)
        A_TS(0) A_FETCH(0) A_TS(1) A_PUT(T0(1)) A_FETCH(1) A_RC A_LG(0) A_SYNC(0) A_PUT(T0(2)) A_FETCH(2) A_TS(2)
        A_RK(T0(1)) A_R0(T0(1)) A_LG(1) A_QK(32) A_RK(T1(1)) "s_nop 11\n\t"
        A_SECT(32, 48, "", A_R1(T0(1)), A_LG(1) A_QK(48), A_RK(T2(1)), A_R0(T1(1)), A_RC)
        "s_branch .Lentry_%=\n"
        ".Lloop_%=:\n\t"
        A_CHUNK(0, 1, A_SYNC(1) A_PUT(T0(2)) A_FETCH(3))
        A_FULL(32, 48, T0(1), T1(1), T2(1), A_RC)
        ".Lentry_%=:\n\t"
        A_FULL(48, 32, T1(1), T2(1), T3(1), "")
        A_SYNC(2) A_PUT(T0(3)) A_LAST(".Lk1") A_FETCH(4) ".Lk1_%=:\n\t"
        A_FULL(32, 48, T2(1), T3(1), T0(2), "") A_FULL(48, 32, T3(1), T0(2), T1(2), "")
        A_CHUNK(2, 3, A_SYNC(3) "s_cmp_eq_u32 %[wt], 64\n\ts_cbranch_scc1 .Lk2_%=\n\t" A_PUT(T0(0)) A_FETCH(5) ".Lk2_%=:\n\t")
        A_CHUNK(3, 0, "s_cmp_eq_u32 %[wt], 64\n\ts_cbranch_scc1 .Lk3_%=\n\t" A_SYNC(4) A_PUT(T0(1)) A_FETCH(6) ".Lk3_%=:\n\t")
        "s_cmp_lg_u32 %[wt], 64\n\ts_cbranch_scc1 .Lloop_%=\n\t"
        A_LG(0) "s_nop 0\n\t" A_SMH(48)
        "s_branch .Lgiveup_%=\n"
        A_POLL(0) A_POLL(1) A_POLL(2) A_POLL(3) A_POLL(4) A_POLL(5) A_POLL(6)
        A_SYNCSLOW(0) A_SYNCSLOW(1) A_SYNCSLOW(2) A_SYNCSLOW(3) A_SYNCSLOW(4)
        ".Lgiveup_%=:\n\ts_waitcnt vmcnt(0) lgkmcnt(0)\n\ts_nop 11"
        : [o] "+v"(o), [kf] "=&v"(kf), [v0] "=&v"(v0), [v1] "=&v"(v1), [pk] "=&v"(pk), [pv] "=&v"(pv), [tm] "=&v"(tm),
          [st] "=&s"(st), [rd] "=&s"(rd), [nc] "=&s"(nc), [wt] "=&s"(wt), [sp] "=&s"(sp), [t0] "=&s"(t0), [t1] "=&s"(t1), [t2] "=&s"(t2)
        : [cm] "v"(cm), [qf] "v"(qf), [ak] "v"(a_k), [a0] "v"(a_v0), [a1] "v"(a_v1), [ap] "v"(a_put), [ac] "v"(a_cnt),
          [gf] "v"(g_flag), [gs] "v"(g_src), [ix] "v"(0xDDDD), [one] "v"(1), [rs] "s"(rs), [tag] "s"(tag), [rb] "s"(rb)
        : "memory", "vcc", "scc", "v32", "v33", "v34", "v35", "v36", "v37", "v38", "v39", "v40", "v41", "v42", "v43", "v44", "v45", "v46", "v47",
          "v48", "v49", "v50", "v51", "v52", "v53", "v54", "v55", "v56", "v57", "v58", "v59", "v60", "v61", "v62", "v63");
}

__device__ __forceinline__ void role_a(const void* x, const void* wqkv, const void* gate_w, char* ws, ldsp L, unsigned lds0) {
    const int a = blockIdx.x, g8 = a & 7, idx = a >> 3;
    const int hp = (g8 & 3) * 4 + (idx & 3), rb = (g8 >> 2) * 8 + (idx >> 2);
    const int lane = threadIdx.x & 63, w = __builtin_amdgcn_readfirstlane(threadIdx.x >> 6);
    const int tt = w & 3, hs = w >> 2, r = lane & 31, hf = lane >> 5, head = 2 * hp + hs;
    const __amdgpu_buffer_rsrc_t rs = __builtin_amdgcn_make_buffer_rsrc(ws, 0, WS_END, 0x00020000);
    STAMP(0); CSTAMP(14);
    unsigned gate_raw, epoch;
    { const u32x4 rg = mk_rsrc(gate_w, DM * 2), re = mk_rsrc(ws, 64);
      asm volatile("buffer_load_ushort %0, %1, %2, 0 offen" : "=v"(gate_raw) : "v"(threadIdx.x * 2), "s"(rg));
      asm volatile("buffer_load_dword %0, off, %1, 0 sc1" : "=v"(epoch) : "s"(re)); }
    if (threadIdx.x < 2) *(LAS unsigned*)(L + L_CNT + threadIdx.x * 32) = 0u;
    f32x16 aqk = {}, av = {};
    gemm_a<0>(L, lds0, mk_rsrc(x, SEQ * DM * 2), mk_rsrc(wqkv, DM * 3 * DM * 2), rb * 128, hp * 64, w, lane, aqk, av, gate_raw, epoch);
    const unsigned tag = __builtin_amdgcn_readfirstlane(epoch) + 1u;
    STAMP(1); CSTAMP(12);
    const bf16x8 qf = __builtin_bit_cast(bf16x8, pack8<0>(aqk, QSCALE));
    const u32x4 kpk = pack8<8>(aqk);
    u32x4 vpk;
    { const u32x4 v0 = pack8<0>(av), v1 = pack8<8>(av);
#pragma unroll
      for (int i = 0; i < 4; ++i) { auto sw = __builtin_amdgcn_permlane16_swap(v0[i], v1[i], false, false); vpk[i] = sw[0]; } }
    const int kslot = (r & 19) | ((r & 4) << 1) | ((r & 8) >> 1);
    const int koff = hf * 512 + kslot * 16;
    const int gkv = WS_KV + (head * 64 + rb * 4 + tt) * 2048;
    __builtin_amdgcn_raw_buffer_store_b128(kpk, rs, gkv + koff, 0, AUX_SC1);
    __builtin_amdgcn_raw_buffer_store_b128(vpk, rs, gkv + 1024 + lane * 16, 0, AUX_SC1);
    if (r == 16) {
#pragma unroll
        for (int i = 0; i < 4; ++i) { f32x4 t = {av[4 * i], av[4 * i + 1], av[4 * i + 2], av[4 * i + 3]}; *(LAS f32x4*)(L + L_GX + w * 128 + (8 * i + 4 * hf) * 4) = t; }
    }
    asm volatile("s_waitcnt lgkmcnt(0)" ::: "memory");
    const float zg = *(const LAS float*)(L + L_GX + w * 128 + r * 4) * LOG2E;
    const float gate = __builtin_amdgcn_rcpf(1.f + __builtin_amdgcn_exp2f(-zg));
    if (head == 0 && hf == 0)
        __builtin_amdgcn_raw_buffer_store_b32(__float_as_uint(__builtin_amdgcn_rcpf(1.f + __builtin_amdgcn_exp2f(zg))), rs, WS_G + (rb * 128 + tt * 32 + r) * 4, 0, AUX_SC1);
    wg_bar();
    const ldsp ring = L + hs * RINGB;
    *(LAS u32x4*)(ring + tt * TILEB + koff) = kpk;
    *(LAS u32x4*)(ring + tt * TILEB + 1024 + lane * 16) = vpk;
    if (lane < 4) { u32x4 one = {0x3F803F80u, 0x3F803F80u, 0x3F803F80u, 0x3F803F80u}; *(LAS u32x4*)(ring + (lane * 4 + tt) * TILEB + 2048) = one; }
    drain();
    if (lane == 0) st_flag(rs, WS_KVF + ((head * 4 + tt) * 16 + rb) * 4, tag);
    if (threadIdx.x == 0) st_flag(rs, WS_ACK + a * 4, tag);
    wg_bar();
    STAMP(2); CSTAMP(11);

    const unsigned a_k = lds0 + hs * RINGB + lane * 16;
    const unsigned a_v0 = lds0 + hs * RINGB + (r < 16 ? 1024 + hf * 512 + r * 16 : 2048);
    const unsigned a_v1 = lds0 + hs * RINGB + (r < 16 ? 1280 + hf * 512 + r * 16 : 2048);
    const unsigned a_put = a_k + tt * TILEB, a_cnt = lds0 + L_CNT + hs * 32;
    const unsigned g_flag = WS_KVF + ((head * 4 + tt) * 16 + (lane & 15)) * 4;
    const unsigned g_src = WS_KV + (head * 64 + tt) * 2048 + lane * 16;
    f32x16 o = {}, cm;
    {
        const f32x16 s0 = MFMA(lds128(ring + lane * 16), qf, o);
        cm = splat16(-xhalf_max(rowmax16(s0)));
    }
    attn_loop(o, cm, qf, a_k, a_v0, a_v1, a_put, a_cnt, g_flag, g_src, mk_rsrc(ws, WS_END), tag, rb, ws);
    STAMP(5); CSTAMP(10);
    {
        bool bad = !(o[8] < 1.0e18f);
#pragma unroll
        for (int i = 0; i < 8; ++i) bad |= !(__builtin_fabsf(o[i]) < 3.0e38f);
        if (__builtin_expect(__any(bad), 0)) {
            const int gb = WS_KV + head * 64 * 2048;
            const f32x16 z16 = {};
            float m = -3.0e38f;
#pragma unroll 1
            for (int t = 0; t < 64; ++t) {
                const u32x4 kk = __builtin_amdgcn_raw_buffer_load_b128(rs, gb + lane * 16, t * 2048, AUX_SC1);
                m = __builtin_fmaxf(m, rowmax16(MFMA(__builtin_bit_cast(bf16x8, kk), qf, z16)));
            }
            cm = splat16(-xhalf_max(m));
            o = z16;
            const u32x4 one = {0x3F803F80u, 0x3F803F80u, 0x3F803F80u, 0x3F803F80u};
#pragma unroll 1
            for (int t = 0; t < 64; ++t) {
                const u32x4 kk = __builtin_amdgcn_raw_buffer_load_b128(rs, gb + lane * 16, t * 2048, AUX_SC1);
                u32x4 a0 = __builtin_amdgcn_raw_buffer_load_b128(rs, gb + 1024 + hf * 512 + (r & 15) * 16, t * 2048, AUX_SC1);
                u32x4 a1 = __builtin_amdgcn_raw_buffer_load_b128(rs, gb + 1280 + hf * 512 + (r & 15) * 16, t * 2048, AUX_SC1);
                if (r >= 16) { a0 = one; a1 = one; }
                f32x16 s = MFMA(__builtin_bit_cast(bf16x8, kk), qf, cm);
#pragma unroll
                for (int i = 0; i < 16; ++i) s[i] = __builtin_amdgcn_exp2f(s[i]);
                o = smf(__builtin_bit_cast(bf16x8, a0), lo8(s), o); o = smf(__builtin_bit_cast(bf16x8, a1), hi8(s), o);
            }
        }
    }
    const u32x4 opk = pack8<0>(o, gate * __builtin_amdgcn_rcpf(o[8]));
    __builtin_amdgcn_raw_buffer_store_b128(opk, rs, WS_Z + (rb * 4 + tt) * ZT + (112 + head) * 1024 + lane * 16, 0, AUX_SC1);
    drain();
    if (lane == 0) st_flag(rs, WS_OF + ((rb * 4 + tt) * 32 + head) * 4, tag);
    STAMP(6); CSTAMP(15);
}

__device__ __forceinline__ void role_f(const void* x, const void* wo, const void* w1, const void* w2, void* out, char* ws, ldsp L, unsigned lds0) {
    const int f = blockIdx.x - 256, g8 = f & 7, idx = f >> 3;
    const int lane = threadIdx.x & 63, w = __builtin_amdgcn_readfirstlane(threadIdx.x >> 6);
    const int r = lane & 31, hf = lane >> 5;
    const __amdgpu_buffer_rsrc_t rs = __builtin_amdgcn_make_buffer_rsrc(ws, 0, WS_END, 0x00020000);
    STAMP(0);
    const unsigned tag = __builtin_amdgcn_readfirstlane(ld_flag(rs, WS_EPOCH)) + 1u;
    wait_bar<0>();
    if (threadIdx.x == 0) st_flag(rs, WS_ACK + blockIdx.x * 4, tag);
    {
        const int hp = (g8 & 3) * 4 + (idx & 3), rb = (g8 >> 2) * 8 + (idx >> 2);
        wait_flags(rs, WS_KVF + ((2 * hp * 4) * 16 + rb) * 4, tag);
    }
    STAMP(1);
    if (idx < 28) {
        const int rbk = (g8 >> 1) * 4 + (idx & 3), cbk = (g8 & 1) * 7 + (idx >> 2);
        const int tt = w & 3, ch = w >> 2;
        drain();
        f32x16 h0 = {}, h1 = {};
        unsigned dummy = 0;
        gemm_a<1>(L, lds0, mk_rsrc(x, SEQ * DM * 2), mk_rsrc(w1, DM * DFF * 2), rbk * 128, cbk * 256, w, lane, h0, h1, 0u, dummy);
        STAMP(2);
        wait_flags(rs, WS_KVF + (tt * 16 + rbk) * 4, tag);
        const float og = __uint_as_float(ld_flag(rs, WS_G + (rbk * 128 + tt * 32 + r) * 4));
#pragma unroll
        for (int i = 0; i < 16; ++i) { h0[i] = __builtin_fmaxf(h0[i], 0.f); h1[i] = __builtin_fmaxf(h1[i], 0.f); }
        const int zo = WS_Z + (rbk * 4 + tt) * ZT + (cbk * 8 + ch * 4) * 1024 + lane * 16;
        __builtin_amdgcn_raw_buffer_store_b128(pack8<0>(h0, og), rs, zo, 0, AUX_SC1);
        __builtin_amdgcn_raw_buffer_store_b128(pack8<8>(h0, og), rs, zo + 1024, 0, AUX_SC1);
        __builtin_amdgcn_raw_buffer_store_b128(pack8<0>(h1, og), rs, zo + 2048, 0, AUX_SC1);
        __builtin_amdgcn_raw_buffer_store_b128(pack8<8>(h1, og), rs, zo + 3072, 0, AUX_SC1);
        drain();
        if (lane == 0) st_flag(rs, WS_HF + ((rbk * 4 + tt) * 32 + cbk * 2 + ch) * 4, tag);
        wg_bar();
    } else if (f == 255 && w == 0) {
#pragma unroll 1
        for (int i = 0; i < 8; ++i) wait_flags(rs, WS_ACK + (i * 64 + lane) * 4, tag);
        if (lane == 0) st_flag(rs, WS_EPOCH, tag);
    }
    STAMP(3);
    const int rb64 = g8 * 4 + (idx & 3), cb64 = idx >> 2;
    const int tt = w & 1, kq = w >> 1, tile = rb64 * 2 + tt;
    const u32x4 r2 = mk_rsrc(w2, DFF * DM * 2), ro = mk_rsrc(wo, DM * DM * 2), rz = mk_rsrc(ws, WS_END);
    const unsigned wv = (unsigned)((16 * tt + 4 * (lane >> 5) + ((lane >> 2) & 3)) * 1024 + cb64 * 128 + ((lane >> 4) & 1) * 64 + (lane & 3) * 16);
    const unsigned wd = lds0 + (unsigned)(kq * 4096 + tt * 2048);
    auto dw = [&](auto J) { constexpr int j = J;
        const unsigned so = j < 14 ? (unsigned)((448 * kq + 32 * j) * 1024) : (unsigned)((128 * kq + 32 * (j - 14)) * 1024);
        dma16(j < 14 ? r2 : ro, wv, so, wd + (j % 5) * 16384);
        dma16(j < 14 ? r2 : ro, wv, so + 8192, wd + (j % 5) * 16384 + 1024);
    };
    const unsigned zv = (unsigned)(WS_Z + tile * ZT + lane * 16);
    u32x4 z[4][2];
    auto dz = [&](auto J) { constexpr int j = J;
        const unsigned so = j < 14 ? (unsigned)((28 * kq + 2 * j) * 1024) : (unsigned)((112 + 8 * kq + 2 * (j - 14)) * 1024);
        ld2_sc1(z[j & 3][0], z[j & 3][1], rz, zv, so);
    };
    drain();
    static_for<0, 4>(dw);
    wait_flags(rs, WS_HF + (tile * 32 + 7 * kq + (lane < 7 ? lane : 0)) * 4, tag);
    STAMP(4);
    static_for<0, 4>(dz);
    const ldsp wb = L + kq * 4096 + hf * 512 + ((lane >> 2) & 3) * 64 + ((lane >> 4) & 1) * 32 + (lane & 3) * 8;
    f32x16 c0 = {}, c1 = {};
    auto mm = [&](auto J) { constexpr int j = J; constexpr int so = (j % 5) * 16384;
#pragma unroll
        for (int k = 0; k < 2; ++k) {
            const bf16x8 a0 = lds_tr2(wb + so + k * 2048, wb + so + k * 2048 + 1024);
            const bf16x8 a1 = lds_tr2(wb + so + k * 2048 + 256, wb + so + k * 2048 + 1280);
            const bf16x8 b = __builtin_bit_cast(bf16x8, z[j & 3][k]);
            c0 = MFMA(a0, b, c0); c1 = MFMA(a1, b, c1);
        }
        SBAR();
    };
    static_for<0, 14>([&](auto J) { constexpr int j = J;
        constexpr int n = j < 4 ? (3 - j) * 2 + j * 4 : (j + 3 < 14 ? 12 : 6 + (13 - j) * 2);
        wait_bar2<n>(z[j & 3][0], z[j & 3][1]);
        dw(std::integral_constant<int, j + 4>{});
        mm(J);
        if constexpr (j + 4 < 14) dz(std::integral_constant<int, j + 4>{});
    });
    wait_bar<0>();
    STAMP(5);
    wait_flags(rs, WS_OF + (tile * 32 + 8 * kq + (lane & 7)) * 4, tag);
    STAMP(6);
    static_for<14, 18>(dz);
    static_for<14, 18>([&](auto J) { constexpr int j = J;
        wait2<(17 - j) * 2>(z[j & 3][0], z[j & 3][1]);
        mm(J);
    });
    wg_bar();
    constexpr int RP = 272;
    { const ldsp rp = L + kq * (64 * RP) + (tt * 32 + r) * RP + hf * 16;
#pragma unroll
      for (int i = 0; i < 4; ++i) {
          f32x4 t0 = {c0[4 * i], c0[4 * i + 1], c0[4 * i + 2], c0[4 * i + 3]}, t1 = {c1[4 * i], c1[4 * i + 1], c1[4 * i + 2], c1[4 * i + 3]};
          *(LAS f32x4*)(rp + i * 32) = t0; *(LAS f32x4*)(rp + 128 + i * 32) = t1; } }
    wg_bar();
    { const int row = threadIdx.x >> 3, c8 = threadIdx.x & 7;
      const ldsp rp = L + row * RP + c8 * 32;
      f32x4 s0 = *(const LAS f32x4*)rp, s1 = *(const LAS f32x4*)(rp + 16);
#pragma unroll
      for (int q = 1; q < 4; ++q) { s0 += *(const LAS f32x4*)(rp + q * 64 * RP); s1 += *(const LAS f32x4*)(rp + q * 64 * RP + 16); }
      u32x4 ov = {pk2(s0[0], s0[1]), pk2(s0[2], s0[3]), pk2(s1[0], s1[1]), pk2(s1[2], s1[3])};
      *(GAS u32x4*)((GAS char*)out + (size_t)(rb64 * 64 + row) * 1024 + cb64 * 128 + c8 * 16) = ov; }
    STAMP(7);
}

__global__ __launch_bounds__(512, 4) void fused(const void* x, const void* wqkv, const void* wo, const void* w1, const void* w2,
                                                const void* gate_w, void* out, char* ws) {
    extern __shared__ __attribute__((aligned(16))) char smem[];
    const ldsp L = (ldsp)smem;
    const unsigned lds0 = (unsigned)(uintptr_t)L;
    if (blockIdx.x < 256) role_a(x, wqkv, gate_w, ws, L, lds0);
    else role_f(x, wo, w1, w2, out, ws, L, lds0);
}
}

extern "C" void kernel_launch(void* const* d_in, const int* in_sizes, int n_in, void* d_out, int out_size, void* d_ws, size_t ws_size,
                              hipStream_t stream) {
    (void)in_sizes; (void)n_in; (void)out_size; (void)ws_size;
    (void)hipFuncSetAttribute((const void*)gm::fused, hipFuncAttributeMaxDynamicSharedMemorySize, gm::LDS_BYTES);
    gm::fused<<<512, 512, gm::LDS_BYTES, stream>>>(d_in[0], d_in[1], d_in[2], d_in[4], d_in[5], d_in[6], d_out, (char*)d_ws);
}
```

```cpp
#define GM_TELE_IDX 0
#define GM_TELE_BLK 0
#include <hip/hip_runtime.h>
#include <hip/hip_bf16.h>
#include <stdint.h>
#include <utility>

#define LAS __attribute__((address_space(3)))
#define GAS __attribute__((address_space(1)))
typedef unsigned u32x4 __attribute__((ext_vector_type(4)));
typedef unsigned u32x2 __attribute__((ext_vector_type(2)));
typedef float f32x4 __attribute__((ext_vector_type(4)));
typedef float f32x8 __attribute__((ext_vector_type(8)));
typedef float f32x16 __attribute__((ext_vector_type(16)));
typedef __bf16 bf16x8 __attribute__((ext_vector_type(8)));
typedef __bf16 bf16x16 __attribute__((ext_vector_type(16)));
typedef __bf16 bf16x2 __attribute__((ext_vector_type(2)));
typedef float f32x2 __attribute__((ext_vector_type(2)));
typedef short s16x4 __attribute__((ext_vector_type(4)));
typedef LAS char* ldsp;

#define GM_STAMPS 0

namespace gm {
constexpr int SEQ = 2048, DM = 512, NH = 32, DFF = 1792;
constexpr int WS_EPOCH = 0;
constexpr int WS_ACK = 4096;
constexpr int WS_KVF = 8192;
constexpr int WS_HF = 16384;
constexpr int WS_OF = 24576;
constexpr int WS_G = 32768;
constexpr int WS_KV = 1 << 20;
constexpr int WS_Z = WS_KV + (4 << 20);
constexpr int ZT = 144 * 1024;
constexpr int WS_END = WS_Z + 64 * ZT;
constexpr int WS_STAMP = 16 << 20;
constexpr int LDS_BYTES = 81920;
constexpr int XSTG = 16640;
constexpr int L_X = 0, L_W = 3 * XSTG;
constexpr int L_GI = L_W + 4 * 6144;
constexpr int L_MISC = L_GI + 4096;
constexpr int L_GX = L_MISC, L_CNT = L_MISC + 1024, L_TAG = L_MISC + 1024 + 64;
constexpr int TILEB = 2064, SLOTB = 4 * TILEB, RINGB = 4 * SLOTB;
constexpr unsigned SPIN_CAP = 1u << 14;
constexpr float QSCALE = 0.25f * 1.4426950408889634f, LOG2E = 1.4426950408889634f;
constexpr int AUX_SC1 = 16;

template <int B, int E, class F> __device__ __forceinline__ void static_for(F&& f) {
    if constexpr (B < E) { f(std::integral_constant<int, B>{}); static_for<B + 1, E>(f); }
}
#define SBAR() __builtin_amdgcn_sched_barrier(0)
#define PIN(x) asm volatile("" : "+v"(x))
#define MFMA(a, b, c) __builtin_amdgcn_mfma_f32_32x32x16_bf16(a, b, c, 0, 0, 0)

__device__ __forceinline__ u32x4 mk_rsrc(const void* p, unsigned bytes) {
    const uint64_t a = (uint64_t)p;
    u32x4 r = {(unsigned)a, (unsigned)(a >> 32) & 0xffffu, bytes, 0x00020000u};
    return r;
}
__device__ __forceinline__ void dma16(u32x4 rs, unsigned voff, unsigned soff, unsigned ldsdst) {
    unsigned sv;
    asm volatile("s_mov_b32 %0, m0\n\ts_mov_b32 m0, %4\n\ts_nop 0\n\tbuffer_load_dwordx4 %1, %2, %3 offen lds\n\ts_mov_b32 m0, %0"
                 : "=&s"(sv) : "v"(voff), "s"(rs), "s"(soff), "s"(ldsdst) : "memory");
}
template <int N> __device__ __forceinline__ void wait_bar() {
    asm volatile("s_waitcnt vmcnt(%0) lgkmcnt(0)\n\ts_barrier" ::"n"(N) : "memory");
}
__device__ __forceinline__ void ld2_sc1(u32x4& a, u32x4& b, u32x4 rs, unsigned voff, unsigned soff) {
    asm volatile("buffer_load_dwordx4 %0, %1, %2, %3 offen sc1" : "=v"(a) : "v"(voff), "s"(rs), "s"(soff));
    asm volatile("buffer_load_dwordx4 %0, %1, %2, %3 offen offset:1024 sc1" : "=v"(b) : "v"(voff), "s"(rs), "s"(soff));
}
template <int N> __device__ __forceinline__ void wait_bar2(u32x4& a, u32x4& b) {
    asm volatile("s_waitcnt vmcnt(%2) lgkmcnt(0)\n\ts_barrier" : "+v"(a), "+v"(b) : "n"(N) : "memory");
}
template <int N> __device__ __forceinline__ void wait2(u32x4& a, u32x4& b) {
    asm volatile("s_waitcnt vmcnt(%2)" : "+v"(a), "+v"(b) : "n"(N) : "memory");
}
__device__ __forceinline__ void drain() { asm volatile("s_waitcnt vmcnt(0)" ::: "memory"); }
__device__ __forceinline__ void wg_bar() { asm volatile("s_waitcnt lgkmcnt(0)\n\ts_barrier" ::: "memory"); }
__device__ __forceinline__ bf16x8 lds128(ldsp p) { return *(const LAS bf16x8*)p; }
__device__ __forceinline__ bf16x8 lds_tr2(ldsp p0, ldsp p1) {
    s16x4 a = __builtin_amdgcn_ds_read_tr16_b64_v4i16((LAS s16x4*)p0);
    s16x4 b = __builtin_amdgcn_ds_read_tr16_b64_v4i16((LAS s16x4*)p1);
    return __builtin_bit_cast(bf16x8, __builtin_shufflevector(a, b, 0, 1, 2, 3, 4, 5, 6, 7));
}
__device__ __forceinline__ unsigned pk2(float lo, float hi) {
    f32x2 v = {lo, hi};
    return __builtin_bit_cast(unsigned, __builtin_convertvector(v, bf16x2));
}
template <int O> __device__ __forceinline__ u32x4 pack8(const f32x16& a, float s) {
    u32x4 r = {pk2(a[O] * s, a[O + 1] * s), pk2(a[O + 2] * s, a[O + 3] * s), pk2(a[O + 4] * s, a[O + 5] * s), pk2(a[O + 6] * s, a[O + 7] * s)};
    return r;
}
template <int O> __device__ __forceinline__ u32x4 pack8(const f32x16& a) {
    u32x4 r = {pk2(a[O], a[O + 1]), pk2(a[O + 2], a[O + 3]), pk2(a[O + 4], a[O + 5]), pk2(a[O + 6], a[O + 7])};
    return r;
}
__device__ __forceinline__ unsigned ld_flag(__amdgpu_buffer_rsrc_t rs, int off) { return __builtin_amdgcn_raw_buffer_load_b32(rs, off, 0, AUX_SC1); }
__device__ __forceinline__ void st_flag(__amdgpu_buffer_rsrc_t rs, int off, unsigned v) { __builtin_amdgcn_raw_buffer_store_b32(v, rs, off, 0, AUX_SC1); }
__device__ __forceinline__ void wait_flags(__amdgpu_buffer_rsrc_t rs, int off, unsigned tag) {
#pragma unroll 1
    for (unsigned spins = 0; spins < SPIN_CAP; ++spins) {
        asm volatile("" ::: "memory");
        const unsigned v = ld_flag(rs, off);
        if (__all(v == tag)) break;
        __builtin_amdgcn_s_sleep(2);
    }
    __builtin_amdgcn_fence(__ATOMIC_ACQUIRE, "wavefront");
}
#define TELE_DECL unsigned long long tele0 = __builtin_amdgcn_s_memrealtime(), telec0 = __builtin_amdgcn_s_memtime(), telev = 0;
#define STAMP(i) do { if ((i) == GM_TELE_IDX) telev = __builtin_amdgcn_s_memrealtime() - tele0; } while (0)
#define CSTAMP(i) do { } while (0)
#define TELE_END do { if (blockIdx.x == GM_TELE_BLK) { if (GM_TELE_IDX == 99) telev = (__builtin_amdgcn_s_memtime() - telec0) * 100 / (__builtin_amdgcn_s_memrealtime() - tele0); \
    while (__builtin_amdgcn_s_memrealtime() - tele0 < 6000 + telev) __builtin_amdgcn_s_sleep(2); } } while (0)

constexpr int gemma_wait(int j, int NWR, bool hasW) {
    int seq[64] = {}; int n = 0;
    seq[n++] = 100; seq[n++] = 100;
    if (hasW) { seq[n++] = 0; seq[n++] = 1; }
    seq[n++] = 101; seq[n++] = 101;
    if (hasW) { seq[n++] = 2; if (NWR == 4) seq[n++] = 3; }
    seq[n++] = 102; seq[n++] = 102;
    for (int i = 0; i < j; ++i) {
        if (hasW && i >= 1 && i + NWR - 1 <= 15) seq[n++] = i + NWR - 1;
        if ((i & 1) == 0 && i >= 2 && i / 2 + 2 <= 7) { seq[n++] = 100 + i / 2 + 2; seq[n++] = 100 + i / 2 + 2; }
    }
    int last = -1;
    for (int i = 0; i < n; ++i) if (seq[i] == 100 + j / 2 || seq[i] == j) last = i;
    return n - 1 - last;
}

template <int ROLE>
__device__ __forceinline__ void gemm_a(ldsp L, unsigned lds0, u32x4 rx, u32x4 rw, int row0, int colb, int w, int lane,
                                       f32x16& acc0, f32x16& acc1, unsigned gate_raw, unsigned& epoch_raw) {
    constexpr int NSEG = ROLE == 0 ? 3 : 4, NWR = ROLE == 0 ? 4 : 3, WU = NSEG * 2048, PITCH = ROLE == 0 ? 3072 : 3584;
    const int tt = w & 3, sel = w >> 2, r = lane & 31, hf = lane >> 5;
    const bool hasW = ROLE == 1 || w < 6;
    const unsigned xv = (unsigned)((row0 + 32 * (lane >> 4) + ((lane >> 3) & 1)) * 1024 + (lane & 7) * 16);
    const unsigned xs = (unsigned)(w * 4096), xd = lds0 + L_X + (unsigned)(w * 2080);
    unsigned wv;
    { const int blk = 4 * w + (lane >> 4), rw_ = (lane >> 2) & 3, pc = lane & 3;
      if (ROLE == 0) { const int kq = blk / 3, sg = blk % 3; wv = (unsigned)((4 * kq + rw_) * PITCH + sg * 1024 + colb + ((pc ^ (sg == 1 ? 2 : 0)) * 16)); }
      else wv = (unsigned)((4 * w + rw_) * PITCH + colb + (lane >> 4) * 64 + pc * 16); }
    const unsigned wd = lds0 + L_W + (unsigned)(w * 1024);
#define DX(s) do { dma16(rx, xv, xs + (s) * 128, xd + ((s) % 3) * XSTG); dma16(rx, xv, xs + (s) * 128 + 2048, xd + ((s) % 3) * XSTG + 1040); } while (0)
#define DW(u) do { if (hasW) dma16(rw, wv, (unsigned)((u) * 32 * PITCH), wd + ((u) % NWR) * WU); } while (0)
    DX(0); DW(0); DW(1); DX(1); DW(2); if (NWR == 4) DW(3); DX(2);
    if (ROLE == 0) {
        asm volatile("s_waitcnt vmcnt(6)" : "+v"(gate_raw), "+v"(epoch_raw));
        u32x2 gi = {gate_raw & 0xffffu, 0u};
        *(LAS u32x2*)(L + L_GI + (w * 64 + lane) * 8) = gi;
    }
    const ldsp xb = L + L_X + (r >> 1) * 1040 + (2 * tt + (r & 1)) * 128 + hf * 16;
    const int ch = (lane >> 4) & 1, q = (lane >> 2) & 3, p = lane & 3;
    ldsp wb0, wb1; int st1kq, st1u;
    if (ROLE == 0) {
        wb0 = L + L_W + (2 * hf * NSEG + ch) * 256 + q * 64 + (ch ? 1 - sel : sel) * 32 + p * 8;
        wb1 = ch ? L + L_GI + (8 * hf + q) * 8 : L + L_W + (2 * hf * NSEG + 2) * 256 + q * 64 + sel * 32 + p * 8;
        st1kq = ch ? 32 : NSEG * 256;
    } else {
        wb0 = L + L_W + (2 * hf * NSEG + 2 * sel) * 256 + q * 64 + ch * 32 + p * 8;
        wb1 = wb0 + 256; st1kq = NSEG * 256;
    }
    (void)st1u;
    bf16x8 xf[2][2], f0[2][2], f1[2][2];
    static_for<0, 17>([&](auto J) {
        constexpr int j = J;
        if constexpr (j < 16) {
            if (hasW) wait_bar<gemma_wait(j, NWR, true)>(); else wait_bar<gemma_wait(j, NWR, false)>();
            if constexpr (j >= 1 && j + NWR - 1 <= 15) DW(j + NWR - 1);
            if constexpr ((j & 1) == 0 && j >= 2 && j / 2 + 2 <= 7) DX(j / 2 + 2);
            constexpr int xo = ((j / 2) % 3) * XSTG + (j & 1) * 64, wo_ = (j % NWR) * WU;
            const int u1 = ROLE == 0 ? (ch ? j * 256 : wo_) : wo_;
#pragma unroll
            for (int k = 0; k < 2; ++k) {
                xf[j & 1][k] = lds128(xb + xo + k * 32);
                f0[j & 1][k] = lds_tr2(wb0 + wo_ + (4 * k) * NSEG * 256, wb0 + wo_ + (4 * k + 1) * NSEG * 256);
                f1[j & 1][k] = lds_tr2(wb1 + u1 + (4 * k) * st1kq, wb1 + u1 + (4 * k + 1) * st1kq);
            }
            SBAR();
        }
        if constexpr (j >= 1) {
            constexpr int b = (j - 1) & 1;
#pragma unroll
            for (int k = 0; k < 2; ++k) {
                acc0 = MFMA(f0[b][k], xf[b][k], acc0);
                if (ROLE == 0) acc1 = MFMA(xf[b][k], f1[b][k], acc1); else acc1 = MFMA(f1[b][k], xf[b][k], acc1);
            }
            SBAR();
        }
    });
#undef DX
#undef DW
}

__device__ __forceinline__ f32x16 smf(bf16x8 a, f32x8 b, f32x16 c) {
    return __builtin_amdgcn_smfmac_f32_32x32x32_bf16(a, __builtin_bit_cast(bf16x16, b), c, 0xDDDD, 0, 0);
}
__device__ __forceinline__ f32x8 lo8(const f32x16& s) { return __builtin_shufflevector(s, s, 0, 1, 2, 3, 4, 5, 6, 7); }
__device__ __forceinline__ f32x8 hi8(const f32x16& s) { return __builtin_shufflevector(s, s, 8, 9, 10, 11, 12, 13, 14, 15); }
__device__ __forceinline__ float rowmax16(const f32x16& s) {
    float a = __builtin_fmaxf(__builtin_fmaxf(s[0], s[1]), s[2]), b = __builtin_fmaxf(__builtin_fmaxf(s[3], s[4]), s[5]);
    a = __builtin_fmaxf(__builtin_fmaxf(a, s[6]), s[7]); b = __builtin_fmaxf(__builtin_fmaxf(b, s[8]), s[9]);
    a = __builtin_fmaxf(__builtin_fmaxf(a, s[10]), s[11]); b = __builtin_fmaxf(__builtin_fmaxf(b, s[12]), s[13]);
    a = __builtin_fmaxf(__builtin_fmaxf(a, s[14]), s[15]);
    return __builtin_fmaxf(a, b);
}
__device__ __forceinline__ float xhalf_max(float m) {
    auto rr = __builtin_amdgcn_permlane32_swap(__float_as_uint(m), __float_as_uint(m), false, false);
    return __builtin_fmaxf(__uint_as_float(rr[0]), __uint_as_float(rr[1]));
}
__device__ __forceinline__ f32x16 splat16(float v) { f32x16 r; for (int i = 0; i < 16; ++i) r[i] = v; return r; }
#define EXPR(S, a, b) do { _Pragma("unroll") for (int i_ = (a); i_ < (b); ++i_) S[i_] = __builtin_amdgcn_exp2f(S[i_]); PIN(S); SBAR(); } while (0)

#define A_EX(b, i) "v_exp_f32 v[" #b "+" #i "], v[" #b "+" #i "]\n\t"
#define A_QK(sp) "v_mfma_f32_32x32x16_bf16 v[" #sp ":" #sp "+15], %[kf], %[qf], %[cm]\n\t"
#define A_SMH(sp) "v_smfmac_f32_32x32x32_bf16 %[o], %[v1], v[" #sp "+8:" #sp "+15], %[ix]\n\t"
#define A_SML(sc) "v_smfmac_f32_32x32x32_bf16 %[o], %[v0], v[" #sc ":" #sc "+7], %[ix]\n\t"
#define A_RK(off) "ds_read_b128 %[kf], %[ak] offset:" off "\n\t"
#define A_R0(off) "ds_read_b128 %[v0], %[a0] offset:" off "\n\t"
#define A_R1(off) "ds_read_b128 %[v1], %[a1] offset:" off "\n\t"
#define A_LG(n) "s_waitcnt lgkmcnt(" #n ")\n\t"
#define A_SECT(sc, sp, SMH, R1, QK, RK, R0, XTRA) \
    A_EX(sc, 0) A_EX(sc, 1) A_EX(sc, 2) SMH R1 A_EX(sc, 3) A_EX(sc, 4) A_EX(sc, 5) QK RK \
    A_EX(sc, 6) A_EX(sc, 7) A_EX(sc, 8) A_EX(sc, 9) A_EX(sc, 10) A_LG(2) A_SML(sc) R0 XTRA \
    A_EX(sc, 11) A_EX(sc, 12) A_EX(sc, 13) A_EX(sc, 14) A_EX(sc, 15)
#define A_FULL(sc, sp, o0, o1, o2, XTRA) A_SECT(sc, sp, A_LG(1) A_SMH(sp), A_R1(o0), A_QK(sp), A_RK(o2), A_R0(o1), XTRA)
#define A_PUT(off) \
    "s_waitcnt vmcnt(0)\n\tds_write_b128 %[ap], %[pk] offset:" off "\n\tds_write_b128 %[ap], %[pv] offset:" off "+1024\n\t" \
    "s_mov_b64 exec, 1\n\tds_add_u32 %[ac], %[one]\n\ts_mov_b64 exec, -1\n\t"
#define A_FETCH(id) \
    "s_xor_b32 %[st], %[rb], %[nc]\n\ts_bitcmp1_b32 %[rd], %[st]\n\ts_cbranch_scc0 .Lpoll" #id "_%=\n" \
    ".Lpolled" #id "_%=:\n\ts_lshl_b32 %[st], %[st], 13\n\ts_add_u32 %[nc], %[nc], 1\n\t" \
    "buffer_load_dwordx4 %[pk], %[gs], %[rs], %[st] offen sc1\n\tbuffer_load_dwordx4 %[pv], %[gs], %[rs], %[st] offen offset:1024 sc1\n\t"
#define A_POLL(id) \
    ".Lpoll" #id "_%=:\n\tbuffer_load_dword %[tm], %[gf], %[rs], 0 offen sc1\n\ts_waitcnt vmcnt(0)\n\t" \
    "v_cmp_eq_u32 vcc, %[tag], %[tm]\n\ts_nop 1\n\ts_mov_b32 %[rd], vcc_lo\n\ts_bitcmp1_b32 %[rd], %[st]\n\ts_cbranch_scc1 .Lpolled" #id "_%=\n\t" \
    "s_sub_u32 %[sp], %[sp], 16\n\ts_cbranch_scc1 .Lgiveup_%=\n\ts_sleep 1\n\ts_branch .Lpoll" #id "_%=\n"
#define A_SYNC(id) \
    "v_readfirstlane_b32 %[st], %[tm]\n\ts_nop 0\n\ts_cmp_ge_u32 %[st], %[wt]\n\ts_cbranch_scc0 .Lsync" #id "_%=\n" \
    ".Lsynced" #id "_%=:\n\ts_add_u32 %[wt], %[wt], 4\n\t"
#define A_SYNCSLOW(id) \
    ".Lsync" #id "_%=:\n\tds_read_b32 %[tm], %[ac]\n\ts_waitcnt lgkmcnt(0)\n\tv_readfirstlane_b32 %[st], %[tm]\n\ts_nop 0\n\t" \
    "s_cmp_ge_u32 %[st], %[wt]\n\ts_cbranch_scc1 .Lsynced" #id "_%=\n\t" \
    "s_sub_u32 %[sp], %[sp], 1\n\ts_cbranch_scc1 .Lgiveup_%=\n\ts_sleep 1\n\ts_branch .Lsync" #id "_%=\n"
#define A_RC "ds_read_b32 %[tm], %[ac]\n\t"
#define T0(s) #s "*8256"
#define T1(s) #s "*8256+2064"
#define T2(s) #s "*8256+4128"
#define T3(s) #s "*8256+6192"
#define A_CHUNK(s, n, MID) \
    A_FULL(32, 48, T0(s), T1(s), T2(s), A_RC) A_FULL(48, 32, T1(s), T2(s), T3(s), "") MID \
    A_FULL(32, 48, T2(s), T3(s), T0(n), "") A_FULL(48, 32, T3(s), T0(n), T1(n), "")
#define A_TS(n) ""
#define A_LAST(lbl) "s_cmp_eq_u32 %[nc], 16\n\ts_cbranch_scc1 " lbl "_%=\n\t"
__device__ __forceinline__ void attn_loop(f32x16& o, const f32x16& cm, bf16x8 qf, unsigned a_k, unsigned a_v0, unsigned a_v1, unsigned a_put,
                                          unsigned a_cnt, unsigned g_flag, unsigned g_src, u32x4 rs, unsigned tag, int rb, char* ws) {
    u32x4 kf, v0, v1, pk, pv; unsigned tm, st, rd, nc, wt, sp;
    unsigned long long t0, t1, t2;
    asm volatile(
        "s_mov_b32 %[rd], 0\n\ts_mov_b32 %[nc], 1\n\ts_mov_b32 %[wt], 4\n\ts_mov_b32 %[sp], 0x40000\n\t"
        A_RK(T0(0)) A_R0(T0(0)) A_LG(1) A_QK(32) A_RK(T1(0)) "s_nop 11\n\t"
        A_SECT(32, 48, "", A_R1(T0(0)), A_LG(1) A_QK(48), A_RK(T2(0)), A_R0(T1(0)), "")
        A_FULL(48, 32, T1(0), T2(0), T3(0), "")
        A_SECT(32, 48, A_LG(1) A_SMH(48), A_R1(T2(0)), A_QK(48), A_LG(1), A_R0(T3(0)), "")
        A_SECT(48, 32, A_LG(1) A_SMH(32), A_R1(T3(0)), "", A_LG(1), "", "")
        A_LG(0) "s_nop 0\n\t" A_SMH(48)
        A_TS(0) A_FETCH(0) A_TS(1) A_PUT(T0(1)) A_FETCH(1) A_RC A_LG(0) A_SYNC(0) A_PUT(T0(2)) A_FETCH(2) A_TS(2)
        A_RK(T0(1)) A_R0(T0(1)) A_LG(1) A_QK(32) A_RK(T1(1)) "s_nop 11\n\t"
        A_SECT(32, 48, "", A_R1(T0(1)), A_LG(1) A_QK(48), A_RK(T2(1)), A_R0(T1(1)), A_RC)
        "s_branch .Lentry_%=\n"
        ".Lloop_%=:\n\t"
        A_CHUNK(0, 1, A_SYNC(1) A_PUT(T0(2)) A_FETCH(3))
        A_FULL(32, 48, T0(1), T1(1), T2(1), A_RC)
        ".Lentry_%=:\n\t"
        A_FULL(48, 32, T1(1), T2(1), T3(1), "")
        A_SYNC(2) A_PUT(T0(3)) A_LAST(".Lk1") A_FETCH(4) ".Lk1_%=:\n\t"
        A_FULL(32, 48, T2(1), T3(1), T0(2), "") A_FULL(48, 32, T3(1), T0(2), T1(2), "")
        A_CHUNK(2, 3, A_SYNC(3) "s_cmp_eq_u32 %[wt], 64\n\ts_cbranch_scc1 .Lk2_%=\n\t" A_PUT(T0(0)) A_FETCH(5) ".Lk2_%=:\n\t")
        A_CHUNK(3, 0, "s_cmp_eq_u32 %[wt], 64\n\ts_cbranch_scc1 .Lk3_%=\n\t" A_SYNC(4) A_PUT(T0(1)) A_FETCH(6) ".Lk3_%=:\n\t")
        "s_cmp_lg_u32 %[wt], 64\n\ts_cbranch_scc1 .Lloop_%=\n\t"
        A_LG(0) "s_nop 0\n\t" A_SMH(48)
        "s_branch .Lgiveup_%=\n"
        A_POLL(0) A_POLL(1) A_POLL(2) A_POLL(3) A_POLL(4) A_POLL(5) A_POLL(6)
        A_SYNCSLOW(0) A_SYNCSLOW(1) A_SYNCSLOW(2) A_SYNCSLOW(3) A_SYNCSLOW(4)
        ".Lgiveup_%=:\n\ts_waitcnt vmcnt(0) lgkmcnt(0)\n\ts_nop 11"
        : [o] "+v"(o), [kf] "=&v"(kf), [v0] "=&v"(v0), [v1] "=&v"(v1), [pk] "=&v"(pk), [pv] "=&v"(pv), [tm] "=&v"(tm),
          [st] "=&s"(st), [rd] "=&s"(rd), [nc] "=&s"(nc), [wt] "=&s"(wt), [sp] "=&s"(sp), [t0] "=&s"(t0), [t1] "=&s"(t1), [t2] "=&s"(t2)
        : [cm] "v"(cm), [qf] "v"(qf), [ak] "v"(a_k), [a0] "v"(a_v0), [a1] "v"(a_v1), [ap] "v"(a_put), [ac] "v"(a_cnt),
          [gf] "v"(g_flag), [gs] "v"(g_src), [ix] "v"(0xDDDD), [one] "v"(1), [rs] "s"(rs), [tag] "s"(tag), [rb] "s"(rb)
        : "memory", "vcc", "scc", "v32", "v33", "v34", "v35", "v36", "v37", "v38", "v39", "v40", "v41", "v42", "v43", "v44", "v45", "v46", "v47",
          "v48", "v49", "v50", "v51", "v52", "v53", "v54", "v55", "v56", "v57", "v58", "v59", "v60", "v61", "v62", "v63");
}

__device__ __forceinline__ void role_a(const void* x, const void* wqkv, const void* gate_w, char* ws, ldsp L, unsigned lds0) {
    const int a = blockIdx.x, g8 = a & 7, idx = a >> 3;
    const int hp = (g8 & 3) * 4 + (idx & 3), rb = (g8 >> 2) * 8 + (idx >> 2);
    const int lane = threadIdx.x & 63, w = __builtin_amdgcn_readfirstlane(threadIdx.x >> 6);
    const int tt = w & 3, hs = w >> 2, r = lane & 31, hf = lane >> 5, head = 2 * hp + hs;
    const __amdgpu_buffer_rsrc_t rs = __builtin_amdgcn_make_buffer_rsrc(ws, 0, WS_END, 0x00020000);
    TELE_DECL
    STAMP(0); CSTAMP(14);
    unsigned gate_raw, epoch;
    { const u32x4 rg = mk_rsrc(gate_w, DM * 2), re = mk_rsrc(ws, 64);
      asm volatile("buffer_load_ushort %0, %1, %2, 0 offen" : "=v"(gate_raw) : "v"(threadIdx.x * 2), "s"(rg));
      asm volatile("buffer_load_dword %0, off, %1, 0 sc1" : "=v"(epoch) : "s"(re)); }
    if (threadIdx.x < 2) *(LAS unsigned*)(L + L_CNT + threadIdx.x * 32) = 0u;
    f32x16 aqk = {}, av = {};
    gemm_a<0>(L, lds0, mk_rsrc(x, SEQ * DM * 2), mk_rsrc(wqkv, DM * 3 * DM * 2), rb * 128, hp * 64, w, lane, aqk, av, gate_raw, epoch);
    const unsigned tag = __builtin_amdgcn_readfirstlane(epoch) + 1u;
    STAMP(1); CSTAMP(12);
    const bf16x8 qf = __builtin_bit_cast(bf16x8, pack8<0>(aqk, QSCALE));
    const u32x4 kpk = pack8<8>(aqk);
    u32x4 vpk;
    { const u32x4 v0 = pack8<0>(av), v1 = pack8<8>(av);
#pragma unroll
      for (int i = 0; i < 4; ++i) { auto sw = __builtin_amdgcn_permlane16_swap(v0[i], v1[i], false, false); vpk[i] = sw[0]; } }
    const int kslot = (r & 19) | ((r & 4) << 1) | ((r & 8) >> 1);
    const int koff = hf * 512 + kslot * 16;
    const int gkv = WS_KV + (head * 64 + rb * 4 + tt) * 2048;
    __builtin_amdgcn_raw_buffer_store_b128(kpk, rs, gkv + koff, 0, AUX_SC1);
    __builtin_amdgcn_raw_buffer_store_b128(vpk, rs, gkv + 1024 + lane * 16, 0, AUX_SC1);
    if (r == 16) {
#pragma unroll
        for (int i = 0; i < 4; ++i) { f32x4 t = {av[4 * i], av[4 * i + 1], av[4 * i + 2], av[4 * i + 3]}; *(LAS f32x4*)(L + L_GX + w * 128 + (8 * i + 4 * hf) * 4) = t; }
    }
    asm volatile("s_waitcnt lgkmcnt(0)" ::: "memory");
    const float zg = *(const LAS float*)(L + L_GX + w * 128 + r * 4) * LOG2E;
    const float gate = __builtin_amdgcn_rcpf(1.f + __builtin_amdgcn_exp2f(-zg));
    if (head == 0 && hf == 0)
        __builtin_amdgcn_raw_buffer_store_b32(__float_as_uint(__builtin_amdgcn_rcpf(1.f + __builtin_amdgcn_exp2f(zg))), rs, WS_G + (rb * 128 + tt * 32 + r) * 4, 0, AUX_SC1);
    wg_bar();
    const ldsp ring = L + hs * RINGB;
    *(LAS u32x4*)(ring + tt * TILEB + koff) = kpk;
    *(LAS u32x4*)(ring + tt * TILEB + 1024 + lane * 16) = vpk;
    if (lane < 4) { u32x4 one = {0x3F803F80u, 0x3F803F80u, 0x3F803F80u, 0x3F803F80u}; *(LAS u32x4*)(ring + (lane * 4 + tt) * TILEB + 2048) = one; }
    drain();
    if (lane == 0) st_flag(rs, WS_KVF + ((head * 4 + tt) * 16 + rb) * 4, tag);
    if (threadIdx.x == 0) st_flag(rs, WS_ACK + a * 4, tag);
    wg_bar();
    STAMP(2); CSTAMP(11);

    const unsigned a_k = lds0 + hs * RINGB + lane * 16;
    const unsigned a_v0 = lds0 + hs * RINGB + (r < 16 ? 1024 + hf * 512 + r * 16 : 2048);
    const unsigned a_v1 = lds0 + hs * RINGB + (r < 16 ? 1280 + hf * 512 + r * 16 : 2048);
    const unsigned a_put = a_k + tt * TILEB, a_cnt = lds0 + L_CNT + hs * 32;
    const unsigned g_flag = WS_KVF + ((head * 4 + tt) * 16 + (lane & 15)) * 4;
    const unsigned g_src = WS_KV + (head * 64 + tt) * 2048 + lane * 16;
    f32x16 o = {}, cm;
    {
        const f32x16 s0 = MFMA(lds128(ring + lane * 16), qf, o);
        cm = splat16(-xhalf_max(rowmax16(s0)));
    }
    attn_loop(o, cm, qf, a_k, a_v0, a_v1, a_put, a_cnt, g_flag, g_src, mk_rsrc(ws, WS_END), tag, rb, ws);
    STAMP(5); CSTAMP(10);
    {
        bool bad = !(o[8] < 1.0e18f);
#pragma unroll
        for (int i = 0; i < 8; ++i) bad |= !(__builtin_fabsf(o[i]) < 3.0e38f);
        if (__builtin_expect(__any(bad), 0)) {
            const int gb = WS_KV + head * 64 * 2048;
            const f32x16 z16 = {};
            float m = -3.0e38f;
#pragma unroll 1
            for (int t = 0; t < 64; ++t) {
                const u32x4 kk = __builtin_amdgcn_raw_buffer_load_b128(rs, gb + lane * 16, t * 2048, AUX_SC1);
                m = __builtin_fmaxf(m, rowmax16(MFMA(__builtin_bit_cast(bf16x8, kk), qf, z16)));
            }
            cm = splat16(-xhalf_max(m));
            o = z16;
            const u32x4 one = {0x3F803F80u, 0x3F803F80u, 0x3F803F80u, 0x3F803F80u};
#pragma unroll 1
            for (int t = 0; t < 64; ++t) {
                const u32x4 kk = __builtin_amdgcn_raw_buffer_load_b128(rs, gb + lane * 16, t * 2048, AUX_SC1);
                u32x4 a0 = __builtin_amdgcn_raw_buffer_load_b128(rs, gb + 1024 + hf * 512 + (r & 15) * 16, t * 2048, AUX_SC1);
                u32x4 a1 = __builtin_amdgcn_raw_buffer_load_b128(rs, gb + 1280 + hf * 512 + (r & 15) * 16, t * 2048, AUX_SC1);
                if (r >= 16) { a0 = one; a1 = one; }
                f32x16 s = MFMA(__builtin_bit_cast(bf16x8, kk), qf, cm);
#pragma unroll
                for (int i = 0; i < 16; ++i) s[i] = __builtin_amdgcn_exp2f(s[i]);
                o = smf(__builtin_bit_cast(bf16x8, a0), lo8(s), o); o = smf(__builtin_bit_cast(bf16x8, a1), hi8(s), o);
            }
        }
    }
    const u32x4 opk = pack8<0>(o, gate * __builtin_amdgcn_rcpf(o[8]));
    __builtin_amdgcn_raw_buffer_store_b128(opk, rs, WS_Z + (rb * 4 + tt) * ZT + (112 + head) * 1024 + lane * 16, 0, AUX_SC1);
    drain();
    if (lane == 0) st_flag(rs, WS_OF + ((rb * 4 + tt) * 32 + head) * 4, tag);
    STAMP(6); CSTAMP(15);
    TELE_END;
}

__device__ __forceinline__ void role_f(const void* x, const void* wo, const void* w1, const void* w2, void* out, char* ws, ldsp L, unsigned lds0) {
    const int f = blockIdx.x - 256, g8 = f & 7, idx = f >> 3;
    const int lane = threadIdx.x & 63, w = __builtin_amdgcn_readfirstlane(threadIdx.x >> 6);
    const int r = lane & 31, hf = lane >> 5;
    const __amdgpu_buffer_rsrc_t rs = __builtin_amdgcn_make_buffer_rsrc(ws, 0, WS_END, 0x00020000);
    TELE_DECL
    STAMP(0);
    const unsigned tag = __builtin_amdgcn_readfirstlane(ld_flag(rs, WS_EPOCH)) + 1u;
    wait_bar<0>();
    if (threadIdx.x == 0) st_flag(rs, WS_ACK + blockIdx.x * 4, tag);
    {
        const int hp = (g8 & 3) * 4 + (idx & 3), rb = (g8 >> 2) * 8 + (idx >> 2);
        wait_flags(rs, WS_KVF + ((2 * hp * 4) * 16 + rb) * 4, tag);
    }
    STAMP(1);
    if (idx < 28) {
        const int rbk = (g8 >> 1) * 4 + (idx & 3), cbk = (g8 & 1) * 7 + (idx >> 2);
        const int tt = w & 3, ch = w >> 2;
        drain();
        f32x16 h0 = {}, h1 = {};
        unsigned dummy = 0;
        gemm_a<1>(L, lds0, mk_rsrc(x, SEQ * DM * 2), mk_rsrc(w1, DM * DFF * 2), rbk * 128, cbk * 256, w, lane, h0, h1, 0u, dummy);
        STAMP(2);
        wait_flags(rs, WS_KVF + (tt * 16 + rbk) * 4, tag);
        const float og = __uint_as_float(ld_flag(rs, WS_G + (rbk * 128 + tt * 32 + r) * 4));
#pragma unroll
        for (int i = 0; i < 16; ++i) { h0[i] = __builtin_fmaxf(h0[i], 0.f); h1[i] = __builtin_fmaxf(h1[i], 0.f); }
        const int zo = WS_Z + (rbk * 4 + tt) * ZT + (cbk * 8 + ch * 4) * 1024 + lane * 16;
        __builtin_amdgcn_raw_buffer_store_b128(pack8<0>(h0, og), rs, zo, 0, AUX_SC1);
        __builtin_amdgcn_raw_buffer_store_b128(pack8<8>(h0, og), rs, zo + 1024, 0, AUX_SC1);
        __builtin_amdgcn_raw_buffer_store_b128(pack8<0>(h1, og), rs, zo + 2048, 0, AUX_SC1);
        __builtin_amdgcn_raw_buffer_store_b128(pack8<8>(h1, og), rs, zo + 3072, 0, AUX_SC1);
        drain();
        if (lane == 0) st_flag(rs, WS_HF + ((rbk * 4 + tt) * 32 + cbk * 2 + ch) * 4, tag);
        wg_bar();
    } else if (f == 255 && w == 0) {
#pragma unroll 1
        for (int i = 0; i < 8; ++i) wait_flags(rs, WS_ACK + (i * 64 + lane) * 4, tag);
        if (lane == 0) st_flag(rs, WS_EPOCH, tag);
    }
    STAMP(3);
    const int rb64 = g8 * 4 + (idx & 3), cb64 = idx >> 2;
    const int tt = w & 1, kq = w >> 1, tile = rb64 * 2 + tt;
    const u32x4 r2 = mk_rsrc(w2, DFF * DM * 2), ro = mk_rsrc(wo, DM * DM * 2), rz = mk_rsrc(ws, WS_END);
    const unsigned wv = (unsigned)((16 * tt + 4 * (lane >> 5) + ((lane >> 2) & 3)) * 1024 + cb64 * 128 + ((lane >> 4) & 1) * 64 + (lane & 3) * 16);
    const unsigned wd = lds0 + (unsigned)(kq * 4096 + tt * 2048);
    auto dw = [&](auto J) { constexpr int j = J;
        const unsigned so = j < 14 ? (unsigned)((448 * kq + 32 * j) * 1024) : (unsigned)((128 * kq + 32 * (j - 14)) * 1024);
        dma16(j < 14 ? r2 : ro, wv, so, wd + (j % 5) * 16384);
        dma16(j < 14 ? r2 : ro, wv, so + 8192, wd + (j % 5) * 16384 + 1024);
    };
    const unsigned zv = (unsigned)(WS_Z + tile * ZT + lane * 16);
    u32x4 z[4][2];
    auto dz = [&](auto J) { constexpr int j = J;
        const unsigned so = j < 14 ? (unsigned)((28 * kq + 2 * j) * 1024) : (unsigned)((112 + 8 * kq + 2 * (j - 14)) * 1024);
        ld2_sc1(z[j & 3][0], z[j & 3][1], rz, zv, so);
    };
    drain();
    static_for<0, 4>(dw);
    wait_flags(rs, WS_HF + (tile * 32 + 7 * kq + (lane < 7 ? lane : 0)) * 4, tag);
    STAMP(4);
    static_for<0, 4>(dz);
    const ldsp wb = L + kq * 4096 + hf * 512 + ((lane >> 2) & 3) * 64 + ((lane >> 4) & 1) * 32 + (lane & 3) * 8;
    f32x16 c0 = {}, c1 = {};
    auto mm = [&](auto J) { constexpr int j = J; constexpr int so = (j % 5) * 16384;
#pragma unroll
        for (int k = 0; k < 2; ++k) {
            const bf16x8 a0 = lds_tr2(wb + so + k * 2048, wb + so + k * 2048 + 1024);
            const bf16x8 a1 = lds_tr2(wb + so + k * 2048 + 256, wb + so + k * 2048 + 1280);
            const bf16x8 b = __builtin_bit_cast(bf16x8, z[j & 3][k]);
            c0 = MFMA(a0, b, c0); c1 = MFMA(a1, b, c1);
        }
        SBAR();
    };
    static_for<0, 14>([&](auto J) { constexpr int j = J;
        constexpr int n = j < 4 ? (3 - j) * 2 + j * 4 : (j + 3 < 14 ? 12 : 6 + (13 - j) * 2);
        wait_bar2<n>(z[j & 3][0], z[j & 3][1]);
        dw(std::integral_constant<int, j + 4>{});
        mm(J);
        if constexpr (j + 4 < 14) dz(std::integral_constant<int, j + 4>{});
    });
    wait_bar<0>();
    STAMP(5);
    wait_flags(rs, WS_OF + (tile * 32 + 8 * kq + (lane & 7)) * 4, tag);
    STAMP(6);
    static_for<14, 18>(dz);
    static_for<14, 18>([&](auto J) { constexpr int j = J;
        wait2<(17 - j) * 2>(z[j & 3][0], z[j & 3][1]);
        mm(J);
    });
    wg_bar();
    constexpr int RP = 272;
    { const ldsp rp = L + kq * (64 * RP) + (tt * 32 + r) * RP + hf * 16;
#pragma unroll
      for (int i = 0; i < 4; ++i) {
          f32x4 t0 = {c0[4 * i], c0[4 * i + 1], c0[4 * i + 2], c0[4 * i + 3]}, t1 = {c1[4 * i], c1[4 * i + 1], c1[4 * i + 2], c1[4 * i + 3]};
          *(LAS f32x4*)(rp + i * 32) = t0; *(LAS f32x4*)(rp + 128 + i * 32) = t1; } }
    wg_bar();
    { const int row = threadIdx.x >> 3, c8 = threadIdx.x & 7;
      const ldsp rp = L + row * RP + c8 * 32;
      f32x4 s0 = *(const LAS f32x4*)rp, s1 = *(const LAS f32x4*)(rp + 16);
#pragma unroll
      for (int q = 1; q < 4; ++q) { s0 += *(const LAS f32x4*)(rp + q * 64 * RP); s1 += *(const LAS f32x4*)(rp + q * 64 * RP + 16); }
      u32x4 ov = {pk2(s0[0], s0[1]), pk2(s0[2], s0[3]), pk2(s1[0], s1[1]), pk2(s1[2], s1[3])};
      *(GAS u32x4*)((GAS char*)out + (size_t)(rb64 * 64 + row) * 1024 + cb64 * 128 + c8 * 16) = ov; }
    STAMP(7);
    TELE_END;
}

__global__ __launch_bounds__(512, 4) void fused(const void* x, const void* wqkv, const void* wo, const void* w1, const void* w2,
                                                const void* gate_w, void* out, char* ws) {
    extern __shared__ __attribute__((aligned(16))) char smem[];
    const ldsp L = (ldsp)smem;
    const unsigned lds0 = (unsigned)(uintptr_t)L;
    if (blockIdx.x < 256) role_a(x, wqkv, gate_w, ws, L, lds0);
    else role_f(x, wo, w1, w2, out, ws, L, lds0);
}
}

extern "C" void kernel_launch(void* const* d_in, const int* in_sizes, int n_in, void* d_out, int out_size, void* d_ws, size_t ws_size,
                              hipStream_t stream) {
    (void)in_sizes; (void)n_in; (void)out_size; (void)ws_size;
    (void)hipFuncSetAttribute((const void*)gm::fused, hipFuncAttributeMaxDynamicSharedMemorySize, gm::LDS_BYTES);
    gm::fused<<<512, 512, gm::LDS_BYTES, stream>>>(d_in[0], d_in[1], d_in[2], d_in[4], d_in[5], d_in[6], d_out, (char*)d_ws);
}
```

```cpp
#define GM_TELE_IDX 50
#define GM_TELE_BLK 0
#include <hip/hip_runtime.h>
#include <hip/hip_bf16.h>
#include <stdint.h>
#include <utility>

#define LAS __attribute__((address_space(3)))
#define GAS __attribute__((address_space(1)))
typedef unsigned u32x4 __attribute__((ext_vector_type(4)));
typedef unsigned u32x2 __attribute__((ext_vector_type(2)));
typedef float f32x4 __attribute__((ext_vector_type(4)));
typedef float f32x8 __attribute__((ext_vector_type(8)));
typedef float f32x16 __attribute__((ext_vector_type(16)));
typedef __bf16 bf16x8 __attribute__((ext_vector_type(8)));
typedef __bf16 bf16x16 __attribute__((ext_vector_type(16)));
typedef __bf16 bf16x2 __attribute__((ext_vector_type(2)));
typedef float f32x2 __attribute__((ext_vector_type(2)));
typedef short s16x4 __attribute__((ext_vector_type(4)));
typedef LAS char* ldsp;

#define GM_STAMPS 0

namespace gm {
constexpr int SEQ = 2048, DM = 512, NH = 32, DFF = 1792;
constexpr int WS_EPOCH = 0;
constexpr int WS_ACK = 4096;
constexpr int WS_KVF = 8192;
constexpr int WS_HF = 16384;
constexpr int WS_OF = 24576;
constexpr int WS_G = 32768;
constexpr int WS_KV = 1 << 20;
constexpr int WS_Z = WS_KV + (4 << 20);
constexpr int ZT = 144 * 1024;
constexpr int WS_END = WS_Z + 64 * ZT;
constexpr int WS_STAMP = 16 << 20;
constexpr int LDS_BYTES = 81920;
constexpr int XSTG = 16640;
constexpr int L_X = 0, L_W = 3 * XSTG;
constexpr int L_GI = L_W + 4 * 6144;
constexpr int L_MISC = L_GI + 4096;
constexpr int L_GX = L_MISC, L_CNT = L_MISC + 1024, L_TAG = L_MISC + 1024 + 64;
constexpr int TILEB = 2064, SLOTB = 4 * TILEB, RINGB = 4 * SLOTB;
constexpr unsigned SPIN_CAP = 1u << 14;
constexpr float QSCALE = 0.25f * 1.4426950408889634f, LOG2E = 1.4426950408889634f;
constexpr int AUX_SC1 = 16;

template <int B, int E, class F> __device__ __forceinline__ void static_for(F&& f) {
    if constexpr (B < E) { f(std::integral_constant<int, B>{}); static_for<B + 1, E>(f); }
}
#define SBAR() __builtin_amdgcn_sched_barrier(0)
#define PIN(x) asm volatile("" : "+v"(x))
#define MFMA(a, b, c) __builtin_amdgcn_mfma_f32_32x32x16_bf16(a, b, c, 0, 0, 0)

__device__ __forceinline__ u32x4 mk_rsrc(const void* p, unsigned bytes) {
    const uint64_t a = (uint64_t)p;
    u32x4 r = {(unsigned)a, (unsigned)(a >> 32) & 0xffffu, bytes, 0x00020000u};
    return r;
}
__device__ __forceinline__ void dma16(u32x4 rs, unsigned voff, unsigned soff, unsigned ldsdst) {
    unsigned sv;
    asm volatile("s_mov_b32 %0, m0\n\ts_mov_b32 m0, %4\n\ts_nop 0\n\tbuffer_load_dwordx4 %1, %2, %3 offen lds\n\ts_mov_b32 m0, %0"
                 : "=&s"(sv) : "v"(voff), "s"(rs), "s"(soff), "s"(ldsdst) : "memory");
}
template <int N> __device__ __forceinline__ void wait_bar() {
    asm volatile("s_waitcnt vmcnt(%0) lgkmcnt(0)\n\ts_barrier" ::"n"(N) : "memory");
}
__device__ __forceinline__ void ld2_sc1(u32x4& a, u32x4& b, u32x4 rs, unsigned voff, unsigned soff) {
    asm volatile("buffer_load_dwordx4 %0, %1, %2, %3 offen sc1" : "=v"(a) : "v"(voff), "s"(rs), "s"(soff));
    asm volatile("buffer_load_dwordx4 %0, %1, %2, %3 offen offset:1024 sc1" : "=v"(b) : "v"(voff), "s"(rs), "s"(soff));
}
template <int N> __device__ __forceinline__ void wait_bar2(u32x4& a, u32x4& b) {
    asm volatile("s_waitcnt vmcnt(%2) lgkmcnt(0)\n\ts_barrier" : "+v"(a), "+v"(b) : "n"(N) : "memory");
}
template <int N> __device__ __forceinline__ void wait2(u32x4& a, u32x4& b) {
    asm volatile("s_waitcnt vmcnt(%2)" : "+v"(a), "+v"(b) : "n"(N) : "memory");
}
__device__ __forceinline__ void drain() { asm volatile("s_waitcnt vmcnt(0)" ::: "memory"); }
__device__ __forceinline__ void wg_bar() { asm volatile("s_waitcnt lgkmcnt(0)\n\ts_barrier" ::: "memory"); }
__device__ __forceinline__ bf16x8 lds128(ldsp p) { return *(const LAS bf16x8*)p; }
__device__ __forceinline__ bf16x8 lds_tr2(ldsp p0, ldsp p1) {
    s16x4 a = __builtin_amdgcn_ds_read_tr16_b64_v4i16((LAS s16x4*)p0);
    s16x4 b = __builtin_amdgcn_ds_read_tr16_b64_v4i16((LAS s16x4*)p1);
    return __builtin_bit_cast(bf16x8, __builtin_shufflevector(a, b, 0, 1, 2, 3, 4, 5, 6, 7));
}
__device__ __forceinline__ unsigned pk2(float lo, float hi) {
    f32x2 v = {lo, hi};
    return __builtin_bit_cast(unsigned, __builtin_convertvector(v, bf16x2));
}
template <int O> __device__ __forceinline__ u32x4 pack8(const f32x16& a, float s) {
    u32x4 r = {pk2(a[O] * s, a[O + 1] * s), pk2(a[O + 2] * s, a[O + 3] * s), pk2(a[O + 4] * s, a[O + 5] * s), pk2(a[O + 6] * s, a[O + 7] * s)};
    return r;
}
template <int O> __device__ __forceinline__ u32x4 pack8(const f32x16& a) {
    u32x4 r = {pk2(a[O], a[O + 1]), pk2(a[O + 2], a[O + 3]), pk2(a[O + 4], a[O + 5]), pk2(a[O + 6], a[O + 7])};
    return r;
}
__device__ __forceinline__ unsigned ld_flag(__amdgpu_buffer_rsrc_t rs, int off) { return __builtin_amdgcn_raw_buffer_load_b32(rs, off, 0, AUX_SC1); }
__device__ __forceinline__ void st_flag(__amdgpu_buffer_rsrc_t rs, int off, unsigned v) { __builtin_amdgcn_raw_buffer_store_b32(v, rs, off, 0, AUX_SC1); }
__device__ __forceinline__ void wait_flags(__amdgpu_buffer_rsrc_t rs, int off, unsigned tag) {
#pragma unroll 1
    for (unsigned spins = 0; spins < SPIN_CAP; ++spins) {
        asm volatile("" ::: "memory");
        const unsigned v = ld_flag(rs, off);
        if (__all(v == tag)) break;
        __builtin_amdgcn_s_sleep(2);
    }
    __builtin_amdgcn_fence(__ATOMIC_ACQUIRE, "wavefront");
}
#define TELE_DECL unsigned long long tele0 = tent, telec0 = __builtin_amdgcn_s_memtime(), telev = 0;
#define STAMP(i) do { if ((i) == GM_TELE_IDX) telev = __builtin_amdgcn_s_memrealtime() - tele0; } while (0)
#define CSTAMP(i) do { } while (0)
#define TELE_END do { if (blockIdx.x == GM_TELE_BLK) { if (GM_TELE_IDX == 99) telev = (__builtin_amdgcn_s_memtime() - telec0) * 100 / (__builtin_amdgcn_s_memrealtime() - tele0); \
    while (__builtin_amdgcn_s_memrealtime() - tele0 < 6000 + telev) __builtin_amdgcn_s_sleep(2); } } while (0)

constexpr int gemma_wait(int j, int NWR, bool hasW) {
    int seq[64] = {}; int n = 0;
    seq[n++] = 100; seq[n++] = 100;
    if (hasW) { seq[n++] = 0; seq[n++] = 1; }
    seq[n++] = 101; seq[n++] = 101;
    if (hasW) { seq[n++] = 2; if (NWR == 4) seq[n++] = 3; }
    seq[n++] = 102; seq[n++] = 102;
    for (int i = 0; i < j; ++i) {
        if (hasW && i >= 1 && i + NWR - 1 <= 15) seq[n++] = i + NWR - 1;
        if ((i & 1) == 0 && i >= 2 && i / 2 + 2 <= 7) { seq[n++] = 100 + i / 2 + 2; seq[n++] = 100 + i / 2 + 2; }
    }
    int last = -1;
    for (int i = 0; i < n; ++i) if (seq[i] == 100 + j / 2 || seq[i] == j) last = i;
    return n - 1 - last;
}

template <int ROLE>
__device__ __forceinline__ void gemm_a(ldsp L, unsigned lds0, u32x4 rx, u32x4 rw, int row0, int colb, int w, int lane,
                                       f32x16& acc0, f32x16& acc1, unsigned gate_raw, unsigned& epoch_raw) {
    constexpr int NSEG = ROLE == 0 ? 3 : 4, NWR = ROLE == 0 ? 4 : 3, WU = NSEG * 2048, PITCH = ROLE == 0 ? 3072 : 3584;
    const int tt = w & 3, sel = w >> 2, r = lane & 31, hf = lane >> 5;
    const bool hasW = ROLE == 1 || w < 6;
    const unsigned xv = (unsigned)((row0 + 32 * (lane >> 4) + ((lane >> 3) & 1)) * 1024 + (lane & 7) * 16);
    const unsigned xs = (unsigned)(w * 4096), xd = lds0 + L_X + (unsigned)(w * 2080);
    unsigned wv;
    { const int blk = 4 * w + (lane >> 4), rw_ = (lane >> 2) & 3, pc = lane & 3;
      if (ROLE == 0) { const int kq = blk / 3, sg = blk % 3; wv = (unsigned)((4 * kq + rw_) * PITCH + sg * 1024 + colb + ((pc ^ (sg == 1 ? 2 : 0)) * 16)); }
      else wv = (unsigned)((4 * w + rw_) * PITCH + colb + (lane >> 4) * 64 + pc * 16); }
    const unsigned wd = lds0 + L_W + (unsigned)(w * 1024);
#define DX(s) do { dma16(rx, xv, xs + (s) * 128, xd + ((s) % 3) * XSTG); dma16(rx, xv, xs + (s) * 128 + 2048, xd + ((s) % 3) * XSTG + 1040); } while (0)
#define DW(u) do { if (hasW) dma16(rw, wv, (unsigned)((u) * 32 * PITCH), wd + ((u) % NWR) * WU); } while (0)
    DX(0); DW(0); DW(1); DX(1); DW(2); if (NWR == 4) DW(3); DX(2);
    if (ROLE == 0) {
        asm volatile("s_waitcnt vmcnt(6)" : "+v"(gate_raw), "+v"(epoch_raw));
        u32x2 gi = {gate_raw & 0xffffu, 0u};
        *(LAS u32x2*)(L + L_GI + (w * 64 + lane) * 8) = gi;
    }
    const ldsp xb = L + L_X + (r >> 1) * 1040 + (2 * tt + (r & 1)) * 128 + hf * 16;
    const int ch = (lane >> 4) & 1, q = (lane >> 2) & 3, p = lane & 3;
    ldsp wb0, wb1; int st1kq, st1u;
    if (ROLE == 0) {
        wb0 = L + L_W + (2 * hf * NSEG + ch) * 256 + q * 64 + (ch ? 1 - sel : sel) * 32 + p * 8;
        wb1 = ch ? L + L_GI + (8 * hf + q) * 8 : L + L_W + (2 * hf * NSEG + 2) * 256 + q * 64 + sel * 32 + p * 8;
        st1kq = ch ? 32 : NSEG * 256;
    } else {
        wb0 = L + L_W + (2 * hf * NSEG + 2 * sel) * 256 + q * 64 + ch * 32 + p * 8;
        wb1 = wb0 + 256; st1kq = NSEG * 256;
    }
    (void)st1u;
    bf16x8 xf[2][2], f0[2][2], f1[2][2];
    static_for<0, 17>([&](auto J) {
        constexpr int j = J;
        if constexpr (j < 16) {
            if (hasW) wait_bar<gemma_wait(j, NWR, true)>(); else wait_bar<gemma_wait(j, NWR, false)>();
            if constexpr (j >= 1 && j + NWR - 1 <= 15) DW(j + NWR - 1);
            if constexpr ((j & 1) == 0 && j >= 2 && j / 2 + 2 <= 7) DX(j / 2 + 2);
            constexpr int xo = ((j / 2) % 3) * XSTG + (j & 1) * 64, wo_ = (j % NWR) * WU;
            const int u1 = ROLE == 0 ? (ch ? j * 256 : wo_) : wo_;
#pragma unroll
            for (int k = 0; k < 2; ++k) {
                xf[j & 1][k] = lds128(xb + xo + k * 32);
                f0[j & 1][k] = lds_tr2(wb0 + wo_ + (4 * k) * NSEG * 256, wb0 + wo_ + (4 * k + 1) * NSEG * 256);
                f1[j & 1][k] = lds_tr2(wb1 + u1 + (4 * k) * st1kq, wb1 + u1 + (4 * k + 1) * st1kq);
            }
            SBAR();
        }
        if constexpr (j >= 1) {
            constexpr int b = (j - 1) & 1;
#pragma unroll
            for (int k = 0; k < 2; ++k) {
                acc0 = MFMA(f0[b][k], xf[b][k], acc0);
                if (ROLE == 0) acc1 = MFMA(xf[b][k], f1[b][k], acc1); else acc1 = MFMA(f1[b][k], xf[b][k], acc1);
            }
            SBAR();
        }
    });
#undef DX
#undef DW
}

__device__ __forceinline__ f32x16 smf(bf16x8 a, f32x8 b, f32x16 c) {
    return __builtin_amdgcn_smfmac_f32_32x32x32_bf16(a, __builtin_bit_cast(bf16x16, b), c, 0xDDDD, 0, 0);
}
__device__ __forceinline__ f32x8 lo8(const f32x16& s) { return __builtin_shufflevector(s, s, 0, 1, 2, 3, 4, 5, 6, 7); }
__device__ __forceinline__ f32x8 hi8(const f32x16& s) { return __builtin_shufflevector(s, s, 8, 9, 10, 11, 12, 13, 14, 15); }
__device__ __forceinline__ float rowmax16(const f32x16& s) {
    float a = __builtin_fmaxf(__builtin_fmaxf(s[0], s[1]), s[2]), b = __builtin_fmaxf(__builtin_fmaxf(s[3], s[4]), s[5]);
    a = __builtin_fmaxf(__builtin_fmaxf(a, s[6]), s[7]); b = __builtin_fmaxf(__builtin_fmaxf(b, s[8]), s[9]);
    a = __builtin_fmaxf(__builtin_fmaxf(a, s[10]), s[11]); b = __builtin_fmaxf(__builtin_fmaxf(b, s[12]), s[13]);
    a = __builtin_fmaxf(__builtin_fmaxf(a, s[14]), s[15]);
    return __builtin_fmaxf(a, b);
}
__device__ __forceinline__ float xhalf_max(float m) {
    auto rr = __builtin_amdgcn_permlane32_swap(__float_as_uint(m), __float_as_uint(m), false, false);
    return __builtin_fmaxf(__uint_as_float(rr[0]), __uint_as_float(rr[1]));
}
__device__ __forceinline__ f32x16 splat16(float v) { f32x16 r; for (int i = 0; i < 16; ++i) r[i] = v; return r; }
#define EXPR(S, a, b) do { _Pragma("unroll") for (int i_ = (a); i_ < (b); ++i_) S[i_] = __builtin_amdgcn_exp2f(S[i_]); PIN(S); SBAR(); } while (0)

#define A_EX(b, i) "v_exp_f32 v[" #b "+" #i "], v[" #b "+" #i "]\n\t"
#define A_QK(sp) "v_mfma_f32_32x32x16_bf16 v[" #sp ":" #sp "+15], %[kf], %[qf], %[cm]\n\t"
#define A_SMH(sp) "v_smfmac_f32_32x32x32_bf16 %[o], %[v1], v[" #sp "+8:" #sp "+15], %[ix]\n\t"
#define A_SML(sc) "v_smfmac_f32_32x32x32_bf16 %[o], %[v0], v[" #sc ":" #sc "+7], %[ix]\n\t"
#define A_RK(off) "ds_read_b128 %[kf], %[ak] offset:" off "\n\t"
#define A_R0(off) "ds_read_b128 %[v0], %[a0] offset:" off "\n\t"
#define A_R1(off) "ds_read_b128 %[v1], %[a1] offset:" off "\n\t"
#define A_LG(n) "s_waitcnt lgkmcnt(" #n ")\n\t"
#define A_SECT(sc, sp, SMH, R1, QK, RK, R0, XTRA) \
    A_EX(sc, 0) A_EX(sc, 1) A_EX(sc, 2) SMH R1 A_EX(sc, 3) A_EX(sc, 4) A_EX(sc, 5) QK RK \
    A_EX(sc, 6) A_EX(sc, 7) A_EX(sc, 8) A_EX(sc, 9) A_EX(sc, 10) A_LG(2) A_SML(sc) R0 XTRA \
    A_EX(sc, 11) A_EX(sc, 12) A_EX(sc, 13) A_EX(sc, 14) A_EX(sc, 15)
#define A_FULL(sc, sp, o0, o1, o2, XTRA) A_SECT(sc, sp, A_LG(1) A_SMH(sp), A_R1(o0), A_QK(sp), A_RK(o2), A_R0(o1), XTRA)
#define A_PUT(off) \
    "s_waitcnt vmcnt(0)\n\tds_write_b128 %[ap], %[pk] offset:" off "\n\tds_write_b128 %[ap], %[pv] offset:" off "+1024\n\t" \
    "s_mov_b64 exec, 1\n\tds_add_u32 %[ac], %[one]\n\ts_mov_b64 exec, -1\n\t"
#define A_FETCH(id) \
    "s_xor_b32 %[st], %[rb], %[nc]\n\ts_bitcmp1_b32 %[rd], %[st]\n\ts_cbranch_scc0 .Lpoll" #id "_%=\n" \
    ".Lpolled" #id "_%=:\n\ts_lshl_b32 %[st], %[st], 13\n\ts_add_u32 %[nc], %[nc], 1\n\t" \
    "buffer_load_dwordx4 %[pk], %[gs], %[rs], %[st] offen sc1\n\tbuffer_load_dwordx4 %[pv], %[gs], %[rs], %[st] offen offset:1024 sc1\n\t"
#define A_POLL(id) \
    ".Lpoll" #id "_%=:\n\tbuffer_load_dword %[tm], %[gf], %[rs], 0 offen sc1\n\ts_waitcnt vmcnt(0)\n\t" \
    "v_cmp_eq_u32 vcc, %[tag], %[tm]\n\ts_nop 1\n\ts_mov_b32 %[rd], vcc_lo\n\ts_bitcmp1_b32 %[rd], %[st]\n\ts_cbranch_scc1 .Lpolled" #id "_%=\n\t" \
    "s_sub_u32 %[sp], %[sp], 16\n\ts_cbranch_scc1 .Lgiveup_%=\n\ts_sleep 1\n\ts_branch .Lpoll" #id "_%=\n"
#define A_SYNC(id) \
    "v_readfirstlane_b32 %[st], %[tm]\n\ts_nop 0\n\ts_cmp_ge_u32 %[st], %[wt]\n\ts_cbranch_scc0 .Lsync" #id "_%=\n" \
    ".Lsynced" #id "_%=:\n\ts_add_u32 %[wt], %[wt], 4\n\t"
#define A_SYNCSLOW(id) \
    ".Lsync" #id "_%=:\n\tds_read_b32 %[tm], %[ac]\n\ts_waitcnt lgkmcnt(0)\n\tv_readfirstlane_b32 %[st], %[tm]\n\ts_nop 0\n\t" \
    "s_cmp_ge_u32 %[st], %[wt]\n\ts_cbranch_scc1 .Lsynced" #id "_%=\n\t" \
    "s_sub_u32 %[sp], %[sp], 1\n\ts_cbranch_scc1 .Lgiveup_%=\n\ts_sleep 1\n\ts_branch .Lsync" #id "_%=\n"
#define A_RC "ds_read_b32 %[tm], %[ac]\n\t"
#define T0(s) #s "*8256"
#define T1(s) #s "*8256+2064"
#define T2(s) #s "*8256+4128"
#define T3(s) #s "*8256+6192"
#define A_CHUNK(s, n, MID) \
    A_FULL(32, 48, T0(s), T1(s), T2(s), A_RC) A_FULL(48, 32, T1(s), T2(s), T3(s), "") MID \
    A_FULL(32, 48, T2(s), T3(s), T0(n), "") A_FULL(48, 32, T3(s), T0(n), T1(n), "")
#define A_TS(n) ""
#define A_LAST(lbl) "s_cmp_eq_u32 %[nc], 16\n\ts_cbranch_scc1 " lbl "_%=\n\t"
__device__ __forceinline__ void attn_loop(f32x16& o, const f32x16& cm, bf16x8 qf, unsigned a_k, unsigned a_v0, unsigned a_v1, unsigned a_put,
                                          unsigned a_cnt, unsigned g_flag, unsigned g_src, u32x4 rs, unsigned tag, int rb, char* ws) {
    u32x4 kf, v0, v1, pk, pv; unsigned tm, st, rd, nc, wt, sp;
    unsigned long long t0, t1, t2;
    asm volatile(
        "s_mov_b32 %[rd], 0\n\ts_mov_b32 %[nc], 1\n\ts_mov_b32 %[wt], 4\n\ts_mov_b32 %[sp], 0x40000\n\t"
        A_RK(T0(0)) A_R0(T0(0)) A_LG(1) A_QK(32) A_RK(T1(0)) "s_nop 11\n\t"
        A_SECT(32, 48, "", A_R1(T0(0)), A_LG(1) A_QK(48), A_RK(T2(0)), A_R0(T1(0)), "")
        A_FULL(48, 32, T1(0), T2(0), T3(0), "")
        A_SECT(32, 48, A_LG(1) A_SMH(48), A_R1(T2(0)), A_QK(48), A_LG(1), A_R0(T3(0)), "")
        A_SECT(48, 32, A_LG(1) A_SMH(32), A_R1(T3(0)), "", A_LG(1), "", "")
        A_LG(0) "s_nop 0\n\t" A_SMH(48)
        A_TS(0) A_FETCH(0) A_TS(1) A_PUT(T0(1)) A_FETCH(1) A_RC A_LG(0) A_SYNC(0) A_PUT(T0(2)) A_FETCH(2) A_TS(2)
        A_RK(T0(1)) A_R0(T0(1)) A_LG(1) A_QK(32) A_RK(T1(1)) "s_nop 11\n\t"
        A_SECT(32, 48, "", A_R1(T0(1)), A_LG(1) A_QK(48), A_RK(T2(1)), A_R0(T1(1)), A_RC)
        "s_branch .Lentry_%=\n"
        ".Lloop_%=:\n\t"
        A_CHUNK(0, 1, A_SYNC(1) A_PUT(T0(2)) A_FETCH(3))
        A_FULL(32, 48, T0(1), T1(1), T2(1), A_RC)
        ".Lentry_%=:\n\t"
        A_FULL(48, 32, T1(1), T2(1), T3(1), "")
        A_SYNC(2) A_PUT(T0(3)) A_LAST(".Lk1") A_FETCH(4) ".Lk1_%=:\n\t"
        A_FULL(32, 48, T2(1), T3(1), T0(2), "") A_FULL(48, 32, T3(1), T0(2), T1(2), "")
        A_CHUNK(2, 3, A_SYNC(3) "s_cmp_eq_u32 %[wt], 64\n\ts_cbranch_scc1 .Lk2_%=\n\t" A_PUT(T0(0)) A_FETCH(5) ".Lk2_%=:\n\t")
        A_CHUNK(3, 0, "s_cmp_eq_u32 %[wt], 64\n\ts_cbranch_scc1 .Lk3_%=\n\t" A_SYNC(4) A_PUT(T0(1)) A_FETCH(6) ".Lk3_%=:\n\t")
        "s_cmp_lg_u32 %[wt], 64\n\ts_cbranch_scc1 .Lloop_%=\n\t"
        A_LG(0) "s_nop 0\n\t" A_SMH(48)
        "s_branch .Lgiveup_%=\n"
        A_POLL(0) A_POLL(1) A_POLL(2) A_POLL(3) A_POLL(4) A_POLL(5) A_POLL(6)
        A_SYNCSLOW(0) A_SYNCSLOW(1) A_SYNCSLOW(2) A_SYNCSLOW(3) A_SYNCSLOW(4)
        ".Lgiveup_%=:\n\ts_waitcnt vmcnt(0) lgkmcnt(0)\n\ts_nop 11"
        : [o] "+v"(o), [kf] "=&v"(kf), [v0] "=&v"(v0), [v1] "=&v"(v1), [pk] "=&v"(pk), [pv] "=&v"(pv), [tm] "=&v"(tm),
          [st] "=&s"(st), [rd] "=&s"(rd), [nc] "=&s"(nc), [wt] "=&s"(wt), [sp] "=&s"(sp), [t0] "=&s"(t0), [t1] "=&s"(t1), [t2] "=&s"(t2)
        : [cm] "v"(cm), [qf] "v"(qf), [ak] "v"(a_k), [a0] "v"(a_v0), [a1] "v"(a_v1), [ap] "v"(a_put), [ac] "v"(a_cnt),
          [gf] "v"(g_flag), [gs] "v"(g_src), [ix] "v"(0xDDDD), [one] "v"(1), [rs] "s"(rs), [tag] "s"(tag), [rb] "s"(rb)
        : "memory", "vcc", "scc", "v32", "v33", "v34", "v35", "v36", "v37", "v38", "v39", "v40", "v41", "v42", "v43", "v44", "v45", "v46", "v47",
          "v48", "v49", "v50", "v51", "v52", "v53", "v54", "v55", "v56", "v57", "v58", "v59", "v60", "v61", "v62", "v63");
}

__device__ __forceinline__ void role_a(const void* x, const void* wqkv, const void* gate_w, char* ws, ldsp L, unsigned lds0, unsigned long long tent) {
    const int a = blockIdx.x, g8 = a & 7, idx = a >> 3;
    const int hp = (g8 & 3) * 4 + (idx & 3), rb = (g8 >> 2) * 8 + (idx >> 2);
    const int lane = threadIdx.x & 63, w = __builtin_amdgcn_readfirstlane(threadIdx.x >> 6);
    const int tt = w & 3, hs = w >> 2, r = lane & 31, hf = lane >> 5, head = 2 * hp + hs;
    const __amdgpu_buffer_rsrc_t rs = __builtin_amdgcn_make_buffer_rsrc(ws, 0, WS_END, 0x00020000);
    TELE_DECL
    STAMP(0); CSTAMP(14);
    unsigned gate_raw, epoch;
    { const u32x4 rg = mk_rsrc(gate_w, DM * 2), re = mk_rsrc(ws, 64);
      asm volatile("buffer_load_ushort %0, %1, %2, 0 offen" : "=v"(gate_raw) : "v"(threadIdx.x * 2), "s"(rg));
      asm volatile("buffer_load_dword %0, off, %1, 0 sc1" : "=v"(epoch) : "s"(re)); }
    if (threadIdx.x < 2) *(LAS unsigned*)(L + L_CNT + threadIdx.x * 32) = 0u;
    f32x16 aqk = {}, av = {};
    gemm_a<0>(L, lds0, mk_rsrc(x, SEQ * DM * 2), mk_rsrc(wqkv, DM * 3 * DM * 2), rb * 128, hp * 64, w, lane, aqk, av, gate_raw, epoch);
    const unsigned tag = __builtin_amdgcn_readfirstlane(epoch) + 1u;
    STAMP(1); CSTAMP(12);
    const bf16x8 qf = __builtin_bit_cast(bf16x8, pack8<0>(aqk, QSCALE));
    const u32x4 kpk = pack8<8>(aqk);
    u32x4 vpk;
    { const u32x4 v0 = pack8<0>(av), v1 = pack8<8>(av);
#pragma unroll
      for (int i = 0; i < 4; ++i) { auto sw = __builtin_amdgcn_permlane16_swap(v0[i], v1[i], false, false); vpk[i] = sw[0]; } }
    const int kslot = (r & 19) | ((r & 4) << 1) | ((r & 8) >> 1);
    const int koff = hf * 512 + kslot * 16;
    const int gkv = WS_KV + (head * 64 + rb * 4 + tt) * 2048;
    __builtin_amdgcn_raw_buffer_store_b128(kpk, rs, gkv + koff, 0, AUX_SC1);
    __builtin_amdgcn_raw_buffer_store_b128(vpk, rs, gkv + 1024 + lane * 16, 0, AUX_SC1);
    if (r == 16) {
#pragma unroll
        for (int i = 0; i < 4; ++i) { f32x4 t = {av[4 * i], av[4 * i + 1], av[4 * i + 2], av[4 * i + 3]}; *(LAS f32x4*)(L + L_GX + w * 128 + (8 * i + 4 * hf) * 4) = t; }
    }
    asm volatile("s_waitcnt lgkmcnt(0)" ::: "memory");
    const float zg = *(const LAS float*)(L + L_GX + w * 128 + r * 4) * LOG2E;
    const float gate = __builtin_amdgcn_rcpf(1.f + __builtin_amdgcn_exp2f(-zg));
    if (head == 0 && hf == 0)
        __builtin_amdgcn_raw_buffer_store_b32(__float_as_uint(__builtin_amdgcn_rcpf(1.f + __builtin_amdgcn_exp2f(zg))), rs, WS_G + (rb * 128 + tt * 32 + r) * 4, 0, AUX_SC1);
    wg_bar();
    const ldsp ring = L + hs * RINGB;
    *(LAS u32x4*)(ring + tt * TILEB + koff) = kpk;
    *(LAS u32x4*)(ring + tt * TILEB + 1024 + lane * 16) = vpk;
    if (lane < 4) { u32x4 one = {0x3F803F80u, 0x3F803F80u, 0x3F803F80u, 0x3F803F80u}; *(LAS u32x4*)(ring + (lane * 4 + tt) * TILEB + 2048) = one; }
    drain();
    if (lane == 0) st_flag(rs, WS_KVF + ((head * 4 + tt) * 16 + rb) * 4, tag);
    if (threadIdx.x == 0) st_flag(rs, WS_ACK + a * 4, tag);
    wg_bar();
    STAMP(2); CSTAMP(11);

    const unsigned a_k = lds0 + hs * RINGB + lane * 16;
    const unsigned a_v0 = lds0 + hs * RINGB + (r < 16 ? 1024 + hf * 512 + r * 16 : 2048);
    const unsigned a_v1 = lds0 + hs * RINGB + (r < 16 ? 1280 + hf * 512 + r * 16 : 2048);
    const unsigned a_put = a_k + tt * TILEB, a_cnt = lds0 + L_CNT + hs * 32;
    const unsigned g_flag = WS_KVF + ((head * 4 + tt) * 16 + (lane & 15)) * 4;
    const unsigned g_src = WS_KV + (head * 64 + tt) * 2048 + lane * 16;
    f32x16 o = {}, cm;
    {
        const f32x16 s0 = MFMA(lds128(ring + lane * 16), qf, o);
        cm = splat16(-xhalf_max(rowmax16(s0)));
    }
    attn_loop(o, cm, qf, a_k, a_v0, a_v1, a_put, a_cnt, g_flag, g_src, mk_rsrc(ws, WS_END), tag, rb, ws);
    STAMP(5); CSTAMP(10);
    {
        bool bad = !(o[8] < 1.0e18f);
#pragma unroll
        for (int i = 0; i < 8; ++i) bad |= !(__builtin_fabsf(o[i]) < 3.0e38f);
        if (__builtin_expect(__any(bad), 0)) {
            const int gb = WS_KV + head * 64 * 2048;
            const f32x16 z16 = {};
            float m = -3.0e38f;
#pragma unroll 1
            for (int t = 0; t < 64; ++t) {
                const u32x4 kk = __builtin_amdgcn_raw_buffer_load_b128(rs, gb + lane * 16, t * 2048, AUX_SC1);
                m = __builtin_fmaxf(m, rowmax16(MFMA(__builtin_bit_cast(bf16x8, kk), qf, z16)));
            }
            cm = splat16(-xhalf_max(m));
            o = z16;
            const u32x4 one = {0x3F803F80u, 0x3F803F80u, 0x3F803F80u, 0x3F803F80u};
#pragma unroll 1
            for (int t = 0; t < 64; ++t) {
                const u32x4 kk = __builtin_amdgcn_raw_buffer_load_b128(rs, gb + lane * 16, t * 2048, AUX_SC1);
                u32x4 a0 = __builtin_amdgcn_raw_buffer_load_b128(rs, gb + 1024 + hf * 512 + (r & 15) * 16, t * 2048, AUX_SC1);
                u32x4 a1 = __builtin_amdgcn_raw_buffer_load_b128(rs, gb + 1280 + hf * 512 + (r & 15) * 16, t * 2048, AUX_SC1);
                if (r >= 16) { a0 = one; a1 = one; }
                f32x16 s = MFMA(__builtin_bit_cast(bf16x8, kk), qf, cm);
#pragma unroll
                for (int i = 0; i < 16; ++i) s[i] = __builtin_amdgcn_exp2f(s[i]);
                o = smf(__builtin_bit_cast(bf16x8, a0), lo8(s), o); o = smf(__builtin_bit_cast(bf16x8, a1), hi8(s), o);
            }
        }
    }
    const u32x4 opk = pack8<0>(o, gate * __builtin_amdgcn_rcpf(o[8]));
    __builtin_amdgcn_raw_buffer_store_b128(opk, rs, WS_Z + (rb * 4 + tt) * ZT + (112 + head) * 1024 + lane * 16, 0, AUX_SC1);
    drain();
    if (lane == 0) st_flag(rs, WS_OF + ((rb * 4 + tt) * 32 + head) * 4, tag);
    STAMP(6); CSTAMP(15);
    TELE_END;
}

__device__ __forceinline__ void role_f(const void* x, const void* wo, const void* w1, const void* w2, void* out, char* ws, ldsp L, unsigned lds0, unsigned long long tent) {
    const int f = blockIdx.x - 256, g8 = f & 7, idx = f >> 3;
    const int lane = threadIdx.x & 63, w = __builtin_amdgcn_readfirstlane(threadIdx.x >> 6);
    const int r = lane & 31, hf = lane >> 5;
    const __amdgpu_buffer_rsrc_t rs = __builtin_amdgcn_make_buffer_rsrc(ws, 0, WS_END, 0x00020000);
    TELE_DECL
    STAMP(0);
    const unsigned tag = __builtin_amdgcn_readfirstlane(ld_flag(rs, WS_EPOCH)) + 1u;
    wait_bar<0>();
    if (threadIdx.x == 0) st_flag(rs, WS_ACK + blockIdx.x * 4, tag);
    {
        const int hp = (g8 & 3) * 4 + (idx & 3), rb = (g8 >> 2) * 8 + (idx >> 2);
        wait_flags(rs, WS_KVF + ((2 * hp * 4) * 16 + rb) * 4, tag);
    }
    STAMP(1);
    if (idx < 28) {
        const int rbk = (g8 >> 1) * 4 + (idx & 3), cbk = (g8 & 1) * 7 + (idx >> 2);
        const int tt = w & 3, ch = w >> 2;
        drain();
        f32x16 h0 = {}, h1 = {};
        unsigned dummy = 0;
        gemm_a<1>(L, lds0, mk_rsrc(x, SEQ * DM * 2), mk_rsrc(w1, DM * DFF * 2), rbk * 128, cbk * 256, w, lane, h0, h1, 0u, dummy);
        STAMP(2);
        wait_flags(rs, WS_KVF + (tt * 16 + rbk) * 4, tag);
        const float og = __uint_as_float(ld_flag(rs, WS_G + (rbk * 128 + tt * 32 + r) * 4));
#pragma unroll
        for (int i = 0; i < 16; ++i) { h0[i] = __builtin_fmaxf(h0[i], 0.f); h1[i] = __builtin_fmaxf(h1[i], 0.f); }
        const int zo = WS_Z + (rbk * 4 + tt) * ZT + (cbk * 8 + ch * 4) * 1024 + lane * 16;
        __builtin_amdgcn_raw_buffer_store_b128(pack8<0>(h0, og), rs, zo, 0, AUX_SC1);
        __builtin_amdgcn_raw_buffer_store_b128(pack8<8>(h0, og), rs, zo + 1024, 0, AUX_SC1);
        __builtin_amdgcn_raw_buffer_store_b128(pack8<0>(h1, og), rs, zo + 2048, 0, AUX_SC1);
        __builtin_amdgcn_raw_buffer_store_b128(pack8<8>(h1, og), rs, zo + 3072, 0, AUX_SC1);
        drain();
        if (lane == 0) st_flag(rs, WS_HF + ((rbk * 4 + tt) * 32 + cbk * 2 + ch) * 4, tag);
        wg_bar();
    } else if (f == 255 && w == 0) {
#pragma unroll 1
        for (int i = 0; i < 8; ++i) wait_flags(rs, WS_ACK + (i * 64 + lane) * 4, tag);
        if (lane == 0) st_flag(rs, WS_EPOCH, tag);
    }
    STAMP(3);
    const int rb64 = g8 * 4 + (idx & 3), cb64 = idx >> 2;
    const int tt = w & 1, kq = w >> 1, tile = rb64 * 2 + tt;
    const u32x4 r2 = mk_rsrc(w2, DFF * DM * 2), ro = mk_rsrc(wo, DM * DM * 2), rz = mk_rsrc(ws, WS_END);
    const unsigned wv = (unsigned)((16 * tt + 4 * (lane >> 5) + ((lane >> 2) & 3)) * 1024 + cb64 * 128 + ((lane >> 4) & 1) * 64 + (lane & 3) * 16);
    const unsigned wd = lds0 + (unsigned)(kq * 4096 + tt * 2048);
    auto dw = [&](auto J) { constexpr int j = J;
        const unsigned so = j < 14 ? (unsigned)((448 * kq + 32 * j) * 1024) : (unsigned)((128 * kq + 32 * (j - 14)) * 1024);
        dma16(j < 14 ? r2 : ro, wv, so, wd + (j % 5) * 16384);
        dma16(j < 14 ? r2 : ro, wv, so + 8192, wd + (j % 5) * 16384 + 1024);
    };
    const unsigned zv = (unsigned)(WS_Z + tile * ZT + lane * 16);
    u32x4 z[4][2];
    auto dz = [&](auto J) { constexpr int j = J;
        const unsigned so = j < 14 ? (unsigned)((28 * kq + 2 * j) * 1024) : (unsigned)((112 + 8 * kq + 2 * (j - 14)) * 1024);
        ld2_sc1(z[j & 3][0], z[j & 3][1], rz, zv, so);
    };
    drain();
    static_for<0, 4>(dw);
    wait_flags(rs, WS_HF + (tile * 32 + 7 * kq + (lane < 7 ? lane : 0)) * 4, tag);
    STAMP(4);
    static_for<0, 4>(dz);
    const ldsp wb = L + kq * 4096 + hf * 512 + ((lane >> 2) & 3) * 64 + ((lane >> 4) & 1) * 32 + (lane & 3) * 8;
    f32x16 c0 = {}, c1 = {};
    auto mm = [&](auto J) { constexpr int j = J; constexpr int so = (j % 5) * 16384;
#pragma unroll
        for (int k = 0; k < 2; ++k) {
            const bf16x8 a0 = lds_tr2(wb + so + k * 2048, wb + so + k * 2048 + 1024);
            const bf16x8 a1 = lds_tr2(wb + so + k * 2048 + 256, wb + so + k * 2048 + 1280);
            const bf16x8 b = __builtin_bit_cast(bf16x8, z[j & 3][k]);
            c0 = MFMA(a0, b, c0); c1 = MFMA(a1, b, c1);
        }
        SBAR();
    };
    static_for<0, 14>([&](auto J) { constexpr int j = J;
        constexpr int n = j < 4 ? (3 - j) * 2 + j * 4 : (j + 3 < 14 ? 12 : 6 + (13 - j) * 2);
        wait_bar2<n>(z[j & 3][0], z[j & 3][1]);
        dw(std::integral_constant<int, j + 4>{});
        mm(J);
        if constexpr (j + 4 < 14) dz(std::integral_constant<int, j + 4>{});
    });
    wait_bar<0>();
    STAMP(5);
    wait_flags(rs, WS_OF + (tile * 32 + 8 * kq + (lane & 7)) * 4, tag);
    STAMP(6);
    static_for<14, 18>(dz);
    static_for<14, 18>([&](auto J) { constexpr int j = J;
        wait2<(17 - j) * 2>(z[j & 3][0], z[j & 3][1]);
        mm(J);
    });
    wg_bar();
    constexpr int RP = 272;
    { const ldsp rp = L + kq * (64 * RP) + (tt * 32 + r) * RP + hf * 16;
#pragma unroll
      for (int i = 0; i < 4; ++i) {
          f32x4 t0 = {c0[4 * i], c0[4 * i + 1], c0[4 * i + 2], c0[4 * i + 3]}, t1 = {c1[4 * i], c1[4 * i + 1], c1[4 * i + 2], c1[4 * i + 3]};
          *(LAS f32x4*)(rp + i * 32) = t0; *(LAS f32x4*)(rp + 128 + i * 32) = t1; } }
    wg_bar();
    { const int row = threadIdx.x >> 3, c8 = threadIdx.x & 7;
      const ldsp rp = L + row * RP + c8 * 32;
      f32x4 s0 = *(const LAS f32x4*)rp, s1 = *(const LAS f32x4*)(rp + 16);
#pragma unroll
      for (int q = 1; q < 4; ++q) { s0 += *(const LAS f32x4*)(rp + q * 64 * RP); s1 += *(const LAS f32x4*)(rp + q * 64 * RP + 16); }
      u32x4 ov = {pk2(s0[0], s0[1]), pk2(s0[2], s0[3]), pk2(s1[0], s1[1]), pk2(s1[2], s1[3])};
      *(GAS u32x4*)((GAS char*)out + (size_t)(rb64 * 64 + row) * 1024 + cb64 * 128 + c8 * 16) = ov; }
    STAMP(7);
    TELE_END;
}

__global__ __launch_bounds__(512, 4) void fused(const void* x, const void* wqkv, const void* wo, const void* w1, const void* w2,
                                                const void* gate_w, void* out, char* ws) {
    unsigned long long tent; asm volatile("s_memrealtime %0\n\ts_waitcnt lgkmcnt(0)" : "=s"(tent));
    extern __shared__ __attribute__((aligned(16))) char smem[];
    const ldsp L = (ldsp)smem;
    const unsigned lds0 = (unsigned)(uintptr_t)L;
    if (blockIdx.x < 256) role_a(x, wqkv, gate_w, ws, L, lds0, tent);
    else role_f(x, wo, w1, w2, out, ws, L, lds0, tent);
}
}

extern "C" void kernel_launch(void* const* d_in, const int* in_sizes, int n_in, void* d_out, int out_size, void* d_ws, size_t ws_size,
                              hipStream_t stream) {
    (void)in_sizes; (void)n_in; (void)out_size; (void)ws_size;
    (void)hipFuncSetAttribute((const void*)gm::fused, hipFuncAttributeMaxDynamicSharedMemorySize, gm::LDS_BYTES);
    gm::fused<<<512, 512, gm::LDS_BYTES, stream>>>(d_in[0], d_in[1], d_in[2], d_in[4], d_in[5], d_in[6], d_out, (char*)d_ws);
}
```

```cpp
#include <hip/hip_runtime.h>
#include <hip/hip_bf16.h>
#include <stdint.h>

#define LAS __attribute__((address_space(3)))
#define GAS __attribute__((address_space(1)))
typedef unsigned u32x4 __attribute__((ext_vector_type(4)));
typedef unsigned u32x2 __attribute__((ext_vector_type(2)));
typedef float f32x4 __attribute__((ext_vector_type(4)));
typedef float f32x8 __attribute__((ext_vector_type(8)));
typedef float f32x16 __attribute__((ext_vector_type(16)));
typedef __bf16 bf16x8 __attribute__((ext_vector_type(8)));
typedef __bf16 bf16x16 __attribute__((ext_vector_type(16)));
typedef __bf16 bf16x2 __attribute__((ext_vector_type(2)));
typedef float f32x2 __attribute__((ext_vector_type(2)));
typedef LAS char* ldsp;

namespace gm {
constexpr int SEQ = 2048, DM = 512, NH = 32, DFF = 1792;
constexpr int WS_EPOCH = 0;
constexpr int WS_G = 4096;
constexpr int FS = 128;
constexpr int WS_ACK = 64 << 10;
constexpr int WS_HF = 512 << 10;
constexpr int WS_KV = 1 << 20;
constexpr int WS_Z = WS_KV + (4 << 20);
constexpr int ZT = 144 * 1024;
constexpr int WS_END = WS_Z + 64 * ZT;
constexpr int LDS_BYTES = 163840;
constexpr int XSTG = 16640;
constexpr int L_X = 0, L_WA = 4 * XSTG, L_WF = L_WA + 6 * 6144, L_GI = L_WF + 6 * 8192;
constexpr int L_XM = L_GI + 4096, L_GX = L_XM + 2048, L_CNT = L_GX + 512;
constexpr int TILEB = 2080, SLOTB = 8 * TILEB;
constexpr int L_XQ = L_WA, L_F = L_WA + 8192, L_XO = L_F + 65536, L_XL = L_CNT + 1024;
constexpr int C_PROJ = 64, C_STAGE = 72, C_SUM = 80, C_REQ = 88, C_GATE = 96;
static_assert(SLOTB == XSTG && L_XO + 16384 <= L_XM && L_XL + 2048 <= LDS_BYTES, "LDS map");
constexpr unsigned SPIN_CAP = 1u << 14;
constexpr float QSCALE = 0.25f * 1.4426950408889634f, LOG2E = 1.4426950408889634f;
constexpr int AUX_SC1 = 16;

#define MFMA(a, b, c) __builtin_amdgcn_mfma_f32_32x32x16_bf16(a, b, c, 0, 0, 0)

__device__ __forceinline__ u32x4 mk_rsrc(const void* p, unsigned bytes) {
    const uint64_t a = (uint64_t)p;
    u32x4 r = {(unsigned)a, (unsigned)(a >> 32) & 0xffffu, bytes, 0x00020000u};
    return r;
}
__device__ __forceinline__ void drain() { asm volatile("s_waitcnt vmcnt(0)" ::: "memory"); }
__device__ __forceinline__ float mul1(float a, float b) { float r; asm("v_mul_f32 %0, %1, %2" : "=v"(r) : "v"(b), "v"(a)); return r; }
__device__ __forceinline__ float add1(float a, float b) { float r; asm("v_add_f32 %0, %1, %2" : "=v"(r) : "v"(b), "v"(a)); return r; }
__device__ __forceinline__ void fmac1(float& d, float a, float b) { asm("v_fmac_f32 %0, %1, %2" : "+v"(d) : "v"(a), "v"(b)); }
__device__ __forceinline__ unsigned pk2(float lo, float hi) {
    f32x2 v = {lo, hi};
    return __builtin_bit_cast(unsigned, __builtin_convertvector(v, bf16x2));
}
template <int O> __device__ __forceinline__ u32x4 pack8(const f32x16& a, float s) {
    asm("s_nop 0" : "+v"(s));
    u32x4 r = {pk2(mul1(a[O], s), mul1(a[O + 1], s)), pk2(mul1(a[O + 2], s), mul1(a[O + 3], s)), pk2(mul1(a[O + 4], s), mul1(a[O + 5], s)), pk2(mul1(a[O + 6], s), mul1(a[O + 7], s))};
    return r;
}
template <int O> __device__ __forceinline__ u32x4 pack8(const f32x16& a) {
    u32x4 r = {pk2(a[O], a[O + 1]), pk2(a[O + 2], a[O + 3]), pk2(a[O + 4], a[O + 5]), pk2(a[O + 6], a[O + 7])};
    return r;
}
typedef short s16x8 __attribute__((ext_vector_type(8)));
__device__ __forceinline__ u32x4 relu8(u32x4 v) {
    const s16x8 z = {};
    return __builtin_bit_cast(u32x4, __builtin_elementwise_max(__builtin_bit_cast(s16x8, v), z));
}
__device__ __forceinline__ u32x4 not_sentinel(u32x4 v) {
    u32x4 r = {v[0] < 0xFFFFFFFEu ? v[0] : 0xFFFFFFFEu, v[1] < 0xFFFFFFFEu ? v[1] : 0xFFFFFFFEu, v[2] < 0xFFFFFFFEu ? v[2] : 0xFFFFFFFEu, v[3] < 0xFFFFFFFEu ? v[3] : 0xFFFFFFFEu};
    return r;
}
__device__ __forceinline__ int blk_of(int hp, int rb) { return (hp >> 2) | ((hp & 3) << 3) | ((rb >> 3) << 2) | ((rb & 7) << 5); }
__device__ __forceinline__ unsigned ld_flag(__amdgpu_buffer_rsrc_t rs, int off) { return __builtin_amdgcn_raw_buffer_load_b32(rs, off, 0, AUX_SC1); }
__device__ __forceinline__ void st_flag(__amdgpu_buffer_rsrc_t rs, int off, unsigned v) { __builtin_amdgcn_raw_buffer_store_b32(v, rs, off, 0, AUX_SC1); }
template <int SLEEP> __device__ __forceinline__ void wait_flags(__amdgpu_buffer_rsrc_t rs, int off, unsigned tag) {
#pragma unroll 1
    for (unsigned spins = 0; spins < SPIN_CAP; ++spins) {
        asm volatile("" ::: "memory");
        const unsigned v = ld_flag(rs, off);
        if (__all(v == tag)) break;
        __builtin_amdgcn_s_sleep(SLEEP);
    }
    __builtin_amdgcn_fence(__ATOMIC_ACQUIRE, "wavefront");
}

__device__ __forceinline__ f32x16 smf(bf16x8 a, f32x8 b, f32x16 c) {
    return __builtin_amdgcn_smfmac_f32_32x32x32_bf16(a, __builtin_bit_cast(bf16x16, b), c, 0xDDDD, 0, 0);
}
__device__ __forceinline__ f32x8 lo8(const f32x16& s) { return __builtin_shufflevector(s, s, 0, 1, 2, 3, 4, 5, 6, 7); }
__device__ __forceinline__ f32x8 hi8(const f32x16& s) { return __builtin_shufflevector(s, s, 8, 9, 10, 11, 12, 13, 14, 15); }
__device__ __forceinline__ float rowmax16(const f32x16& s) {
    float a = __builtin_fmaxf(__builtin_fmaxf(s[0], s[1]), s[2]), b = __builtin_fmaxf(__builtin_fmaxf(s[3], s[4]), s[5]);
    a = __builtin_fmaxf(__builtin_fmaxf(a, s[6]), s[7]); b = __builtin_fmaxf(__builtin_fmaxf(b, s[8]), s[9]);
    a = __builtin_fmaxf(__builtin_fmaxf(a, s[10]), s[11]); b = __builtin_fmaxf(__builtin_fmaxf(b, s[12]), s[13]);
    a = __builtin_fmaxf(__builtin_fmaxf(a, s[14]), s[15]);
    return __builtin_fmaxf(a, b);
}
__device__ __forceinline__ float xhalf_max(float m) {
    auto rr = __builtin_amdgcn_permlane32_swap(__float_as_uint(m), __float_as_uint(m), false, false);
    return __builtin_fmaxf(__uint_as_float(rr[0]), __uint_as_float(rr[1]));
}
__device__ __forceinline__ f32x16 splat16(float v) { f32x16 r; for (int i = 0; i < 16; ++i) r[i] = v; return r; }

template <int SLEEP> __device__ __forceinline__ void lds_wait_ge(volatile LAS unsigned* p, unsigned want) {
#pragma unroll 1
    for (unsigned spins = 0; spins < (1u << 18); ++spins) {
        if ((unsigned)__builtin_amdgcn_readfirstlane(*p) >= want) break;
        if (SLEEP) __builtin_amdgcn_s_sleep(SLEEP);
    }
    asm volatile("" ::: "memory");
}
template <int SLEEP> __device__ __forceinline__ void lds_wait_all(volatile LAS unsigned* p, unsigned want) {
#pragma unroll 1
    for (unsigned spins = 0; spins < (1u << 18); ++spins) {
        if (__all(*p >= want)) break;
        __builtin_amdgcn_s_sleep(SLEEP);
    }
    asm volatile("" ::: "memory");
}
__device__ __forceinline__ int lane_id() { int l; asm volatile("v_mbcnt_lo_u32_b32 %0, -1, 0\n\tv_mbcnt_hi_u32_b32 %0, -1, %0" : "=v"(l)); return l; }
struct Stage { u32x4 rx, rw; unsigned xv, xs, xd, wv, wd, w0, kx, ku; int w16, hp, rb; };

__device__ __forceinline__ void role_a(Stage& sg, char* ws, ldsp L, unsigned lds0) {
    const int hp = sg.hp, rb = sg.rb, lane = lane_id(), w = sg.w16;
    const int hs = w >> 2, qp = w & 1, kh = ((w >> 1) ^ hs) & 1, tt = 2 * qp + kh, r = lane & 31, hf = lane >> 5, head = 2 * hp + hs, cb = (hp + 8) & 15;
    const __amdgpu_buffer_rsrc_t rs = __builtin_amdgcn_make_buffer_rsrc(ws, 0, WS_END, 0x00020000);
    unsigned a_k, a_v0, a_v1, a_pad, a_q;
    const f32x16 z16 = {};
    {
        const int q = (lane >> 2) & 3, p = lane & 3, ch = (lane >> 4) & 1;
        unsigned sv, st;
        if (w == 0) *(LAS u32x4*)(L + L_CNT + lane * 16) = u32x4{0u, 0u, 0u, 0u};
        const unsigned rbase = lds0 + (hs * 4 + 2 * kh) * TILEB, pad = (r & 11) == 0 ? 2048u : 2064u;
        a_k = rbase + lane * 16;
        a_v0 = rbase + (r < 16 ? 1024 + hf * 512 + r * 16 : pad);
        a_v1 = rbase + (r < 16 ? 1280 + hf * 512 + r * 16 : pad);
        a_pad = lds0 + w * TILEB + (lane < 24 ? lane >> 3 : 0) * SLOTB + 2048 + (lane & 7) * 4;
        a_q = lds0 + L_XQ + (hs * 4 + 2 * qp) * 1024 + lane * 16;
        asm volatile("" : "+v"(a_k), "+v"(a_v0), "+v"(a_v1), "+v"(a_pad), "+v"(a_q));
        const unsigned xb = lds0 + L_X + (r >> 1) * 1040 + (r & 1) * 128 + hf * 16, wb = lds0 + L_WA + hf * 512 + q * 64 + p * 8;
        const int gkv = WS_KV + (2 * hp * 64 + rb * 4) * 2048;
        if (kh == 0) {
            unsigned kw = hf * 512 + ((r & 19) | ((r & 4) << 1) | ((r & 8) >> 1)) * 16;
            asm volatile("" : "+v"(kw));
            f32x16 a0, a1;
            asm volatile(
        "s_mov_b32 %[sv], m0\n\t"
        "s_waitcnt vmcnt(3) lgkmcnt(0)\n\t"
        "s_barrier\n\t"
        "s_add_u32 m0, %[wd], 18432\n\t"
        "s_mul_i32 %[st], %[ku], 98304\n\t"
        "s_add_u32 %[st], %[st], %[w0]\n\t"
        "buffer_load_dwordx4 %[wv], %[rw], %[st] offen lds\n\t"
        "s_add_u32 %[ku], %[ku], 1\n\t"
        "s_and_b32 %[ku], %[ku], 15\n\t"
        "ds_read_b128 v[64:67], %[xb] offset:0\n\t"
        "ds_read_b64_tr_b16 v[72:73], %[wb] offset:0\n\t"
        "ds_read_b64_tr_b16 v[74:75], %[wb] offset:256\n\t"
        "ds_read_b128 v[68:71], %[xb] offset:256\n\t"
        "ds_read_b128 v[76:79], %[xb] offset:32\n\t"
        "ds_read_b64_tr_b16 v[84:85], %[wb] offset:1024\n\t"
        "ds_read_b64_tr_b16 v[86:87], %[wb] offset:1280\n\t"
        "ds_read_b128 v[80:83], %[xb] offset:288\n\t"
        "s_add_u32 m0, %[xd], 33280\n\t"
        "s_add_u32 %[st], %[xs], %[kx]\n\t"
        "buffer_load_dwordx4 %[xv], %[rx], %[st] offen lds\n\t"
        "s_add_u32 %[kx], %[kx], 128\n\t"
        "s_and_b32 %[kx], %[kx], 0x3ff\n\t"
        "s_waitcnt vmcnt(4) lgkmcnt(0)\n\t"
        "s_barrier\n\t"
        "v_mfma_f32_32x32x16_bf16 %[a0], v[72:75], v[64:67], 0\n\t"
        "s_add_u32 m0, %[wd], 24576\n\t"
        "s_mul_i32 %[st], %[ku], 98304\n\t"
        "s_add_u32 %[st], %[st], %[w0]\n\t"
        "buffer_load_dwordx4 %[wv], %[rw], %[st] offen lds\n\t"
        "s_add_u32 %[ku], %[ku], 1\n\t"
        "s_and_b32 %[ku], %[ku], 15\n\t"
        "ds_read_b128 v[88:91], %[xb] offset:64\n\t"
        "ds_read_b64_tr_b16 v[96:97], %[wb] offset:6144\n\t"
        "ds_read_b64_tr_b16 v[98:99], %[wb] offset:6400\n\t"
        "ds_read_b128 v[92:95], %[xb] offset:320\n\t"
        "v_mfma_f32_32x32x16_bf16 %[a1], v[72:75], v[68:71], 0\n\t"
        "ds_read_b128 v[100:103], %[xb] offset:96\n\t"
        "ds_read_b64_tr_b16 v[108:109], %[wb] offset:7168\n\t"
        "ds_read_b64_tr_b16 v[110:111], %[wb] offset:7424\n\t"
        "ds_read_b128 v[104:107], %[xb] offset:352\n\t"
        "v_mfma_f32_32x32x16_bf16 %[a0], v[84:87], v[76:79], %[a0]\n\t"
        "v_mfma_f32_32x32x16_bf16 %[a1], v[84:87], v[80:83], %[a1]\n\t"
        "s_waitcnt vmcnt(3) lgkmcnt(0)\n\t"
        "s_barrier\n\t"
        "v_mfma_f32_32x32x16_bf16 %[a0], v[96:99], v[88:91], %[a0]\n\t"
        "s_add_u32 m0, %[wd], 30720\n\t"
        "s_mul_i32 %[st], %[ku], 98304\n\t"
        "s_add_u32 %[st], %[st], %[w0]\n\t"
        "buffer_load_dwordx4 %[wv], %[rw], %[st] offen lds\n\t"
        "s_add_u32 %[ku], %[ku], 1\n\t"
        "s_and_b32 %[ku], %[ku], 15\n\t"
        "ds_read_b128 v[64:67], %[xb] offset:16640\n\t"
        "ds_read_b64_tr_b16 v[72:73], %[wb] offset:12288\n\t"
        "ds_read_b64_tr_b16 v[74:75], %[wb] offset:12544\n\t"
        "ds_read_b128 v[68:71], %[xb] offset:16896\n\t"
        "v_mfma_f32_32x32x16_bf16 %[a1], v[96:99], v[92:95], %[a1]\n\t"
        "ds_read_b128 v[76:79], %[xb] offset:16672\n\t"
        "ds_read_b64_tr_b16 v[84:85], %[wb] offset:13312\n\t"
        "ds_read_b64_tr_b16 v[86:87], %[wb] offset:13568\n\t"
        "ds_read_b128 v[80:83], %[xb] offset:16928\n\t"
        "v_mfma_f32_32x32x16_bf16 %[a0], v[108:111], v[100:103], %[a0]\n\t"
        "v_mfma_f32_32x32x16_bf16 %[a1], v[108:111], v[104:107], %[a1]\n\t"
        "s_add_u32 m0, %[xd], 49920\n\t"
        "s_add_u32 %[st], %[xs], %[kx]\n\t"
        "buffer_load_dwordx4 %[xv], %[rx], %[st] offen lds\n\t"
        "s_add_u32 %[kx], %[kx], 128\n\t"
        "s_and_b32 %[kx], %[kx], 0x3ff\n\t"
        "s_waitcnt vmcnt(4) lgkmcnt(0)\n\t"
        "s_barrier\n\t"
        "v_mfma_f32_32x32x16_bf16 %[a0], v[72:75], v[64:67], %[a0]\n\t"
        "s_add_u32 m0, %[wd], 0\n\t"
        "s_mul_i32 %[st], %[ku], 98304\n\t"
        "s_add_u32 %[st], %[st], %[w0]\n\t"
        "buffer_load_dwordx4 %[wv], %[rw], %[st] offen lds\n\t"
        "s_add_u32 %[ku], %[ku], 1\n\t"
        "s_and_b32 %[ku], %[ku], 15\n\t"
        "ds_read_b128 v[88:91], %[xb] offset:16704\n\t"
        "ds_read_b64_tr_b16 v[96:97], %[wb] offset:18432\n\t"
        "ds_read_b64_tr_b16 v[98:99], %[wb] offset:18688\n\t"
        "ds_read_b128 v[92:95], %[xb] offset:16960\n\t"
        "v_mfma_f32_32x32x16_bf16 %[a1], v[72:75], v[68:71], %[a1]\n\t"
        "ds_read_b128 v[100:103], %[xb] offset:16736\n\t"
        "ds_read_b64_tr_b16 v[108:109], %[wb] offset:19456\n\t"
        "ds_read_b64_tr_b16 v[110:111], %[wb] offset:19712\n\t"
        "ds_read_b128 v[104:107], %[xb] offset:16992\n\t"
        "v_mfma_f32_32x32x16_bf16 %[a0], v[84:87], v[76:79], %[a0]\n\t"
        "v_mfma_f32_32x32x16_bf16 %[a1], v[84:87], v[80:83], %[a1]\n\t"
        "s_waitcnt vmcnt(3) lgkmcnt(0)\n\t"
        "s_barrier\n\t"
        "v_mfma_f32_32x32x16_bf16 %[a0], v[96:99], v[88:91], %[a0]\n\t"
        "s_add_u32 m0, %[wd], 6144\n\t"
        "s_mul_i32 %[st], %[ku], 98304\n\t"
        "s_add_u32 %[st], %[st], %[w0]\n\t"
        "buffer_load_dwordx4 %[wv], %[rw], %[st] offen lds\n\t"
        "s_add_u32 %[ku], %[ku], 1\n\t"
        "s_and_b32 %[ku], %[ku], 15\n\t"
        "ds_read_b128 v[64:67], %[xb] offset:33280\n\t"
        "ds_read_b64_tr_b16 v[72:73], %[wb] offset:24576\n\t"
        "ds_read_b64_tr_b16 v[74:75], %[wb] offset:24832\n\t"
        "ds_read_b128 v[68:71], %[xb] offset:33536\n\t"
        "v_mfma_f32_32x32x16_bf16 %[a1], v[96:99], v[92:95], %[a1]\n\t"
        "ds_read_b128 v[76:79], %[xb] offset:33312\n\t"
        "ds_read_b64_tr_b16 v[84:85], %[wb] offset:25600\n\t"
        "ds_read_b64_tr_b16 v[86:87], %[wb] offset:25856\n\t"
        "ds_read_b128 v[80:83], %[xb] offset:33568\n\t"
        "v_mfma_f32_32x32x16_bf16 %[a0], v[108:111], v[100:103], %[a0]\n\t"
        "v_mfma_f32_32x32x16_bf16 %[a1], v[108:111], v[104:107], %[a1]\n\t"
        "s_add_u32 m0, %[xd], 0\n\t"
        "s_add_u32 %[st], %[xs], %[kx]\n\t"
        "buffer_load_dwordx4 %[xv], %[rx], %[st] offen lds\n\t"
        "s_add_u32 %[kx], %[kx], 128\n\t"
        "s_and_b32 %[kx], %[kx], 0x3ff\n\t"
        "s_waitcnt vmcnt(4) lgkmcnt(0)\n\t"
        "s_barrier\n\t"
        "v_mfma_f32_32x32x16_bf16 %[a0], v[72:75], v[64:67], %[a0]\n\t"
        "s_add_u32 m0, %[wd], 12288\n\t"
        "s_mul_i32 %[st], %[ku], 98304\n\t"
        "s_add_u32 %[st], %[st], %[w0]\n\t"
        "buffer_load_dwordx4 %[wv], %[rw], %[st] offen lds\n\t"
        "s_add_u32 %[ku], %[ku], 1\n\t"
        "s_and_b32 %[ku], %[ku], 15\n\t"
        "ds_read_b128 v[88:91], %[xb] offset:33344\n\t"
        "ds_read_b64_tr_b16 v[96:97], %[wb] offset:30720\n\t"
        "ds_read_b64_tr_b16 v[98:99], %[wb] offset:30976\n\t"
        "ds_read_b128 v[92:95], %[xb] offset:33600\n\t"
        "v_mfma_f32_32x32x16_bf16 %[a1], v[72:75], v[68:71], %[a1]\n\t"
        "ds_read_b128 v[100:103], %[xb] offset:33376\n\t"
        "ds_read_b64_tr_b16 v[108:109], %[wb] offset:31744\n\t"
        "ds_read_b64_tr_b16 v[110:111], %[wb] offset:32000\n\t"
        "ds_read_b128 v[104:107], %[xb] offset:33632\n\t"
        "v_mfma_f32_32x32x16_bf16 %[a0], v[84:87], v[76:79], %[a0]\n\t"
        "v_mfma_f32_32x32x16_bf16 %[a1], v[84:87], v[80:83], %[a1]\n\t"
        "s_waitcnt vmcnt(3) lgkmcnt(0)\n\t"
        "s_barrier\n\t"
        "v_mfma_f32_32x32x16_bf16 %[a0], v[96:99], v[88:91], %[a0]\n\t"
        "s_add_u32 m0, %[wd], 18432\n\t"
        "s_mul_i32 %[st], %[ku], 98304\n\t"
        "s_add_u32 %[st], %[st], %[w0]\n\t"
        "buffer_load_dwordx4 %[wv], %[rw], %[st] offen lds\n\t"
        "s_add_u32 %[ku], %[ku], 1\n\t"
        "s_and_b32 %[ku], %[ku], 15\n\t"
        "ds_read_b128 v[64:67], %[xb] offset:49920\n\t"
        "ds_read_b64_tr_b16 v[72:73], %[wb] offset:0\n\t"
        "ds_read_b64_tr_b16 v[74:75], %[wb] offset:256\n\t"
        "ds_read_b128 v[68:71], %[xb] offset:50176\n\t"
        "v_mfma_f32_32x32x16_bf16 %[a1], v[96:99], v[92:95], %[a1]\n\t"
        "ds_read_b128 v[76:79], %[xb] offset:49952\n\t"
        "ds_read_b64_tr_b16 v[84:85], %[wb] offset:1024\n\t"
        "ds_read_b64_tr_b16 v[86:87], %[wb] offset:1280\n\t"
        "ds_read_b128 v[80:83], %[xb] offset:50208\n\t"
        "v_mfma_f32_32x32x16_bf16 %[a0], v[108:111], v[100:103], %[a0]\n\t"
        "v_mfma_f32_32x32x16_bf16 %[a1], v[108:111], v[104:107], %[a1]\n\t"
        "s_add_u32 m0, %[xd], 16640\n\t"
        "s_add_u32 %[st], %[xs], %[kx]\n\t"
        "buffer_load_dwordx4 %[xv], %[rx], %[st] offen lds\n\t"
        "s_add_u32 %[kx], %[kx], 128\n\t"
        "s_and_b32 %[kx], %[kx], 0x3ff\n\t"
        "s_waitcnt vmcnt(4) lgkmcnt(0)\n\t"
        "s_barrier\n\t"
        "v_mfma_f32_32x32x16_bf16 %[a0], v[72:75], v[64:67], %[a0]\n\t"
        "s_add_u32 m0, %[wd], 24576\n\t"
        "s_mul_i32 %[st], %[ku], 98304\n\t"
        "s_add_u32 %[st], %[st], %[w0]\n\t"
        "buffer_load_dwordx4 %[wv], %[rw], %[st] offen lds\n\t"
        "s_add_u32 %[ku], %[ku], 1\n\t"
        "s_and_b32 %[ku], %[ku], 15\n\t"
        "ds_read_b128 v[88:91], %[xb] offset:49984\n\t"
        "ds_read_b64_tr_b16 v[96:97], %[wb] offset:6144\n\t"
        "ds_read_b64_tr_b16 v[98:99], %[wb] offset:6400\n\t"
        "ds_read_b128 v[92:95], %[xb] offset:50240\n\t"
        "v_mfma_f32_32x32x16_bf16 %[a1], v[72:75], v[68:71], %[a1]\n\t"
        "ds_read_b128 v[100:103], %[xb] offset:50016\n\t"
        "ds_read_b64_tr_b16 v[108:109], %[wb] offset:7168\n\t"
        "ds_read_b64_tr_b16 v[110:111], %[wb] offset:7424\n\t"
        "ds_read_b128 v[104:107], %[xb] offset:50272\n\t"
        "v_mfma_f32_32x32x16_bf16 %[a0], v[84:87], v[76:79], %[a0]\n\t"
        "v_mfma_f32_32x32x16_bf16 %[a1], v[84:87], v[80:83], %[a1]\n\t"
        "s_waitcnt vmcnt(3) lgkmcnt(0)\n\t"
        "s_barrier\n\t"
        "v_mfma_f32_32x32x16_bf16 %[a0], v[96:99], v[88:91], %[a0]\n\t"
        "s_add_u32 m0, %[wd], 30720\n\t"
        "s_mul_i32 %[st], %[ku], 98304\n\t"
        "s_add_u32 %[st], %[st], %[w0]\n\t"
        "buffer_load_dwordx4 %[wv], %[rw], %[st] offen lds\n\t"
        "s_add_u32 %[ku], %[ku], 1\n\t"
        "s_and_b32 %[ku], %[ku], 15\n\t"
        "ds_read_b128 v[64:67], %[xb] offset:0\n\t"
        "ds_read_b64_tr_b16 v[72:73], %[wb] offset:12288\n\t"
        "ds_read_b64_tr_b16 v[74:75], %[wb] offset:12544\n\t"
        "ds_read_b128 v[68:71], %[xb] offset:256\n\t"
        "v_mfma_f32_32x32x16_bf16 %[a1], v[96:99], v[92:95], %[a1]\n\t"
        "ds_read_b128 v[76:79], %[xb] offset:32\n\t"
        "ds_read_b64_tr_b16 v[84:85], %[wb] offset:13312\n\t"
        "ds_read_b64_tr_b16 v[86:87], %[wb] offset:13568\n\t"
        "ds_read_b128 v[80:83], %[xb] offset:288\n\t"
        "v_mfma_f32_32x32x16_bf16 %[a0], v[108:111], v[100:103], %[a0]\n\t"
        "v_mfma_f32_32x32x16_bf16 %[a1], v[108:111], v[104:107], %[a1]\n\t"
        "s_add_u32 m0, %[xd], 33280\n\t"
        "s_add_u32 %[st], %[xs], %[kx]\n\t"
        "buffer_load_dwordx4 %[xv], %[rx], %[st] offen lds\n\t"
        "s_add_u32 %[kx], %[kx], 128\n\t"
        "s_and_b32 %[kx], %[kx], 0x3ff\n\t"
        "s_waitcnt vmcnt(4) lgkmcnt(0)\n\t"
        "s_barrier\n\t"
        "v_mfma_f32_32x32x16_bf16 %[a0], v[72:75], v[64:67], %[a0]\n\t"
        "s_add_u32 m0, %[wd], 0\n\t"
        "s_mul_i32 %[st], %[ku], 98304\n\t"
        "s_add_u32 %[st], %[st], %[w0]\n\t"
        "buffer_load_dwordx4 %[wv], %[rw], %[st] offen lds\n\t"
        "s_add_u32 %[ku], %[ku], 1\n\t"
        "s_and_b32 %[ku], %[ku], 15\n\t"
        "ds_read_b128 v[88:91], %[xb] offset:64\n\t"
        "ds_read_b64_tr_b16 v[96:97], %[wb] offset:18432\n\t"
        "ds_read_b64_tr_b16 v[98:99], %[wb] offset:18688\n\t"
        "ds_read_b128 v[92:95], %[xb] offset:320\n\t"
        "v_mfma_f32_32x32x16_bf16 %[a1], v[72:75], v[68:71], %[a1]\n\t"
        "ds_read_b128 v[100:103], %[xb] offset:96\n\t"
        "ds_read_b64_tr_b16 v[108:109], %[wb] offset:19456\n\t"
        "ds_read_b64_tr_b16 v[110:111], %[wb] offset:19712\n\t"
        "ds_read_b128 v[104:107], %[xb] offset:352\n\t"
        "v_mfma_f32_32x32x16_bf16 %[a0], v[84:87], v[76:79], %[a0]\n\t"
        "v_mfma_f32_32x32x16_bf16 %[a1], v[84:87], v[80:83], %[a1]\n\t"
        "s_waitcnt vmcnt(3) lgkmcnt(0)\n\t"
        "s_barrier\n\t"
        "v_mfma_f32_32x32x16_bf16 %[a0], v[96:99], v[88:91], %[a0]\n\t"
        "s_add_u32 m0, %[wd], 6144\n\t"
        "s_mul_i32 %[st], %[ku], 98304\n\t"
        "s_add_u32 %[st], %[st], %[w0]\n\t"
        "buffer_load_dwordx4 %[wv], %[rw], %[st] offen lds\n\t"
        "s_add_u32 %[ku], %[ku], 1\n\t"
        "s_and_b32 %[ku], %[ku], 15\n\t"
        "ds_read_b128 v[64:67], %[xb] offset:16640\n\t"
        "ds_read_b64_tr_b16 v[72:73], %[wb] offset:24576\n\t"
        "ds_read_b64_tr_b16 v[74:75], %[wb] offset:24832\n\t"
        "ds_read_b128 v[68:71], %[xb] offset:16896\n\t"
        "v_mfma_f32_32x32x16_bf16 %[a1], v[96:99], v[92:95], %[a1]\n\t"
        "ds_read_b128 v[76:79], %[xb] offset:16672\n\t"
        "ds_read_b64_tr_b16 v[84:85], %[wb] offset:25600\n\t"
        "ds_read_b64_tr_b16 v[86:87], %[wb] offset:25856\n\t"
        "ds_read_b128 v[80:83], %[xb] offset:16928\n\t"
        "v_mfma_f32_32x32x16_bf16 %[a0], v[108:111], v[100:103], %[a0]\n\t"
        "v_mfma_f32_32x32x16_bf16 %[a1], v[108:111], v[104:107], %[a1]\n\t"
        "s_add_u32 m0, %[xd], 49920\n\t"
        "s_add_u32 %[st], %[xs], %[kx]\n\t"
        "buffer_load_dwordx4 %[xv], %[rx], %[st] offen lds\n\t"
        "s_add_u32 %[kx], %[kx], 128\n\t"
        "s_and_b32 %[kx], %[kx], 0x3ff\n\t"
        "s_waitcnt vmcnt(4) lgkmcnt(0)\n\t"
        "s_barrier\n\t"
        "v_mfma_f32_32x32x16_bf16 %[a0], v[72:75], v[64:67], %[a0]\n\t"
        "s_add_u32 m0, %[wd], 12288\n\t"
        "s_mul_i32 %[st], %[ku], 98304\n\t"
        "s_add_u32 %[st], %[st], %[w0]\n\t"
        "buffer_load_dwordx4 %[wv], %[rw], %[st] offen lds\n\t"
        "s_add_u32 %[ku], %[ku], 1\n\t"
        "s_and_b32 %[ku], %[ku], 15\n\t"
        "ds_read_b128 v[88:91], %[xb] offset:16704\n\t"
        "ds_read_b64_tr_b16 v[96:97], %[wb] offset:30720\n\t"
        "ds_read_b64_tr_b16 v[98:99], %[wb] offset:30976\n\t"
        "ds_read_b128 v[92:95], %[xb] offset:16960\n\t"
        "v_mfma_f32_32x32x16_bf16 %[a1], v[72:75], v[68:71], %[a1]\n\t"
        "ds_read_b128 v[100:103], %[xb] offset:16736\n\t"
        "ds_read_b64_tr_b16 v[108:109], %[wb] offset:31744\n\t"
        "ds_read_b64_tr_b16 v[110:111], %[wb] offset:32000\n\t"
        "ds_read_b128 v[104:107], %[xb] offset:16992\n\t"
        "v_mfma_f32_32x32x16_bf16 %[a0], v[84:87], v[76:79], %[a0]\n\t"
        "v_mfma_f32_32x32x16_bf16 %[a1], v[84:87], v[80:83], %[a1]\n\t"
        "s_waitcnt vmcnt(3) lgkmcnt(0)\n\t"
        "s_barrier\n\t"
        "v_mfma_f32_32x32x16_bf16 %[a0], v[96:99], v[88:91], %[a0]\n\t"
        "s_add_u32 m0, %[wd], 18432\n\t"
        "s_mul_i32 %[st], %[ku], 98304\n\t"
        "s_add_u32 %[st], %[st], %[w0]\n\t"
        "buffer_load_dwordx4 %[wv], %[rw], %[st] offen lds\n\t"
        "s_add_u32 %[ku], %[ku], 1\n\t"
        "s_and_b32 %[ku], %[ku], 15\n\t"
        "ds_read_b128 v[64:67], %[xb] offset:33280\n\t"
        "ds_read_b64_tr_b16 v[72:73], %[wb] offset:0\n\t"
        "ds_read_b64_tr_b16 v[74:75], %[wb] offset:256\n\t"
        "ds_read_b128 v[68:71], %[xb] offset:33536\n\t"
        "v_mfma_f32_32x32x16_bf16 %[a1], v[96:99], v[92:95], %[a1]\n\t"
        "ds_read_b128 v[76:79], %[xb] offset:33312\n\t"
        "ds_read_b64_tr_b16 v[84:85], %[wb] offset:1024\n\t"
        "ds_read_b64_tr_b16 v[86:87], %[wb] offset:1280\n\t"
        "ds_read_b128 v[80:83], %[xb] offset:33568\n\t"
        "v_mfma_f32_32x32x16_bf16 %[a0], v[108:111], v[100:103], %[a0]\n\t"
        "v_mfma_f32_32x32x16_bf16 %[a1], v[108:111], v[104:107], %[a1]\n\t"
        "s_waitcnt vmcnt(3) lgkmcnt(0)\n\t"
        "s_barrier\n\t"
        "v_mfma_f32_32x32x16_bf16 %[a0], v[72:75], v[64:67], %[a0]\n\t"
        "ds_read_b128 v[88:91], %[xb] offset:33344\n\t"
        "ds_read_b64_tr_b16 v[96:97], %[wb] offset:6144\n\t"
        "ds_read_b64_tr_b16 v[98:99], %[wb] offset:6400\n\t"
        "ds_read_b128 v[92:95], %[xb] offset:33600\n\t"
        "v_mfma_f32_32x32x16_bf16 %[a1], v[72:75], v[68:71], %[a1]\n\t"
        "ds_read_b128 v[100:103], %[xb] offset:33376\n\t"
        "ds_read_b64_tr_b16 v[108:109], %[wb] offset:7168\n\t"
        "ds_read_b64_tr_b16 v[110:111], %[wb] offset:7424\n\t"
        "ds_read_b128 v[104:107], %[xb] offset:33632\n\t"
        "v_mfma_f32_32x32x16_bf16 %[a0], v[84:87], v[76:79], %[a0]\n\t"
        "v_mfma_f32_32x32x16_bf16 %[a1], v[84:87], v[80:83], %[a1]\n\t"
        "s_waitcnt vmcnt(1) lgkmcnt(0)\n\t"
        "s_barrier\n\t"
        "v_mfma_f32_32x32x16_bf16 %[a0], v[96:99], v[88:91], %[a0]\n\t"
        "ds_read_b128 v[64:67], %[xb] offset:49920\n\t"
        "ds_read_b64_tr_b16 v[72:73], %[wb] offset:12288\n\t"
        "ds_read_b64_tr_b16 v[74:75], %[wb] offset:12544\n\t"
        "ds_read_b128 v[68:71], %[xb] offset:50176\n\t"
        "v_mfma_f32_32x32x16_bf16 %[a1], v[96:99], v[92:95], %[a1]\n\t"
        "ds_read_b128 v[76:79], %[xb] offset:49952\n\t"
        "ds_read_b64_tr_b16 v[84:85], %[wb] offset:13312\n\t"
        "ds_read_b64_tr_b16 v[86:87], %[wb] offset:13568\n\t"
        "ds_read_b128 v[80:83], %[xb] offset:50208\n\t"
        "v_mfma_f32_32x32x16_bf16 %[a0], v[108:111], v[100:103], %[a0]\n\t"
        "v_mfma_f32_32x32x16_bf16 %[a1], v[108:111], v[104:107], %[a1]\n\t"
        "s_waitcnt vmcnt(0) lgkmcnt(0)\n\t"
        "s_barrier\n\t"
        "v_mfma_f32_32x32x16_bf16 %[a0], v[72:75], v[64:67], %[a0]\n\t"
        "ds_read_b128 v[88:91], %[xb] offset:49984\n\t"
        "ds_read_b64_tr_b16 v[96:97], %[wb] offset:18432\n\t"
        "ds_read_b64_tr_b16 v[98:99], %[wb] offset:18688\n\t"
        "ds_read_b128 v[92:95], %[xb] offset:50240\n\t"
        "v_mfma_f32_32x32x16_bf16 %[a1], v[72:75], v[68:71], %[a1]\n\t"
        "ds_read_b128 v[100:103], %[xb] offset:50016\n\t"
        "ds_read_b64_tr_b16 v[108:109], %[wb] offset:19456\n\t"
        "ds_read_b64_tr_b16 v[110:111], %[wb] offset:19712\n\t"
        "ds_read_b128 v[104:107], %[xb] offset:50272\n\t"
        "v_mfma_f32_32x32x16_bf16 %[a0], v[84:87], v[76:79], %[a0]\n\t"
        "v_mfma_f32_32x32x16_bf16 %[a1], v[84:87], v[80:83], %[a1]\n\t"
        "s_waitcnt vmcnt(0) lgkmcnt(0)\n\t"
        "s_barrier\n\t"
        "v_mfma_f32_32x32x16_bf16 %[a0], v[96:99], v[88:91], %[a0]\n\t"
        "v_mfma_f32_32x32x16_bf16 %[a1], v[96:99], v[92:95], %[a1]\n\t"
        "v_mfma_f32_32x32x16_bf16 %[a0], v[108:111], v[100:103], %[a0]\n\t"
        "v_mfma_f32_32x32x16_bf16 %[a1], v[108:111], v[104:107], %[a1]\n\t"
        "s_mov_b32 m0, %[sv]\n\t"
        "s_nop 10\n\t"
                : [a0] "=&v"(a0), [a1] "=&v"(a1), [sv] "=&s"(sv), [st] "=&s"(st), [kx] "+s"(sg.kx), [ku] "+s"(sg.ku)
                : [xv] "v"(sg.xv), [wv] "v"(sg.wv), [rx] "s"(sg.rx), [rw] "s"(sg.rw), [xs] "s"(sg.xs), [xd] "s"(sg.xd), [wd] "s"(sg.wd), [w0] "s"(sg.w0),
                  [xb] "v"(xb + qp * 512), [wb] "v"(wb + ch * 2048 + (ch ^ hs) * 32)
                : "memory", "scc", "v64", "v65", "v66", "v67", "v68", "v69", "v70", "v71", "v72", "v73", "v74", "v75", "v76", "v77", "v78", "v79", "v80", "v81", "v82", "v83", "v84", "v85", "v86", "v87", "v88", "v89", "v90", "v91", "v92", "v93", "v94", "v95", "v96", "v97", "v98", "v99", "v100", "v101", "v102", "v103", "v104", "v105", "v106", "v107", "v108", "v109", "v110", "v111");

            const u32x4 k0 = not_sentinel(pack8<8>(a0)), k1 = not_sentinel(pack8<8>(a1));
            __builtin_amdgcn_raw_buffer_store_b128(k0, rs, kw, gkv + (hs * 64 + 2 * qp) * 2048, AUX_SC1);
            __builtin_amdgcn_raw_buffer_store_b128(k1, rs, kw + 2048, gkv + (hs * 64 + 2 * qp) * 2048, AUX_SC1);
            __builtin_amdgcn_sched_barrier(0);
            const ldsp kt = L + (hs * 4 + 2 * qp) * TILEB + kw;
            *(LAS u32x4*)kt = k0; *(LAS u32x4*)(kt + TILEB) = k1;
            float d0 = 0.f, d1 = 0.f;
#pragma unroll
            for (int i = 0; i < 8; ++i) { a0[i] = mul1(a0[i], QSCALE); a1[i] = mul1(a1[i], QSCALE); }
            *(LAS u32x4*)(uintptr_t)a_q = pack8<0>(a0); *(LAS u32x4*)(uintptr_t)(a_q + 1024) = pack8<0>(a1);
#pragma unroll
            for (int i = 0; i < 8; ++i) { fmac1(d0, a0[i], a0[8 + i]); fmac1(d1, a1[i], a1[8 + i]); }
            asm("s_nop 1" : "+v"(d0), "+v"(d1));
            const auto e0 = __builtin_amdgcn_permlane32_swap(__float_as_uint(d0), __float_as_uint(d1), false, false);
            *(LAS float*)(L + L_XM + (hs * 2 + qp) * 256 + lane * 4) = -xhalf_max(__uint_as_float(e0[0]) + __uint_as_float(e0[1]));
        } else {
            const int tv = w - 2;
            unsigned vw = 1024 + hf * 512 + (r & 15) * 16;
            unsigned vg = vw + ch * 64 * 2048, vl = lds0 + vw + (ch * 4 + tv) * TILEB;
            asm volatile("" : "+v"(vg), "+v"(vl));
            f32x16 a0;
            if (cb != 14) {
            asm volatile(
        "s_mov_b32 %[sv], m0\n\t"
        "s_waitcnt vmcnt(3) lgkmcnt(0)\n\t"
        "s_barrier\n\t"
        "s_add_u32 m0, %[wd], 18432\n\t"
        "s_mul_i32 %[st], %[ku], 98304\n\t"
        "s_add_u32 %[st], %[st], %[w0]\n\t"
        "buffer_load_dwordx4 %[wv], %[rw], %[st] offen lds\n\t"
        "s_add_u32 %[ku], %[ku], 1\n\t"
        "s_and_b32 %[ku], %[ku], 15\n\t"
        "ds_read_b128 v[64:67], %[xb] offset:0\n\t"
        "ds_read_b64_tr_b16 v[72:73], %[wb] offset:0\n\t"
        "ds_read_b64_tr_b16 v[74:75], %[wb] offset:256\n\t"
        "ds_read_b128 v[76:79], %[xb] offset:32\n\t"
        "ds_read_b64_tr_b16 v[84:85], %[wb] offset:1024\n\t"
        "ds_read_b64_tr_b16 v[86:87], %[wb] offset:1280\n\t"
        "s_add_u32 m0, %[xd], 33280\n\t"
        "s_add_u32 %[st], %[xs], %[kx]\n\t"
        "buffer_load_dwordx4 %[xv], %[rx], %[st] offen lds\n\t"
        "s_add_u32 %[kx], %[kx], 128\n\t"
        "s_and_b32 %[kx], %[kx], 0x3ff\n\t"
        "s_waitcnt vmcnt(4) lgkmcnt(0)\n\t"
        "s_barrier\n\t"
        "v_mfma_f32_32x32x16_bf16 %[a0], v[64:67], v[72:75], 0\n\t"
        "s_add_u32 m0, %[wd], 24576\n\t"
        "s_mul_i32 %[st], %[ku], 98304\n\t"
        "s_add_u32 %[st], %[st], %[w0]\n\t"
        "buffer_load_dwordx4 %[wv], %[rw], %[st] offen lds\n\t"
        "s_add_u32 %[ku], %[ku], 1\n\t"
        "s_and_b32 %[ku], %[ku], 15\n\t"
        "ds_read_b128 v[88:91], %[xb] offset:64\n\t"
        "ds_read_b64_tr_b16 v[96:97], %[wb] offset:6144\n\t"
        "ds_read_b64_tr_b16 v[98:99], %[wb] offset:6400\n\t"
        "v_mfma_f32_32x32x16_bf16 %[a0], v[76:79], v[84:87], %[a0]\n\t"
        "ds_read_b128 v[100:103], %[xb] offset:96\n\t"
        "ds_read_b64_tr_b16 v[108:109], %[wb] offset:7168\n\t"
        "ds_read_b64_tr_b16 v[110:111], %[wb] offset:7424\n\t"
        "s_waitcnt vmcnt(3) lgkmcnt(0)\n\t"
        "s_barrier\n\t"
        "v_mfma_f32_32x32x16_bf16 %[a0], v[88:91], v[96:99], %[a0]\n\t"
        "s_add_u32 m0, %[wd], 30720\n\t"
        "s_mul_i32 %[st], %[ku], 98304\n\t"
        "s_add_u32 %[st], %[st], %[w0]\n\t"
        "buffer_load_dwordx4 %[wv], %[rw], %[st] offen lds\n\t"
        "s_add_u32 %[ku], %[ku], 1\n\t"
        "s_and_b32 %[ku], %[ku], 15\n\t"
        "ds_read_b128 v[64:67], %[xb] offset:16640\n\t"
        "ds_read_b64_tr_b16 v[72:73], %[wb] offset:12288\n\t"
        "ds_read_b64_tr_b16 v[74:75], %[wb] offset:12544\n\t"
        "v_mfma_f32_32x32x16_bf16 %[a0], v[100:103], v[108:111], %[a0]\n\t"
        "ds_read_b128 v[76:79], %[xb] offset:16672\n\t"
        "ds_read_b64_tr_b16 v[84:85], %[wb] offset:13312\n\t"
        "ds_read_b64_tr_b16 v[86:87], %[wb] offset:13568\n\t"
        "s_add_u32 m0, %[xd], 49920\n\t"
        "s_add_u32 %[st], %[xs], %[kx]\n\t"
        "buffer_load_dwordx4 %[xv], %[rx], %[st] offen lds\n\t"
        "s_add_u32 %[kx], %[kx], 128\n\t"
        "s_and_b32 %[kx], %[kx], 0x3ff\n\t"
        "s_waitcnt vmcnt(4) lgkmcnt(0)\n\t"
        "s_barrier\n\t"
        "v_mfma_f32_32x32x16_bf16 %[a0], v[64:67], v[72:75], %[a0]\n\t"
        "s_add_u32 m0, %[wd], 0\n\t"
        "s_mul_i32 %[st], %[ku], 98304\n\t"
        "s_add_u32 %[st], %[st], %[w0]\n\t"
        "buffer_load_dwordx4 %[wv], %[rw], %[st] offen lds\n\t"
        "s_add_u32 %[ku], %[ku], 1\n\t"
        "s_and_b32 %[ku], %[ku], 15\n\t"
        "ds_read_b128 v[88:91], %[xb] offset:16704\n\t"
        "ds_read_b64_tr_b16 v[96:97], %[wb] offset:18432\n\t"
        "ds_read_b64_tr_b16 v[98:99], %[wb] offset:18688\n\t"
        "v_mfma_f32_32x32x16_bf16 %[a0], v[76:79], v[84:87], %[a0]\n\t"
        "ds_read_b128 v[100:103], %[xb] offset:16736\n\t"
        "ds_read_b64_tr_b16 v[108:109], %[wb] offset:19456\n\t"
        "ds_read_b64_tr_b16 v[110:111], %[wb] offset:19712\n\t"
        "s_waitcnt vmcnt(3) lgkmcnt(0)\n\t"
        "s_barrier\n\t"
        "v_mfma_f32_32x32x16_bf16 %[a0], v[88:91], v[96:99], %[a0]\n\t"
        "s_add_u32 m0, %[wd], 6144\n\t"
        "s_mul_i32 %[st], %[ku], 98304\n\t"
        "s_add_u32 %[st], %[st], %[w0]\n\t"
        "buffer_load_dwordx4 %[wv], %[rw], %[st] offen lds\n\t"
        "s_add_u32 %[ku], %[ku], 1\n\t"
        "s_and_b32 %[ku], %[ku], 15\n\t"
        "ds_read_b128 v[64:67], %[xb] offset:33280\n\t"
        "ds_read_b64_tr_b16 v[72:73], %[wb] offset:24576\n\t"
        "ds_read_b64_tr_b16 v[74:75], %[wb] offset:24832\n\t"
        "v_mfma_f32_32x32x16_bf16 %[a0], v[100:103], v[108:111], %[a0]\n\t"
        "ds_read_b128 v[76:79], %[xb] offset:33312\n\t"
        "ds_read_b64_tr_b16 v[84:85], %[wb] offset:25600\n\t"
        "ds_read_b64_tr_b16 v[86:87], %[wb] offset:25856\n\t"
        "s_add_u32 m0, %[xd], 0\n\t"
        "s_add_u32 %[st], %[xs], %[kx]\n\t"
        "buffer_load_dwordx4 %[xv], %[rx], %[st] offen lds\n\t"
        "s_add_u32 %[kx], %[kx], 128\n\t"
        "s_and_b32 %[kx], %[kx], 0x3ff\n\t"
        "s_waitcnt vmcnt(4) lgkmcnt(0)\n\t"
        "s_barrier\n\t"
        "v_mfma_f32_32x32x16_bf16 %[a0], v[64:67], v[72:75], %[a0]\n\t"
        "s_add_u32 m0, %[wd], 12288\n\t"
        "s_mul_i32 %[st], %[ku], 98304\n\t"
        "s_add_u32 %[st], %[st], %[w0]\n\t"
        "buffer_load_dwordx4 %[wv], %[rw], %[st] offen lds\n\t"
        "s_add_u32 %[ku], %[ku], 1\n\t"
        "s_and_b32 %[ku], %[ku], 15\n\t"
        "ds_read_b128 v[88:91], %[xb] offset:33344\n\t"
        "ds_read_b64_tr_b16 v[96:97], %[wb] offset:30720\n\t"
        "ds_read_b64_tr_b16 v[98:99], %[wb] offset:30976\n\t"
        "v_mfma_f32_32x32x16_bf16 %[a0], v[76:79], v[84:87], %[a0]\n\t"
        "ds_read_b128 v[100:103], %[xb] offset:33376\n\t"
        "ds_read_b64_tr_b16 v[108:109], %[wb] offset:31744\n\t"
        "ds_read_b64_tr_b16 v[110:111], %[wb] offset:32000\n\t"
        "s_waitcnt vmcnt(3) lgkmcnt(0)\n\t"
        "s_barrier\n\t"
        "v_mfma_f32_32x32x16_bf16 %[a0], v[88:91], v[96:99], %[a0]\n\t"
        "s_add_u32 m0, %[wd], 18432\n\t"
        "s_mul_i32 %[st], %[ku], 98304\n\t"
        "s_add_u32 %[st], %[st], %[w0]\n\t"
        "buffer_load_dwordx4 %[wv], %[rw], %[st] offen lds\n\t"
        "s_add_u32 %[ku], %[ku], 1\n\t"
        "s_and_b32 %[ku], %[ku], 15\n\t"
        "ds_read_b128 v[64:67], %[xb] offset:49920\n\t"
        "ds_read_b64_tr_b16 v[72:73], %[wb] offset:0\n\t"
        "ds_read_b64_tr_b16 v[74:75], %[wb] offset:256\n\t"
        "v_mfma_f32_32x32x16_bf16 %[a0], v[100:103], v[108:111], %[a0]\n\t"
        "ds_read_b128 v[76:79], %[xb] offset:49952\n\t"
        "ds_read_b64_tr_b16 v[84:85], %[wb] offset:1024\n\t"
        "ds_read_b64_tr_b16 v[86:87], %[wb] offset:1280\n\t"
        "s_add_u32 m0, %[xd], 16640\n\t"
        "s_add_u32 %[st], %[xs], %[kx]\n\t"
        "buffer_load_dwordx4 %[xv], %[rx], %[st] offen lds\n\t"
        "s_add_u32 %[kx], %[kx], 128\n\t"
        "s_and_b32 %[kx], %[kx], 0x3ff\n\t"
        "s_waitcnt vmcnt(4) lgkmcnt(0)\n\t"
        "s_barrier\n\t"
        "v_mfma_f32_32x32x16_bf16 %[a0], v[64:67], v[72:75], %[a0]\n\t"
        "s_add_u32 m0, %[wd], 24576\n\t"
        "s_mul_i32 %[st], %[ku], 98304\n\t"
        "s_add_u32 %[st], %[st], %[w0]\n\t"
        "buffer_load_dwordx4 %[wv], %[rw], %[st] offen lds\n\t"
        "s_add_u32 %[ku], %[ku], 1\n\t"
        "s_and_b32 %[ku], %[ku], 15\n\t"
        "ds_read_b128 v[88:91], %[xb] offset:49984\n\t"
        "ds_read_b64_tr_b16 v[96:97], %[wb] offset:6144\n\t"
        "ds_read_b64_tr_b16 v[98:99], %[wb] offset:6400\n\t"
        "v_mfma_f32_32x32x16_bf16 %[a0], v[76:79], v[84:87], %[a0]\n\t"
        "ds_read_b128 v[100:103], %[xb] offset:50016\n\t"
        "ds_read_b64_tr_b16 v[108:109], %[wb] offset:7168\n\t"
        "ds_read_b64_tr_b16 v[110:111], %[wb] offset:7424\n\t"
        "s_waitcnt vmcnt(3) lgkmcnt(0)\n\t"
        "s_barrier\n\t"
        "v_mfma_f32_32x32x16_bf16 %[a0], v[88:91], v[96:99], %[a0]\n\t"
        "s_add_u32 m0, %[wd], 30720\n\t"
        "s_mul_i32 %[st], %[ku], 98304\n\t"
        "s_add_u32 %[st], %[st], %[w0]\n\t"
        "buffer_load_dwordx4 %[wv], %[rw], %[st] offen lds\n\t"
        "s_add_u32 %[ku], %[ku], 1\n\t"
        "s_and_b32 %[ku], %[ku], 15\n\t"
        "ds_read_b128 v[64:67], %[xb] offset:0\n\t"
        "ds_read_b64_tr_b16 v[72:73], %[wb] offset:12288\n\t"
        "ds_read_b64_tr_b16 v[74:75], %[wb] offset:12544\n\t"
        "v_mfma_f32_32x32x16_bf16 %[a0], v[100:103], v[108:111], %[a0]\n\t"
        "ds_read_b128 v[76:79], %[xb] offset:32\n\t"
        "ds_read_b64_tr_b16 v[84:85], %[wb] offset:13312\n\t"
        "ds_read_b64_tr_b16 v[86:87], %[wb] offset:13568\n\t"
        "s_add_u32 m0, %[xd], 33280\n\t"
        "s_add_u32 %[st], %[xs], %[kx]\n\t"
        "buffer_load_dwordx4 %[xv], %[rx], %[st] offen lds\n\t"
        "s_add_u32 %[kx], %[kx], 128\n\t"
        "s_and_b32 %[kx], %[kx], 0x3ff\n\t"
        "s_waitcnt vmcnt(4) lgkmcnt(0)\n\t"
        "s_barrier\n\t"
        "v_mfma_f32_32x32x16_bf16 %[a0], v[64:67], v[72:75], %[a0]\n\t"
        "s_add_u32 m0, %[wd], 0\n\t"
        "s_mul_i32 %[st], %[ku], 98304\n\t"
        "s_add_u32 %[st], %[st], %[w0]\n\t"
        "buffer_load_dwordx4 %[wv], %[rw], %[st] offen lds\n\t"
        "s_add_u32 %[ku], %[ku], 1\n\t"
        "s_and_b32 %[ku], %[ku], 15\n\t"
        "ds_read_b128 v[88:91], %[xb] offset:64\n\t"
        "ds_read_b64_tr_b16 v[96:97], %[wb] offset:18432\n\t"
        "ds_read_b64_tr_b16 v[98:99], %[wb] offset:18688\n\t"
        "v_mfma_f32_32x32x16_bf16 %[a0], v[76:79], v[84:87], %[a0]\n\t"
        "ds_read_b128 v[100:103], %[xb] offset:96\n\t"
        "ds_read_b64_tr_b16 v[108:109], %[wb] offset:19456\n\t"
        "ds_read_b64_tr_b16 v[110:111], %[wb] offset:19712\n\t"
        "s_waitcnt vmcnt(3) lgkmcnt(0)\n\t"
        "s_barrier\n\t"
        "v_mfma_f32_32x32x16_bf16 %[a0], v[88:91], v[96:99], %[a0]\n\t"
        "s_add_u32 m0, %[wd], 6144\n\t"
        "s_mul_i32 %[st], %[ku], 98304\n\t"
        "s_add_u32 %[st], %[st], %[w0]\n\t"
        "buffer_load_dwordx4 %[wv], %[rw], %[st] offen lds\n\t"
        "s_add_u32 %[ku], %[ku], 1\n\t"
        "s_and_b32 %[ku], %[ku], 15\n\t"
        "ds_read_b128 v[64:67], %[xb] offset:16640\n\t"
        "ds_read_b64_tr_b16 v[72:73], %[wb] offset:24576\n\t"
        "ds_read_b64_tr_b16 v[74:75], %[wb] offset:24832\n\t"
        "v_mfma_f32_32x32x16_bf16 %[a0], v[100:103], v[108:111], %[a0]\n\t"
        "ds_read_b128 v[76:79], %[xb] offset:16672\n\t"
        "ds_read_b64_tr_b16 v[84:85], %[wb] offset:25600\n\t"
        "ds_read_b64_tr_b16 v[86:87], %[wb] offset:25856\n\t"
        "s_add_u32 m0, %[xd], 49920\n\t"
        "s_add_u32 %[st], %[xs], %[kx]\n\t"
        "buffer_load_dwordx4 %[xv], %[rx], %[st] offen lds\n\t"
        "s_add_u32 %[kx], %[kx], 128\n\t"
        "s_and_b32 %[kx], %[kx], 0x3ff\n\t"
        "s_waitcnt vmcnt(4) lgkmcnt(0)\n\t"
        "s_barrier\n\t"
        "v_mfma_f32_32x32x16_bf16 %[a0], v[64:67], v[72:75], %[a0]\n\t"
        "s_add_u32 m0, %[wd], 12288\n\t"
        "s_mul_i32 %[st], %[ku], 98304\n\t"
        "s_add_u32 %[st], %[st], %[w0]\n\t"
        "buffer_load_dwordx4 %[wv], %[rw], %[st] offen lds\n\t"
        "s_add_u32 %[ku], %[ku], 1\n\t"
        "s_and_b32 %[ku], %[ku], 15\n\t"
        "ds_read_b128 v[88:91], %[xb] offset:16704\n\t"
        "ds_read_b64_tr_b16 v[96:97], %[wb] offset:30720\n\t"
        "ds_read_b64_tr_b16 v[98:99], %[wb] offset:30976\n\t"
        "v_mfma_f32_32x32x16_bf16 %[a0], v[76:79], v[84:87], %[a0]\n\t"
        "ds_read_b128 v[100:103], %[xb] offset:16736\n\t"
        "ds_read_b64_tr_b16 v[108:109], %[wb] offset:31744\n\t"
        "ds_read_b64_tr_b16 v[110:111], %[wb] offset:32000\n\t"
        "s_waitcnt vmcnt(3) lgkmcnt(0)\n\t"
        "s_barrier\n\t"
        "v_mfma_f32_32x32x16_bf16 %[a0], v[88:91], v[96:99], %[a0]\n\t"
        "s_add_u32 m0, %[wd], 18432\n\t"
        "s_mul_i32 %[st], %[ku], 98304\n\t"
        "s_add_u32 %[st], %[st], %[w0]\n\t"
        "buffer_load_dwordx4 %[wv], %[rw], %[st] offen lds\n\t"
        "s_add_u32 %[ku], %[ku], 1\n\t"
        "s_and_b32 %[ku], %[ku], 15\n\t"
        "ds_read_b128 v[64:67], %[xb] offset:33280\n\t"
        "ds_read_b64_tr_b16 v[72:73], %[wb] offset:0\n\t"
        "ds_read_b64_tr_b16 v[74:75], %[wb] offset:256\n\t"
        "v_mfma_f32_32x32x16_bf16 %[a0], v[100:103], v[108:111], %[a0]\n\t"
        "ds_read_b128 v[76:79], %[xb] offset:33312\n\t"
        "ds_read_b64_tr_b16 v[84:85], %[wb] offset:1024\n\t"
        "ds_read_b64_tr_b16 v[86:87], %[wb] offset:1280\n\t"
        "s_waitcnt vmcnt(3) lgkmcnt(0)\n\t"
        "s_barrier\n\t"
        "v_mfma_f32_32x32x16_bf16 %[a0], v[64:67], v[72:75], %[a0]\n\t"
        "ds_read_b128 v[88:91], %[xb] offset:33344\n\t"
        "ds_read_b64_tr_b16 v[96:97], %[wb] offset:6144\n\t"
        "ds_read_b64_tr_b16 v[98:99], %[wb] offset:6400\n\t"
        "v_mfma_f32_32x32x16_bf16 %[a0], v[76:79], v[84:87], %[a0]\n\t"
        "ds_read_b128 v[100:103], %[xb] offset:33376\n\t"
        "ds_read_b64_tr_b16 v[108:109], %[wb] offset:7168\n\t"
        "ds_read_b64_tr_b16 v[110:111], %[wb] offset:7424\n\t"
        "s_waitcnt vmcnt(1) lgkmcnt(0)\n\t"
        "s_barrier\n\t"
        "v_mfma_f32_32x32x16_bf16 %[a0], v[88:91], v[96:99], %[a0]\n\t"
        "ds_read_b128 v[64:67], %[xb] offset:49920\n\t"
        "ds_read_b64_tr_b16 v[72:73], %[wb] offset:12288\n\t"
        "ds_read_b64_tr_b16 v[74:75], %[wb] offset:12544\n\t"
        "v_mfma_f32_32x32x16_bf16 %[a0], v[100:103], v[108:111], %[a0]\n\t"
        "ds_read_b128 v[76:79], %[xb] offset:49952\n\t"
        "ds_read_b64_tr_b16 v[84:85], %[wb] offset:13312\n\t"
        "ds_read_b64_tr_b16 v[86:87], %[wb] offset:13568\n\t"
        "s_waitcnt vmcnt(0) lgkmcnt(0)\n\t"
        "s_barrier\n\t"
        "v_mfma_f32_32x32x16_bf16 %[a0], v[64:67], v[72:75], %[a0]\n\t"
        "ds_read_b128 v[88:91], %[xb] offset:49984\n\t"
        "ds_read_b64_tr_b16 v[96:97], %[wb] offset:18432\n\t"
        "ds_read_b64_tr_b16 v[98:99], %[wb] offset:18688\n\t"
        "v_mfma_f32_32x32x16_bf16 %[a0], v[76:79], v[84:87], %[a0]\n\t"
        "ds_read_b128 v[100:103], %[xb] offset:50016\n\t"
        "ds_read_b64_tr_b16 v[108:109], %[wb] offset:19456\n\t"
        "ds_read_b64_tr_b16 v[110:111], %[wb] offset:19712\n\t"
        "s_waitcnt vmcnt(0) lgkmcnt(0)\n\t"
        "s_barrier\n\t"
        "v_mfma_f32_32x32x16_bf16 %[a0], v[88:91], v[96:99], %[a0]\n\t"
        "v_mfma_f32_32x32x16_bf16 %[a0], v[100:103], v[108:111], %[a0]\n\t"
        "s_mov_b32 m0, %[sv]\n\t"
        "s_nop 10\n\t"
                : [a0] "=&v"(a0), [sv] "=&s"(sv), [st] "=&s"(st), [kx] "+s"(sg.kx), [ku] "+s"(sg.ku)
                : [xv] "v"(sg.xv), [wv] "v"(sg.wv), [rx] "s"(sg.rx), [rw] "s"(sg.rw), [xs] "s"(sg.xs), [xd] "s"(sg.xd), [wd] "s"(sg.wd), [w0] "s"(sg.w0),
                  [xb] "v"(xb + tv * 256), [wb] "v"(wb + 4096 + ch * 32)
                : "memory", "scc", "v64", "v65", "v66", "v67", "v68", "v69", "v70", "v71", "v72", "v73", "v74", "v75", "v76", "v77", "v78", "v79", "v80", "v81", "v82", "v83", "v84", "v85", "v86", "v87", "v88", "v89", "v90", "v91", "v92", "v93", "v94", "v95", "v96", "v97", "v98", "v99", "v100", "v101", "v102", "v103", "v104", "v105", "v106", "v107", "v108", "v109", "v110", "v111");
            } else {
                float gz0 = 0.f, gz1 = 0.f, gz2 = 0.f, gz3 = 0.f; unsigned epoch;
            asm volatile(
        "s_mov_b32 %[sv], m0\n\t"
        "s_waitcnt vmcnt(3) lgkmcnt(0)\n\t"
        "s_barrier\n\t"
        "s_add_u32 m0, %[wd], 18432\n\t"
        "s_mul_i32 %[st], %[ku], 98304\n\t"
        "s_add_u32 %[st], %[st], %[w0]\n\t"
        "buffer_load_dwordx4 %[wv], %[rw], %[st] offen lds\n\t"
        "s_add_u32 %[ku], %[ku], 1\n\t"
        "s_and_b32 %[ku], %[ku], 15\n\t"
        "ds_read_b128 v[64:67], %[xb] offset:0\n\t"
        "ds_read_b64_tr_b16 v[72:73], %[wb] offset:0\n\t"
        "ds_read_b64_tr_b16 v[74:75], %[wb] offset:256\n\t"
        "ds_read_b128 v[68:71], %[gb] offset:0\n\t"
        "ds_read_b128 v[76:79], %[xb] offset:32\n\t"
        "ds_read_b64_tr_b16 v[84:85], %[wb] offset:1024\n\t"
        "ds_read_b64_tr_b16 v[86:87], %[wb] offset:1280\n\t"
        "ds_read_b128 v[80:83], %[gb] offset:32\n\t"
        "s_add_u32 m0, %[xd], 33280\n\t"
        "s_add_u32 %[st], %[xs], %[kx]\n\t"
        "buffer_load_dwordx4 %[xv], %[rx], %[st] offen lds\n\t"
        "s_add_u32 %[kx], %[kx], 128\n\t"
        "s_and_b32 %[kx], %[kx], 0x3ff\n\t"
        "s_waitcnt lgkmcnt(4)\n\t"
        "v_lshlrev_b32 %[gz2], 16, v64\n\t"
        "v_lshlrev_b32 %[gz3], 16, v68\n\t"
        "v_and_b32 v68, %[msk], v68\n\t"
        "v_fmac_f32 %[gz0], %[gz2], %[gz3]\n\t"
        "v_and_b32 %[gz2], %[msk], v64\n\t"
        "v_fmac_f32 %[gz1], %[gz2], v68\n\t"
        "v_lshlrev_b32 %[gz2], 16, v65\n\t"
        "v_lshlrev_b32 %[gz3], 16, v69\n\t"
        "v_and_b32 v69, %[msk], v69\n\t"
        "v_fmac_f32 %[gz0], %[gz2], %[gz3]\n\t"
        "v_and_b32 %[gz2], %[msk], v65\n\t"
        "v_fmac_f32 %[gz1], %[gz2], v69\n\t"
        "v_lshlrev_b32 %[gz2], 16, v66\n\t"
        "v_lshlrev_b32 %[gz3], 16, v70\n\t"
        "v_and_b32 v70, %[msk], v70\n\t"
        "v_fmac_f32 %[gz0], %[gz2], %[gz3]\n\t"
        "v_and_b32 %[gz2], %[msk], v66\n\t"
        "v_fmac_f32 %[gz1], %[gz2], v70\n\t"
        "v_lshlrev_b32 %[gz2], 16, v67\n\t"
        "v_lshlrev_b32 %[gz3], 16, v71\n\t"
        "v_and_b32 v71, %[msk], v71\n\t"
        "v_fmac_f32 %[gz0], %[gz2], %[gz3]\n\t"
        "v_and_b32 %[gz2], %[msk], v67\n\t"
        "v_fmac_f32 %[gz1], %[gz2], v71\n\t"
        "s_waitcnt lgkmcnt(0)\n\t"
        "v_lshlrev_b32 %[gz2], 16, v76\n\t"
        "v_lshlrev_b32 %[gz3], 16, v80\n\t"
        "v_and_b32 v80, %[msk], v80\n\t"
        "v_fmac_f32 %[gz0], %[gz2], %[gz3]\n\t"
        "v_and_b32 %[gz2], %[msk], v76\n\t"
        "v_fmac_f32 %[gz1], %[gz2], v80\n\t"
        "v_lshlrev_b32 %[gz2], 16, v77\n\t"
        "v_lshlrev_b32 %[gz3], 16, v81\n\t"
        "v_and_b32 v81, %[msk], v81\n\t"
        "v_fmac_f32 %[gz0], %[gz2], %[gz3]\n\t"
        "v_and_b32 %[gz2], %[msk], v77\n\t"
        "v_fmac_f32 %[gz1], %[gz2], v81\n\t"
        "v_lshlrev_b32 %[gz2], 16, v78\n\t"
        "v_lshlrev_b32 %[gz3], 16, v82\n\t"
        "v_and_b32 v82, %[msk], v82\n\t"
        "v_fmac_f32 %[gz0], %[gz2], %[gz3]\n\t"
        "v_and_b32 %[gz2], %[msk], v78\n\t"
        "v_fmac_f32 %[gz1], %[gz2], v82\n\t"
        "v_lshlrev_b32 %[gz2], 16, v79\n\t"
        "v_lshlrev_b32 %[gz3], 16, v83\n\t"
        "v_and_b32 v83, %[msk], v83\n\t"
        "v_fmac_f32 %[gz0], %[gz2], %[gz3]\n\t"
        "v_and_b32 %[gz2], %[msk], v79\n\t"
        "v_fmac_f32 %[gz1], %[gz2], v83\n\t"
        "s_waitcnt vmcnt(4) lgkmcnt(0)\n\t"
        "s_barrier\n\t"
        "v_mfma_f32_32x32x16_bf16 %[a0], v[64:67], v[72:75], 0\n\t"
        "s_add_u32 m0, %[wd], 24576\n\t"
        "s_mul_i32 %[st], %[ku], 98304\n\t"
        "s_add_u32 %[st], %[st], %[w0]\n\t"
        "buffer_load_dwordx4 %[wv], %[rw], %[st] offen lds\n\t"
        "s_add_u32 %[ku], %[ku], 1\n\t"
        "s_and_b32 %[ku], %[ku], 15\n\t"
        "ds_read_b128 v[88:91], %[xb] offset:64\n\t"
        "ds_read_b64_tr_b16 v[96:97], %[wb] offset:6144\n\t"
        "ds_read_b64_tr_b16 v[98:99], %[wb] offset:6400\n\t"
        "ds_read_b128 v[92:95], %[gb] offset:64\n\t"
        "v_mfma_f32_32x32x16_bf16 %[a0], v[76:79], v[84:87], %[a0]\n\t"
        "ds_read_b128 v[100:103], %[xb] offset:96\n\t"
        "ds_read_b64_tr_b16 v[108:109], %[wb] offset:7168\n\t"
        "ds_read_b64_tr_b16 v[110:111], %[wb] offset:7424\n\t"
        "ds_read_b128 v[104:107], %[gb] offset:96\n\t"
        "s_waitcnt lgkmcnt(4)\n\t"
        "v_lshlrev_b32 %[gz2], 16, v88\n\t"
        "v_lshlrev_b32 %[gz3], 16, v92\n\t"
        "v_and_b32 v92, %[msk], v92\n\t"
        "v_fmac_f32 %[gz0], %[gz2], %[gz3]\n\t"
        "v_and_b32 %[gz2], %[msk], v88\n\t"
        "v_fmac_f32 %[gz1], %[gz2], v92\n\t"
        "v_lshlrev_b32 %[gz2], 16, v89\n\t"
        "v_lshlrev_b32 %[gz3], 16, v93\n\t"
        "v_and_b32 v93, %[msk], v93\n\t"
        "v_fmac_f32 %[gz0], %[gz2], %[gz3]\n\t"
        "v_and_b32 %[gz2], %[msk], v89\n\t"
        "v_fmac_f32 %[gz1], %[gz2], v93\n\t"
        "v_lshlrev_b32 %[gz2], 16, v90\n\t"
        "v_lshlrev_b32 %[gz3], 16, v94\n\t"
        "v_and_b32 v94, %[msk], v94\n\t"
        "v_fmac_f32 %[gz0], %[gz2], %[gz3]\n\t"
        "v_and_b32 %[gz2], %[msk], v90\n\t"
        "v_fmac_f32 %[gz1], %[gz2], v94\n\t"
        "v_lshlrev_b32 %[gz2], 16, v91\n\t"
        "v_lshlrev_b32 %[gz3], 16, v95\n\t"
        "v_and_b32 v95, %[msk], v95\n\t"
        "v_fmac_f32 %[gz0], %[gz2], %[gz3]\n\t"
        "v_and_b32 %[gz2], %[msk], v91\n\t"
        "v_fmac_f32 %[gz1], %[gz2], v95\n\t"
        "s_waitcnt lgkmcnt(0)\n\t"
        "v_lshlrev_b32 %[gz2], 16, v100\n\t"
        "v_lshlrev_b32 %[gz3], 16, v104\n\t"
        "v_and_b32 v104, %[msk], v104\n\t"
        "v_fmac_f32 %[gz0], %[gz2], %[gz3]\n\t"
        "v_and_b32 %[gz2], %[msk], v100\n\t"
        "v_fmac_f32 %[gz1], %[gz2], v104\n\t"
        "v_lshlrev_b32 %[gz2], 16, v101\n\t"
        "v_lshlrev_b32 %[gz3], 16, v105\n\t"
        "v_and_b32 v105, %[msk], v105\n\t"
        "v_fmac_f32 %[gz0], %[gz2], %[gz3]\n\t"
        "v_and_b32 %[gz2], %[msk], v101\n\t"
        "v_fmac_f32 %[gz1], %[gz2], v105\n\t"
        "v_lshlrev_b32 %[gz2], 16, v102\n\t"
        "v_lshlrev_b32 %[gz3], 16, v106\n\t"
        "v_and_b32 v106, %[msk], v106\n\t"
        "v_fmac_f32 %[gz0], %[gz2], %[gz3]\n\t"
        "v_and_b32 %[gz2], %[msk], v102\n\t"
        "v_fmac_f32 %[gz1], %[gz2], v106\n\t"
        "v_lshlrev_b32 %[gz2], 16, v103\n\t"
        "v_lshlrev_b32 %[gz3], 16, v107\n\t"
        "v_and_b32 v107, %[msk], v107\n\t"
        "v_fmac_f32 %[gz0], %[gz2], %[gz3]\n\t"
        "v_and_b32 %[gz2], %[msk], v103\n\t"
        "v_fmac_f32 %[gz1], %[gz2], v107\n\t"
        "s_waitcnt vmcnt(3) lgkmcnt(0)\n\t"
        "s_barrier\n\t"
        "v_mfma_f32_32x32x16_bf16 %[a0], v[88:91], v[96:99], %[a0]\n\t"
        "s_add_u32 m0, %[wd], 30720\n\t"
        "s_mul_i32 %[st], %[ku], 98304\n\t"
        "s_add_u32 %[st], %[st], %[w0]\n\t"
        "buffer_load_dwordx4 %[wv], %[rw], %[st] offen lds\n\t"
        "s_add_u32 %[ku], %[ku], 1\n\t"
        "s_and_b32 %[ku], %[ku], 15\n\t"
        "ds_read_b128 v[64:67], %[xb] offset:16640\n\t"
        "ds_read_b64_tr_b16 v[72:73], %[wb] offset:12288\n\t"
        "ds_read_b64_tr_b16 v[74:75], %[wb] offset:12544\n\t"
        "ds_read_b128 v[68:71], %[gb] offset:128\n\t"
        "v_mfma_f32_32x32x16_bf16 %[a0], v[100:103], v[108:111], %[a0]\n\t"
        "ds_read_b128 v[76:79], %[xb] offset:16672\n\t"
        "ds_read_b64_tr_b16 v[84:85], %[wb] offset:13312\n\t"
        "ds_read_b64_tr_b16 v[86:87], %[wb] offset:13568\n\t"
        "ds_read_b128 v[80:83], %[gb] offset:160\n\t"
        "s_add_u32 m0, %[xd], 49920\n\t"
        "s_add_u32 %[st], %[xs], %[kx]\n\t"
        "buffer_load_dwordx4 %[xv], %[rx], %[st] offen lds\n\t"
        "s_add_u32 %[kx], %[kx], 128\n\t"
        "s_and_b32 %[kx], %[kx], 0x3ff\n\t"
        "s_waitcnt lgkmcnt(4)\n\t"
        "v_lshlrev_b32 %[gz2], 16, v64\n\t"
        "v_lshlrev_b32 %[gz3], 16, v68\n\t"
        "v_and_b32 v68, %[msk], v68\n\t"
        "v_fmac_f32 %[gz0], %[gz2], %[gz3]\n\t"
        "v_and_b32 %[gz2], %[msk], v64\n\t"
        "v_fmac_f32 %[gz1], %[gz2], v68\n\t"
        "v_lshlrev_b32 %[gz2], 16, v65\n\t"
        "v_lshlrev_b32 %[gz3], 16, v69\n\t"
        "v_and_b32 v69, %[msk], v69\n\t"
        "v_fmac_f32 %[gz0], %[gz2], %[gz3]\n\t"
        "v_and_b32 %[gz2], %[msk], v65\n\t"
        "v_fmac_f32 %[gz1], %[gz2], v69\n\t"
        "v_lshlrev_b32 %[gz2], 16, v66\n\t"
        "v_lshlrev_b32 %[gz3], 16, v70\n\t"
        "v_and_b32 v70, %[msk], v70\n\t"
        "v_fmac_f32 %[gz0], %[gz2], %[gz3]\n\t"
        "v_and_b32 %[gz2], %[msk], v66\n\t"
        "v_fmac_f32 %[gz1], %[gz2], v70\n\t"
        "v_lshlrev_b32 %[gz2], 16, v67\n\t"
        "v_lshlrev_b32 %[gz3], 16, v71\n\t"
        "v_and_b32 v71, %[msk], v71\n\t"
        "v_fmac_f32 %[gz0], %[gz2], %[gz3]\n\t"
        "v_and_b32 %[gz2], %[msk], v67\n\t"
        "v_fmac_f32 %[gz1], %[gz2], v71\n\t"
        "s_waitcnt lgkmcnt(0)\n\t"
        "v_lshlrev_b32 %[gz2], 16, v76\n\t"
        "v_lshlrev_b32 %[gz3], 16, v80\n\t"
        "v_and_b32 v80, %[msk], v80\n\t"
        "v_fmac_f32 %[gz0], %[gz2], %[gz3]\n\t"
        "v_and_b32 %[gz2], %[msk], v76\n\t"
        "v_fmac_f32 %[gz1], %[gz2], v80\n\t"
        "v_lshlrev_b32 %[gz2], 16, v77\n\t"
        "v_lshlrev_b32 %[gz3], 16, v81\n\t"
        "v_and_b32 v81, %[msk], v81\n\t"
        "v_fmac_f32 %[gz0], %[gz2], %[gz3]\n\t"
        "v_and_b32 %[gz2], %[msk], v77\n\t"
        "v_fmac_f32 %[gz1], %[gz2], v81\n\t"
        "v_lshlrev_b32 %[gz2], 16, v78\n\t"
        "v_lshlrev_b32 %[gz3], 16, v82\n\t"
        "v_and_b32 v82, %[msk], v82\n\t"
        "v_fmac_f32 %[gz0], %[gz2], %[gz3]\n\t"
        "v_and_b32 %[gz2], %[msk], v78\n\t"
        "v_fmac_f32 %[gz1], %[gz2], v82\n\t"
        "v_lshlrev_b32 %[gz2], 16, v79\n\t"
        "v_lshlrev_b32 %[gz3], 16, v83\n\t"
        "v_and_b32 v83, %[msk], v83\n\t"
        "v_fmac_f32 %[gz0], %[gz2], %[gz3]\n\t"
        "v_and_b32 %[gz2], %[msk], v79\n\t"
        "v_fmac_f32 %[gz1], %[gz2], v83\n\t"
        "s_waitcnt vmcnt(4) lgkmcnt(0)\n\t"
        "s_barrier\n\t"
        "v_mfma_f32_32x32x16_bf16 %[a0], v[64:67], v[72:75], %[a0]\n\t"
        "s_add_u32 m0, %[wd], 0\n\t"
        "s_mul_i32 %[st], %[ku], 98304\n\t"
        "s_add_u32 %[st], %[st], %[w0]\n\t"
        "buffer_load_dwordx4 %[wv], %[rw], %[st] offen lds\n\t"
        "s_add_u32 %[ku], %[ku], 1\n\t"
        "s_and_b32 %[ku], %[ku], 15\n\t"
        "buffer_load_dword %[ep], off, %[re], 0 sc1\n\t"
        "ds_read_b128 v[88:91], %[xb] offset:16704\n\t"
        "ds_read_b64_tr_b16 v[96:97], %[wb] offset:18432\n\t"
        "ds_read_b64_tr_b16 v[98:99], %[wb] offset:18688\n\t"
        "ds_read_b128 v[92:95], %[gb] offset:192\n\t"
        "v_mfma_f32_32x32x16_bf16 %[a0], v[76:79], v[84:87], %[a0]\n\t"
        "ds_read_b128 v[100:103], %[xb] offset:16736\n\t"
        "ds_read_b64_tr_b16 v[108:109], %[wb] offset:19456\n\t"
        "ds_read_b64_tr_b16 v[110:111], %[wb] offset:19712\n\t"
        "ds_read_b128 v[104:107], %[gb] offset:224\n\t"
        "s_waitcnt lgkmcnt(4)\n\t"
        "v_lshlrev_b32 %[gz2], 16, v88\n\t"
        "v_lshlrev_b32 %[gz3], 16, v92\n\t"
        "v_and_b32 v92, %[msk], v92\n\t"
        "v_fmac_f32 %[gz0], %[gz2], %[gz3]\n\t"
        "v_and_b32 %[gz2], %[msk], v88\n\t"
        "v_fmac_f32 %[gz1], %[gz2], v92\n\t"
        "v_lshlrev_b32 %[gz2], 16, v89\n\t"
        "v_lshlrev_b32 %[gz3], 16, v93\n\t"
        "v_and_b32 v93, %[msk], v93\n\t"
        "v_fmac_f32 %[gz0], %[gz2], %[gz3]\n\t"
        "v_and_b32 %[gz2], %[msk], v89\n\t"
        "v_fmac_f32 %[gz1], %[gz2], v93\n\t"
        "v_lshlrev_b32 %[gz2], 16, v90\n\t"
        "v_lshlrev_b32 %[gz3], 16, v94\n\t"
        "v_and_b32 v94, %[msk], v94\n\t"
        "v_fmac_f32 %[gz0], %[gz2], %[gz3]\n\t"
        "v_and_b32 %[gz2], %[msk], v90\n\t"
        "v_fmac_f32 %[gz1], %[gz2], v94\n\t"
        "v_lshlrev_b32 %[gz2], 16, v91\n\t"
        "v_lshlrev_b32 %[gz3], 16, v95\n\t"
        "v_and_b32 v95, %[msk], v95\n\t"
        "v_fmac_f32 %[gz0], %[gz2], %[gz3]\n\t"
        "v_and_b32 %[gz2], %[msk], v91\n\t"
        "v_fmac_f32 %[gz1], %[gz2], v95\n\t"
        "s_waitcnt lgkmcnt(0)\n\t"
        "v_lshlrev_b32 %[gz2], 16, v100\n\t"
        "v_lshlrev_b32 %[gz3], 16, v104\n\t"
        "v_and_b32 v104, %[msk], v104\n\t"
        "v_fmac_f32 %[gz0], %[gz2], %[gz3]\n\t"
        "v_and_b32 %[gz2], %[msk], v100\n\t"
        "v_fmac_f32 %[gz1], %[gz2], v104\n\t"
        "v_lshlrev_b32 %[gz2], 16, v101\n\t"
        "v_lshlrev_b32 %[gz3], 16, v105\n\t"
        "v_and_b32 v105, %[msk], v105\n\t"
        "v_fmac_f32 %[gz0], %[gz2], %[gz3]\n\t"
        "v_and_b32 %[gz2], %[msk], v101\n\t"
        "v_fmac_f32 %[gz1], %[gz2], v105\n\t"
        "v_lshlrev_b32 %[gz2], 16, v102\n\t"
        "v_lshlrev_b32 %[gz3], 16, v106\n\t"
        "v_and_b32 v106, %[msk], v106\n\t"
        "v_fmac_f32 %[gz0], %[gz2], %[gz3]\n\t"
        "v_and_b32 %[gz2], %[msk], v102\n\t"
        "v_fmac_f32 %[gz1], %[gz2], v106\n\t"
        "v_lshlrev_b32 %[gz2], 16, v103\n\t"
        "v_lshlrev_b32 %[gz3], 16, v107\n\t"
        "v_and_b32 v107, %[msk], v107\n\t"
        "v_fmac_f32 %[gz0], %[gz2], %[gz3]\n\t"
        "v_and_b32 %[gz2], %[msk], v103\n\t"
        "v_fmac_f32 %[gz1], %[gz2], v107\n\t"
        "s_waitcnt vmcnt(4) lgkmcnt(0)\n\t"
        "s_barrier\n\t"
        "v_mfma_f32_32x32x16_bf16 %[a0], v[88:91], v[96:99], %[a0]\n\t"
        "s_add_u32 m0, %[wd], 6144\n\t"
        "s_mul_i32 %[st], %[ku], 98304\n\t"
        "s_add_u32 %[st], %[st], %[w0]\n\t"
        "buffer_load_dwordx4 %[wv], %[rw], %[st] offen lds\n\t"
        "s_add_u32 %[ku], %[ku], 1\n\t"
        "s_and_b32 %[ku], %[ku], 15\n\t"
        "ds_read_b128 v[64:67], %[xb] offset:33280\n\t"
        "ds_read_b64_tr_b16 v[72:73], %[wb] offset:24576\n\t"
        "ds_read_b64_tr_b16 v[74:75], %[wb] offset:24832\n\t"
        "ds_read_b128 v[68:71], %[gb] offset:256\n\t"
        "v_mfma_f32_32x32x16_bf16 %[a0], v[100:103], v[108:111], %[a0]\n\t"
        "ds_read_b128 v[76:79], %[xb] offset:33312\n\t"
        "ds_read_b64_tr_b16 v[84:85], %[wb] offset:25600\n\t"
        "ds_read_b64_tr_b16 v[86:87], %[wb] offset:25856\n\t"
        "ds_read_b128 v[80:83], %[gb] offset:288\n\t"
        "s_add_u32 m0, %[xd], 0\n\t"
        "s_add_u32 %[st], %[xs], %[kx]\n\t"
        "buffer_load_dwordx4 %[xv], %[rx], %[st] offen lds\n\t"
        "s_add_u32 %[kx], %[kx], 128\n\t"
        "s_and_b32 %[kx], %[kx], 0x3ff\n\t"
        "s_waitcnt lgkmcnt(4)\n\t"
        "v_lshlrev_b32 %[gz2], 16, v64\n\t"
        "v_lshlrev_b32 %[gz3], 16, v68\n\t"
        "v_and_b32 v68, %[msk], v68\n\t"
        "v_fmac_f32 %[gz0], %[gz2], %[gz3]\n\t"
        "v_and_b32 %[gz2], %[msk], v64\n\t"
        "v_fmac_f32 %[gz1], %[gz2], v68\n\t"
        "v_lshlrev_b32 %[gz2], 16, v65\n\t"
        "v_lshlrev_b32 %[gz3], 16, v69\n\t"
        "v_and_b32 v69, %[msk], v69\n\t"
        "v_fmac_f32 %[gz0], %[gz2], %[gz3]\n\t"
        "v_and_b32 %[gz2], %[msk], v65\n\t"
        "v_fmac_f32 %[gz1], %[gz2], v69\n\t"
        "v_lshlrev_b32 %[gz2], 16, v66\n\t"
        "v_lshlrev_b32 %[gz3], 16, v70\n\t"
        "v_and_b32 v70, %[msk], v70\n\t"
        "v_fmac_f32 %[gz0], %[gz2], %[gz3]\n\t"
        "v_and_b32 %[gz2], %[msk], v66\n\t"
        "v_fmac_f32 %[gz1], %[gz2], v70\n\t"
        "v_lshlrev_b32 %[gz2], 16, v67\n\t"
        "v_lshlrev_b32 %[gz3], 16, v71\n\t"
        "v_and_b32 v71, %[msk], v71\n\t"
        "v_fmac_f32 %[gz0], %[gz2], %[gz3]\n\t"
        "v_and_b32 %[gz2], %[msk], v67\n\t"
        "v_fmac_f32 %[gz1], %[gz2], v71\n\t"
        "s_waitcnt lgkmcnt(0)\n\t"
        "v_lshlrev_b32 %[gz2], 16, v76\n\t"
        "v_lshlrev_b32 %[gz3], 16, v80\n\t"
        "v_and_b32 v80, %[msk], v80\n\t"
        "v_fmac_f32 %[gz0], %[gz2], %[gz3]\n\t"
        "v_and_b32 %[gz2], %[msk], v76\n\t"
        "v_fmac_f32 %[gz1], %[gz2], v80\n\t"
        "v_lshlrev_b32 %[gz2], 16, v77\n\t"
        "v_lshlrev_b32 %[gz3], 16, v81\n\t"
        "v_and_b32 v81, %[msk], v81\n\t"
        "v_fmac_f32 %[gz0], %[gz2], %[gz3]\n\t"
        "v_and_b32 %[gz2], %[msk], v77\n\t"
        "v_fmac_f32 %[gz1], %[gz2], v81\n\t"
        "v_lshlrev_b32 %[gz2], 16, v78\n\t"
        "v_lshlrev_b32 %[gz3], 16, v82\n\t"
        "v_and_b32 v82, %[msk], v82\n\t"
        "v_fmac_f32 %[gz0], %[gz2], %[gz3]\n\t"
        "v_and_b32 %[gz2], %[msk], v78\n\t"
        "v_fmac_f32 %[gz1], %[gz2], v82\n\t"
        "v_lshlrev_b32 %[gz2], 16, v79\n\t"
        "v_lshlrev_b32 %[gz3], 16, v83\n\t"
        "v_and_b32 v83, %[msk], v83\n\t"
        "v_fmac_f32 %[gz0], %[gz2], %[gz3]\n\t"
        "v_and_b32 %[gz2], %[msk], v79\n\t"
        "v_fmac_f32 %[gz1], %[gz2], v83\n\t"
        "s_waitcnt vmcnt(5) lgkmcnt(0)\n\t"
        "s_barrier\n\t"
        "v_mfma_f32_32x32x16_bf16 %[a0], v[64:67], v[72:75], %[a0]\n\t"
        "s_add_u32 m0, %[wd], 12288\n\t"
        "s_mul_i32 %[st], %[ku], 98304\n\t"
        "s_add_u32 %[st], %[st], %[w0]\n\t"
        "buffer_load_dwordx4 %[wv], %[rw], %[st] offen lds\n\t"
        "s_add_u32 %[ku], %[ku], 1\n\t"
        "s_and_b32 %[ku], %[ku], 15\n\t"
        "ds_read_b128 v[88:91], %[xb] offset:33344\n\t"
        "ds_read_b64_tr_b16 v[96:97], %[wb] offset:30720\n\t"
        "ds_read_b64_tr_b16 v[98:99], %[wb] offset:30976\n\t"
        "ds_read_b128 v[92:95], %[gb] offset:320\n\t"
        "v_mfma_f32_32x32x16_bf16 %[a0], v[76:79], v[84:87], %[a0]\n\t"
        "ds_read_b128 v[100:103], %[xb] offset:33376\n\t"
        "ds_read_b64_tr_b16 v[108:109], %[wb] offset:31744\n\t"
        "ds_read_b64_tr_b16 v[110:111], %[wb] offset:32000\n\t"
        "ds_read_b128 v[104:107], %[gb] offset:352\n\t"
        "s_waitcnt lgkmcnt(4)\n\t"
        "v_lshlrev_b32 %[gz2], 16, v88\n\t"
        "v_lshlrev_b32 %[gz3], 16, v92\n\t"
        "v_and_b32 v92, %[msk], v92\n\t"
        "v_fmac_f32 %[gz0], %[gz2], %[gz3]\n\t"
        "v_and_b32 %[gz2], %[msk], v88\n\t"
        "v_fmac_f32 %[gz1], %[gz2], v92\n\t"
        "v_lshlrev_b32 %[gz2], 16, v89\n\t"
        "v_lshlrev_b32 %[gz3], 16, v93\n\t"
        "v_and_b32 v93, %[msk], v93\n\t"
        "v_fmac_f32 %[gz0], %[gz2], %[gz3]\n\t"
        "v_and_b32 %[gz2], %[msk], v89\n\t"
        "v_fmac_f32 %[gz1], %[gz2], v93\n\t"
        "v_lshlrev_b32 %[gz2], 16, v90\n\t"
        "v_lshlrev_b32 %[gz3], 16, v94\n\t"
        "v_and_b32 v94, %[msk], v94\n\t"
        "v_fmac_f32 %[gz0], %[gz2], %[gz3]\n\t"
        "v_and_b32 %[gz2], %[msk], v90\n\t"
        "v_fmac_f32 %[gz1], %[gz2], v94\n\t"
        "v_lshlrev_b32 %[gz2], 16, v91\n\t"
        "v_lshlrev_b32 %[gz3], 16, v95\n\t"
        "v_and_b32 v95, %[msk], v95\n\t"
        "v_fmac_f32 %[gz0], %[gz2], %[gz3]\n\t"
        "v_and_b32 %[gz2], %[msk], v91\n\t"
        "v_fmac_f32 %[gz1], %[gz2], v95\n\t"
        "s_waitcnt lgkmcnt(0)\n\t"
        "v_lshlrev_b32 %[gz2], 16, v100\n\t"
        "v_lshlrev_b32 %[gz3], 16, v104\n\t"
        "v_and_b32 v104, %[msk], v104\n\t"
        "v_fmac_f32 %[gz0], %[gz2], %[gz3]\n\t"
        "v_and_b32 %[gz2], %[msk], v100\n\t"
        "v_fmac_f32 %[gz1], %[gz2], v104\n\t"
        "v_lshlrev_b32 %[gz2], 16, v101\n\t"
        "v_lshlrev_b32 %[gz3], 16, v105\n\t"
        "v_and_b32 v105, %[msk], v105\n\t"
        "v_fmac_f32 %[gz0], %[gz2], %[gz3]\n\t"
        "v_and_b32 %[gz2], %[msk], v101\n\t"
        "v_fmac_f32 %[gz1], %[gz2], v105\n\t"
        "v_lshlrev_b32 %[gz2], 16, v102\n\t"
        "v_lshlrev_b32 %[gz3], 16, v106\n\t"
        "v_and_b32 v106, %[msk], v106\n\t"
        "v_fmac_f32 %[gz0], %[gz2], %[gz3]\n\t"
        "v_and_b32 %[gz2], %[msk], v102\n\t"
        "v_fmac_f32 %[gz1], %[gz2], v106\n\t"
        "v_lshlrev_b32 %[gz2], 16, v103\n\t"
        "v_lshlrev_b32 %[gz3], 16, v107\n\t"
        "v_and_b32 v107, %[msk], v107\n\t"
        "v_fmac_f32 %[gz0], %[gz2], %[gz3]\n\t"
        "v_and_b32 %[gz2], %[msk], v103\n\t"
        "v_fmac_f32 %[gz1], %[gz2], v107\n\t"
        "s_waitcnt vmcnt(4) lgkmcnt(0)\n\t"
        "s_barrier\n\t"
        "v_mfma_f32_32x32x16_bf16 %[a0], v[88:91], v[96:99], %[a0]\n\t"
        "s_add_u32 m0, %[wd], 18432\n\t"
        "s_mul_i32 %[st], %[ku], 98304\n\t"
        "s_add_u32 %[st], %[st], %[w0]\n\t"
        "buffer_load_dwordx4 %[wv], %[rw], %[st] offen lds\n\t"
        "s_add_u32 %[ku], %[ku], 1\n\t"
        "s_and_b32 %[ku], %[ku], 15\n\t"
        "ds_read_b128 v[64:67], %[xb] offset:49920\n\t"
        "ds_read_b64_tr_b16 v[72:73], %[wb] offset:0\n\t"
        "ds_read_b64_tr_b16 v[74:75], %[wb] offset:256\n\t"
        "ds_read_b128 v[68:71], %[gb] offset:384\n\t"
        "v_mfma_f32_32x32x16_bf16 %[a0], v[100:103], v[108:111], %[a0]\n\t"
        "ds_read_b128 v[76:79], %[xb] offset:49952\n\t"
        "ds_read_b64_tr_b16 v[84:85], %[wb] offset:1024\n\t"
        "ds_read_b64_tr_b16 v[86:87], %[wb] offset:1280\n\t"
        "ds_read_b128 v[80:83], %[gb] offset:416\n\t"
        "s_add_u32 m0, %[xd], 16640\n\t"
        "s_add_u32 %[st], %[xs], %[kx]\n\t"
        "buffer_load_dwordx4 %[xv], %[rx], %[st] offen lds\n\t"
        "s_add_u32 %[kx], %[kx], 128\n\t"
        "s_and_b32 %[kx], %[kx], 0x3ff\n\t"
        "s_waitcnt lgkmcnt(4)\n\t"
        "v_lshlrev_b32 %[gz2], 16, v64\n\t"
        "v_lshlrev_b32 %[gz3], 16, v68\n\t"
        "v_and_b32 v68, %[msk], v68\n\t"
        "v_fmac_f32 %[gz0], %[gz2], %[gz3]\n\t"
        "v_and_b32 %[gz2], %[msk], v64\n\t"
        "v_fmac_f32 %[gz1], %[gz2], v68\n\t"
        "v_lshlrev_b32 %[gz2], 16, v65\n\t"
        "v_lshlrev_b32 %[gz3], 16, v69\n\t"
        "v_and_b32 v69, %[msk], v69\n\t"
        "v_fmac_f32 %[gz0], %[gz2], %[gz3]\n\t"
        "v_and_b32 %[gz2], %[msk], v65\n\t"
        "v_fmac_f32 %[gz1], %[gz2], v69\n\t"
        "v_lshlrev_b32 %[gz2], 16, v66\n\t"
        "v_lshlrev_b32 %[gz3], 16, v70\n\t"
        "v_and_b32 v70, %[msk], v70\n\t"
        "v_fmac_f32 %[gz0], %[gz2], %[gz3]\n\t"
        "v_and_b32 %[gz2], %[msk], v66\n\t"
        "v_fmac_f32 %[gz1], %[gz2], v70\n\t"
        "v_lshlrev_b32 %[gz2], 16, v67\n\t"
        "v_lshlrev_b32 %[gz3], 16, v71\n\t"
        "v_and_b32 v71, %[msk], v71\n\t"
        "v_fmac_f32 %[gz0], %[gz2], %[gz3]\n\t"
        "v_and_b32 %[gz2], %[msk], v67\n\t"
        "v_fmac_f32 %[gz1], %[gz2], v71\n\t"
        "s_waitcnt lgkmcnt(0)\n\t"
        "v_lshlrev_b32 %[gz2], 16, v76\n\t"
        "v_lshlrev_b32 %[gz3], 16, v80\n\t"
        "v_and_b32 v80, %[msk], v80\n\t"
        "v_fmac_f32 %[gz0], %[gz2], %[gz3]\n\t"
        "v_and_b32 %[gz2], %[msk], v76\n\t"
        "v_fmac_f32 %[gz1], %[gz2], v80\n\t"
        "v_lshlrev_b32 %[gz2], 16, v77\n\t"
        "v_lshlrev_b32 %[gz3], 16, v81\n\t"
        "v_and_b32 v81, %[msk], v81\n\t"
        "v_fmac_f32 %[gz0], %[gz2], %[gz3]\n\t"
        "v_and_b32 %[gz2], %[msk], v77\n\t"
        "v_fmac_f32 %[gz1], %[gz2], v81\n\t"
        "v_lshlrev_b32 %[gz2], 16, v78\n\t"
        "v_lshlrev_b32 %[gz3], 16, v82\n\t"
        "v_and_b32 v82, %[msk], v82\n\t"
        "v_fmac_f32 %[gz0], %[gz2], %[gz3]\n\t"
        "v_and_b32 %[gz2], %[msk], v78\n\t"
        "v_fmac_f32 %[gz1], %[gz2], v82\n\t"
        "v_lshlrev_b32 %[gz2], 16, v79\n\t"
        "v_lshlrev_b32 %[gz3], 16, v83\n\t"
        "v_and_b32 v83, %[msk], v83\n\t"
        "v_fmac_f32 %[gz0], %[gz2], %[gz3]\n\t"
        "v_and_b32 %[gz2], %[msk], v79\n\t"
        "v_fmac_f32 %[gz1], %[gz2], v83\n\t"
        "s_waitcnt vmcnt(4) lgkmcnt(0)\n\t"
        "s_barrier\n\t"
        "v_mfma_f32_32x32x16_bf16 %[a0], v[64:67], v[72:75], %[a0]\n\t"
        "s_add_u32 m0, %[wd], 24576\n\t"
        "s_mul_i32 %[st], %[ku], 98304\n\t"
        "s_add_u32 %[st], %[st], %[w0]\n\t"
        "buffer_load_dwordx4 %[wv], %[rw], %[st] offen lds\n\t"
        "s_add_u32 %[ku], %[ku], 1\n\t"
        "s_and_b32 %[ku], %[ku], 15\n\t"
        "ds_read_b128 v[88:91], %[xb] offset:49984\n\t"
        "ds_read_b64_tr_b16 v[96:97], %[wb] offset:6144\n\t"
        "ds_read_b64_tr_b16 v[98:99], %[wb] offset:6400\n\t"
        "ds_read_b128 v[92:95], %[gb] offset:448\n\t"
        "v_mfma_f32_32x32x16_bf16 %[a0], v[76:79], v[84:87], %[a0]\n\t"
        "ds_read_b128 v[100:103], %[xb] offset:50016\n\t"
        "ds_read_b64_tr_b16 v[108:109], %[wb] offset:7168\n\t"
        "ds_read_b64_tr_b16 v[110:111], %[wb] offset:7424\n\t"
        "ds_read_b128 v[104:107], %[gb] offset:480\n\t"
        "s_waitcnt lgkmcnt(4)\n\t"
        "v_lshlrev_b32 %[gz2], 16, v88\n\t"
        "v_lshlrev_b32 %[gz3], 16, v92\n\t"
        "v_and_b32 v92, %[msk], v92\n\t"
        "v_fmac_f32 %[gz0], %[gz2], %[gz3]\n\t"
        "v_and_b32 %[gz2], %[msk], v88\n\t"
        "v_fmac_f32 %[gz1], %[gz2], v92\n\t"
        "v_lshlrev_b32 %[gz2], 16, v89\n\t"
        "v_lshlrev_b32 %[gz3], 16, v93\n\t"
        "v_and_b32 v93, %[msk], v93\n\t"
        "v_fmac_f32 %[gz0], %[gz2], %[gz3]\n\t"
        "v_and_b32 %[gz2], %[msk], v89\n\t"
        "v_fmac_f32 %[gz1], %[gz2], v93\n\t"
        "v_lshlrev_b32 %[gz2], 16, v90\n\t"
        "v_lshlrev_b32 %[gz3], 16, v94\n\t"
        "v_and_b32 v94, %[msk], v94\n\t"
        "v_fmac_f32 %[gz0], %[gz2], %[gz3]\n\t"
        "v_and_b32 %[gz2], %[msk], v90\n\t"
        "v_fmac_f32 %[gz1], %[gz2], v94\n\t"
        "v_lshlrev_b32 %[gz2], 16, v91\n\t"
        "v_lshlrev_b32 %[gz3], 16, v95\n\t"
        "v_and_b32 v95, %[msk], v95\n\t"
        "v_fmac_f32 %[gz0], %[gz2], %[gz3]\n\t"
        "v_and_b32 %[gz2], %[msk], v91\n\t"
        "v_fmac_f32 %[gz1], %[gz2], v95\n\t"
        "s_waitcnt lgkmcnt(0)\n\t"
        "v_lshlrev_b32 %[gz2], 16, v100\n\t"
        "v_lshlrev_b32 %[gz3], 16, v104\n\t"
        "v_and_b32 v104, %[msk], v104\n\t"
        "v_fmac_f32 %[gz0], %[gz2], %[gz3]\n\t"
        "v_and_b32 %[gz2], %[msk], v100\n\t"
        "v_fmac_f32 %[gz1], %[gz2], v104\n\t"
        "v_lshlrev_b32 %[gz2], 16, v101\n\t"
        "v_lshlrev_b32 %[gz3], 16, v105\n\t"
        "v_and_b32 v105, %[msk], v105\n\t"
        "v_fmac_f32 %[gz0], %[gz2], %[gz3]\n\t"
        "v_and_b32 %[gz2], %[msk], v101\n\t"
        "v_fmac_f32 %[gz1], %[gz2], v105\n\t"
        "v_lshlrev_b32 %[gz2], 16, v102\n\t"
        "v_lshlrev_b32 %[gz3], 16, v106\n\t"
        "v_and_b32 v106, %[msk], v106\n\t"
        "v_fmac_f32 %[gz0], %[gz2], %[gz3]\n\t"
        "v_and_b32 %[gz2], %[msk], v102\n\t"
        "v_fmac_f32 %[gz1], %[gz2], v106\n\t"
        "v_lshlrev_b32 %[gz2], 16, v103\n\t"
        "v_lshlrev_b32 %[gz3], 16, v107\n\t"
        "v_and_b32 v107, %[msk], v107\n\t"
        "v_fmac_f32 %[gz0], %[gz2], %[gz3]\n\t"
        "v_and_b32 %[gz2], %[msk], v103\n\t"
        "v_fmac_f32 %[gz1], %[gz2], v107\n\t"
        "s_waitcnt vmcnt(3) lgkmcnt(0)\n\t"
        "s_barrier\n\t"
        "v_mfma_f32_32x32x16_bf16 %[a0], v[88:91], v[96:99], %[a0]\n\t"
        "s_add_u32 m0, %[wd], 30720\n\t"
        "s_mul_i32 %[st], %[ku], 98304\n\t"
        "s_add_u32 %[st], %[st], %[w0]\n\t"
        "buffer_load_dwordx4 %[wv], %[rw], %[st] offen lds\n\t"
        "s_add_u32 %[ku], %[ku], 1\n\t"
        "s_and_b32 %[ku], %[ku], 15\n\t"
        "ds_read_b128 v[64:67], %[xb] offset:0\n\t"
        "ds_read_b64_tr_b16 v[72:73], %[wb] offset:12288\n\t"
        "ds_read_b64_tr_b16 v[74:75], %[wb] offset:12544\n\t"
        "ds_read_b128 v[68:71], %[gb] offset:512\n\t"
        "v_mfma_f32_32x32x16_bf16 %[a0], v[100:103], v[108:111], %[a0]\n\t"
        "ds_read_b128 v[76:79], %[xb] offset:32\n\t"
        "ds_read_b64_tr_b16 v[84:85], %[wb] offset:13312\n\t"
        "ds_read_b64_tr_b16 v[86:87], %[wb] offset:13568\n\t"
        "ds_read_b128 v[80:83], %[gb] offset:544\n\t"
        "s_add_u32 m0, %[xd], 33280\n\t"
        "s_add_u32 %[st], %[xs], %[kx]\n\t"
        "buffer_load_dwordx4 %[xv], %[rx], %[st] offen lds\n\t"
        "s_add_u32 %[kx], %[kx], 128\n\t"
        "s_and_b32 %[kx], %[kx], 0x3ff\n\t"
        "s_waitcnt lgkmcnt(4)\n\t"
        "v_lshlrev_b32 %[gz2], 16, v64\n\t"
        "v_lshlrev_b32 %[gz3], 16, v68\n\t"
        "v_and_b32 v68, %[msk], v68\n\t"
        "v_fmac_f32 %[gz0], %[gz2], %[gz3]\n\t"
        "v_and_b32 %[gz2], %[msk], v64\n\t"
        "v_fmac_f32 %[gz1], %[gz2], v68\n\t"
        "v_lshlrev_b32 %[gz2], 16, v65\n\t"
        "v_lshlrev_b32 %[gz3], 16, v69\n\t"
        "v_and_b32 v69, %[msk], v69\n\t"
        "v_fmac_f32 %[gz0], %[gz2], %[gz3]\n\t"
        "v_and_b32 %[gz2], %[msk], v65\n\t"
        "v_fmac_f32 %[gz1], %[gz2], v69\n\t"
        "v_lshlrev_b32 %[gz2], 16, v66\n\t"
        "v_lshlrev_b32 %[gz3], 16, v70\n\t"
        "v_and_b32 v70, %[msk], v70\n\t"
        "v_fmac_f32 %[gz0], %[gz2], %[gz3]\n\t"
        "v_and_b32 %[gz2], %[msk], v66\n\t"
        "v_fmac_f32 %[gz1], %[gz2], v70\n\t"
        "v_lshlrev_b32 %[gz2], 16, v67\n\t"
        "v_lshlrev_b32 %[gz3], 16, v71\n\t"
        "v_and_b32 v71, %[msk], v71\n\t"
        "v_fmac_f32 %[gz0], %[gz2], %[gz3]\n\t"
        "v_and_b32 %[gz2], %[msk], v67\n\t"
        "v_fmac_f32 %[gz1], %[gz2], v71\n\t"
        "s_waitcnt lgkmcnt(0)\n\t"
        "v_lshlrev_b32 %[gz2], 16, v76\n\t"
        "v_lshlrev_b32 %[gz3], 16, v80\n\t"
        "v_and_b32 v80, %[msk], v80\n\t"
        "v_fmac_f32 %[gz0], %[gz2], %[gz3]\n\t"
        "v_and_b32 %[gz2], %[msk], v76\n\t"
        "v_fmac_f32 %[gz1], %[gz2], v80\n\t"
        "v_lshlrev_b32 %[gz2], 16, v77\n\t"
        "v_lshlrev_b32 %[gz3], 16, v81\n\t"
        "v_and_b32 v81, %[msk], v81\n\t"
        "v_fmac_f32 %[gz0], %[gz2], %[gz3]\n\t"
        "v_and_b32 %[gz2], %[msk], v77\n\t"
        "v_fmac_f32 %[gz1], %[gz2], v81\n\t"
        "v_lshlrev_b32 %[gz2], 16, v78\n\t"
        "v_lshlrev_b32 %[gz3], 16, v82\n\t"
        "v_and_b32 v82, %[msk], v82\n\t"
        "v_fmac_f32 %[gz0], %[gz2], %[gz3]\n\t"
        "v_and_b32 %[gz2], %[msk], v78\n\t"
        "v_fmac_f32 %[gz1], %[gz2], v82\n\t"
        "v_lshlrev_b32 %[gz2], 16, v79\n\t"
        "v_lshlrev_b32 %[gz3], 16, v83\n\t"
        "v_and_b32 v83, %[msk], v83\n\t"
        "v_fmac_f32 %[gz0], %[gz2], %[gz3]\n\t"
        "v_and_b32 %[gz2], %[msk], v79\n\t"
        "v_fmac_f32 %[gz1], %[gz2], v83\n\t"
        "s_waitcnt vmcnt(4) lgkmcnt(0)\n\t"
        "s_barrier\n\t"
        "v_mfma_f32_32x32x16_bf16 %[a0], v[64:67], v[72:75], %[a0]\n\t"
        "s_add_u32 m0, %[wd], 0\n\t"
        "s_mul_i32 %[st], %[ku], 98304\n\t"
        "s_add_u32 %[st], %[st], %[w0]\n\t"
        "buffer_load_dwordx4 %[wv], %[rw], %[st] offen lds\n\t"
        "s_add_u32 %[ku], %[ku], 1\n\t"
        "s_and_b32 %[ku], %[ku], 15\n\t"
        "ds_read_b128 v[88:91], %[xb] offset:64\n\t"
        "ds_read_b64_tr_b16 v[96:97], %[wb] offset:18432\n\t"
        "ds_read_b64_tr_b16 v[98:99], %[wb] offset:18688\n\t"
        "ds_read_b128 v[92:95], %[gb] offset:576\n\t"
        "v_mfma_f32_32x32x16_bf16 %[a0], v[76:79], v[84:87], %[a0]\n\t"
        "ds_read_b128 v[100:103], %[xb] offset:96\n\t"
        "ds_read_b64_tr_b16 v[108:109], %[wb] offset:19456\n\t"
        "ds_read_b64_tr_b16 v[110:111], %[wb] offset:19712\n\t"
        "ds_read_b128 v[104:107], %[gb] offset:608\n\t"
        "s_waitcnt lgkmcnt(4)\n\t"
        "v_lshlrev_b32 %[gz2], 16, v88\n\t"
        "v_lshlrev_b32 %[gz3], 16, v92\n\t"
        "v_and_b32 v92, %[msk], v92\n\t"
        "v_fmac_f32 %[gz0], %[gz2], %[gz3]\n\t"
        "v_and_b32 %[gz2], %[msk], v88\n\t"
        "v_fmac_f32 %[gz1], %[gz2], v92\n\t"
        "v_lshlrev_b32 %[gz2], 16, v89\n\t"
        "v_lshlrev_b32 %[gz3], 16, v93\n\t"
        "v_and_b32 v93, %[msk], v93\n\t"
        "v_fmac_f32 %[gz0], %[gz2], %[gz3]\n\t"
        "v_and_b32 %[gz2], %[msk], v89\n\t"
        "v_fmac_f32 %[gz1], %[gz2], v93\n\t"
        "v_lshlrev_b32 %[gz2], 16, v90\n\t"
        "v_lshlrev_b32 %[gz3], 16, v94\n\t"
        "v_and_b32 v94, %[msk], v94\n\t"
        "v_fmac_f32 %[gz0], %[gz2], %[gz3]\n\t"
        "v_and_b32 %[gz2], %[msk], v90\n\t"
        "v_fmac_f32 %[gz1], %[gz2], v94\n\t"
        "v_lshlrev_b32 %[gz2], 16, v91\n\t"
        "v_lshlrev_b32 %[gz3], 16, v95\n\t"
        "v_and_b32 v95, %[msk], v95\n\t"
        "v_fmac_f32 %[gz0], %[gz2], %[gz3]\n\t"
        "v_and_b32 %[gz2], %[msk], v91\n\t"
        "v_fmac_f32 %[gz1], %[gz2], v95\n\t"
        "s_waitcnt lgkmcnt(0)\n\t"
        "v_lshlrev_b32 %[gz2], 16, v100\n\t"
        "v_lshlrev_b32 %[gz3], 16, v104\n\t"
        "v_and_b32 v104, %[msk], v104\n\t"
        "v_fmac_f32 %[gz0], %[gz2], %[gz3]\n\t"
        "v_and_b32 %[gz2], %[msk], v100\n\t"
        "v_fmac_f32 %[gz1], %[gz2], v104\n\t"
        "v_lshlrev_b32 %[gz2], 16, v101\n\t"
        "v_lshlrev_b32 %[gz3], 16, v105\n\t"
        "v_and_b32 v105, %[msk], v105\n\t"
        "v_fmac_f32 %[gz0], %[gz2], %[gz3]\n\t"
        "v_and_b32 %[gz2], %[msk], v101\n\t"
        "v_fmac_f32 %[gz1], %[gz2], v105\n\t"
        "v_lshlrev_b32 %[gz2], 16, v102\n\t"
        "v_lshlrev_b32 %[gz3], 16, v106\n\t"
        "v_and_b32 v106, %[msk], v106\n\t"
        "v_fmac_f32 %[gz0], %[gz2], %[gz3]\n\t"
        "v_and_b32 %[gz2], %[msk], v102\n\t"
        "v_fmac_f32 %[gz1], %[gz2], v106\n\t"
        "v_lshlrev_b32 %[gz2], 16, v103\n\t"
        "v_lshlrev_b32 %[gz3], 16, v107\n\t"
        "v_and_b32 v107, %[msk], v107\n\t"
        "v_fmac_f32 %[gz0], %[gz2], %[gz3]\n\t"
        "v_and_b32 %[gz2], %[msk], v103\n\t"
        "v_fmac_f32 %[gz1], %[gz2], v107\n\t"
        "s_waitcnt vmcnt(3) lgkmcnt(0)\n\t"
        "s_barrier\n\t"
        "v_mfma_f32_32x32x16_bf16 %[a0], v[88:91], v[96:99], %[a0]\n\t"
        "s_add_u32 m0, %[wd], 6144\n\t"
        "s_mul_i32 %[st], %[ku], 98304\n\t"
        "s_add_u32 %[st], %[st], %[w0]\n\t"
        "buffer_load_dwordx4 %[wv], %[rw], %[st] offen lds\n\t"
        "s_add_u32 %[ku], %[ku], 1\n\t"
        "s_and_b32 %[ku], %[ku], 15\n\t"
        "ds_read_b128 v[64:67], %[xb] offset:16640\n\t"
        "ds_read_b64_tr_b16 v[72:73], %[wb] offset:24576\n\t"
        "ds_read_b64_tr_b16 v[74:75], %[wb] offset:24832\n\t"
        "ds_read_b128 v[68:71], %[gb] offset:640\n\t"
        "v_mfma_f32_32x32x16_bf16 %[a0], v[100:103], v[108:111], %[a0]\n\t"
        "ds_read_b128 v[76:79], %[xb] offset:16672\n\t"
        "ds_read_b64_tr_b16 v[84:85], %[wb] offset:25600\n\t"
        "ds_read_b64_tr_b16 v[86:87], %[wb] offset:25856\n\t"
        "ds_read_b128 v[80:83], %[gb] offset:672\n\t"
        "s_add_u32 m0, %[xd], 49920\n\t"
        "s_add_u32 %[st], %[xs], %[kx]\n\t"
        "buffer_load_dwordx4 %[xv], %[rx], %[st] offen lds\n\t"
        "s_add_u32 %[kx], %[kx], 128\n\t"
        "s_and_b32 %[kx], %[kx], 0x3ff\n\t"
        "s_waitcnt lgkmcnt(4)\n\t"
        "v_lshlrev_b32 %[gz2], 16, v64\n\t"
        "v_lshlrev_b32 %[gz3], 16, v68\n\t"
        "v_and_b32 v68, %[msk], v68\n\t"
        "v_fmac_f32 %[gz0], %[gz2], %[gz3]\n\t"
        "v_and_b32 %[gz2], %[msk], v64\n\t"
        "v_fmac_f32 %[gz1], %[gz2], v68\n\t"
        "v_lshlrev_b32 %[gz2], 16, v65\n\t"
        "v_lshlrev_b32 %[gz3], 16, v69\n\t"
        "v_and_b32 v69, %[msk], v69\n\t"
        "v_fmac_f32 %[gz0], %[gz2], %[gz3]\n\t"
        "v_and_b32 %[gz2], %[msk], v65\n\t"
        "v_fmac_f32 %[gz1], %[gz2], v69\n\t"
        "v_lshlrev_b32 %[gz2], 16, v66\n\t"
        "v_lshlrev_b32 %[gz3], 16, v70\n\t"
        "v_and_b32 v70, %[msk], v70\n\t"
        "v_fmac_f32 %[gz0], %[gz2], %[gz3]\n\t"
        "v_and_b32 %[gz2], %[msk], v66\n\t"
        "v_fmac_f32 %[gz1], %[gz2], v70\n\t"
        "v_lshlrev_b32 %[gz2], 16, v67\n\t"
        "v_lshlrev_b32 %[gz3], 16, v71\n\t"
        "v_and_b32 v71, %[msk], v71\n\t"
        "v_fmac_f32 %[gz0], %[gz2], %[gz3]\n\t"
        "v_and_b32 %[gz2], %[msk], v67\n\t"
        "v_fmac_f32 %[gz1], %[gz2], v71\n\t"
        "s_waitcnt lgkmcnt(0)\n\t"
        "v_lshlrev_b32 %[gz2], 16, v76\n\t"
        "v_lshlrev_b32 %[gz3], 16, v80\n\t"
        "v_and_b32 v80, %[msk], v80\n\t"
        "v_fmac_f32 %[gz0], %[gz2], %[gz3]\n\t"
        "v_and_b32 %[gz2], %[msk], v76\n\t"
        "v_fmac_f32 %[gz1], %[gz2], v80\n\t"
        "v_lshlrev_b32 %[gz2], 16, v77\n\t"
        "v_lshlrev_b32 %[gz3], 16, v81\n\t"
        "v_and_b32 v81, %[msk], v81\n\t"
        "v_fmac_f32 %[gz0], %[gz2], %[gz3]\n\t"
        "v_and_b32 %[gz2], %[msk], v77\n\t"
        "v_fmac_f32 %[gz1], %[gz2], v81\n\t"
        "v_lshlrev_b32 %[gz2], 16, v78\n\t"
        "v_lshlrev_b32 %[gz3], 16, v82\n\t"
        "v_and_b32 v82, %[msk], v82\n\t"
        "v_fmac_f32 %[gz0], %[gz2], %[gz3]\n\t"
        "v_and_b32 %[gz2], %[msk], v78\n\t"
        "v_fmac_f32 %[gz1], %[gz2], v82\n\t"
        "v_lshlrev_b32 %[gz2], 16, v79\n\t"
        "v_lshlrev_b32 %[gz3], 16, v83\n\t"
        "v_and_b32 v83, %[msk], v83\n\t"
        "v_fmac_f32 %[gz0], %[gz2], %[gz3]\n\t"
        "v_and_b32 %[gz2], %[msk], v79\n\t"
        "v_fmac_f32 %[gz1], %[gz2], v83\n\t"
        "s_waitcnt vmcnt(4) lgkmcnt(0)\n\t"
        "s_barrier\n\t"
        "v_mfma_f32_32x32x16_bf16 %[a0], v[64:67], v[72:75], %[a0]\n\t"
        "s_add_u32 m0, %[wd], 12288\n\t"
        "s_mul_i32 %[st], %[ku], 98304\n\t"
        "s_add_u32 %[st], %[st], %[w0]\n\t"
        "buffer_load_dwordx4 %[wv], %[rw], %[st] offen lds\n\t"
        "s_add_u32 %[ku], %[ku], 1\n\t"
        "s_and_b32 %[ku], %[ku], 15\n\t"
        "ds_read_b128 v[88:91], %[xb] offset:16704\n\t"
        "ds_read_b64_tr_b16 v[96:97], %[wb] offset:30720\n\t"
        "ds_read_b64_tr_b16 v[98:99], %[wb] offset:30976\n\t"
        "ds_read_b128 v[92:95], %[gb] offset:704\n\t"
        "v_mfma_f32_32x32x16_bf16 %[a0], v[76:79], v[84:87], %[a0]\n\t"
        "ds_read_b128 v[100:103], %[xb] offset:16736\n\t"
        "ds_read_b64_tr_b16 v[108:109], %[wb] offset:31744\n\t"
        "ds_read_b64_tr_b16 v[110:111], %[wb] offset:32000\n\t"
        "ds_read_b128 v[104:107], %[gb] offset:736\n\t"
        "s_waitcnt lgkmcnt(4)\n\t"
        "v_lshlrev_b32 %[gz2], 16, v88\n\t"
        "v_lshlrev_b32 %[gz3], 16, v92\n\t"
        "v_and_b32 v92, %[msk], v92\n\t"
        "v_fmac_f32 %[gz0], %[gz2], %[gz3]\n\t"
        "v_and_b32 %[gz2], %[msk], v88\n\t"
        "v_fmac_f32 %[gz1], %[gz2], v92\n\t"
        "v_lshlrev_b32 %[gz2], 16, v89\n\t"
        "v_lshlrev_b32 %[gz3], 16, v93\n\t"
        "v_and_b32 v93, %[msk], v93\n\t"
        "v_fmac_f32 %[gz0], %[gz2], %[gz3]\n\t"
        "v_and_b32 %[gz2], %[msk], v89\n\t"
        "v_fmac_f32 %[gz1], %[gz2], v93\n\t"
        "v_lshlrev_b32 %[gz2], 16, v90\n\t"
        "v_lshlrev_b32 %[gz3], 16, v94\n\t"
        "v_and_b32 v94, %[msk], v94\n\t"
        "v_fmac_f32 %[gz0], %[gz2], %[gz3]\n\t"
        "v_and_b32 %[gz2], %[msk], v90\n\t"
        "v_fmac_f32 %[gz1], %[gz2], v94\n\t"
        "v_lshlrev_b32 %[gz2], 16, v91\n\t"
        "v_lshlrev_b32 %[gz3], 16, v95\n\t"
        "v_and_b32 v95, %[msk], v95\n\t"
        "v_fmac_f32 %[gz0], %[gz2], %[gz3]\n\t"
        "v_and_b32 %[gz2], %[msk], v91\n\t"
        "v_fmac_f32 %[gz1], %[gz2], v95\n\t"
        "s_waitcnt lgkmcnt(0)\n\t"
        "v_lshlrev_b32 %[gz2], 16, v100\n\t"
        "v_lshlrev_b32 %[gz3], 16, v104\n\t"
        "v_and_b32 v104, %[msk], v104\n\t"
        "v_fmac_f32 %[gz0], %[gz2], %[gz3]\n\t"
        "v_and_b32 %[gz2], %[msk], v100\n\t"
        "v_fmac_f32 %[gz1], %[gz2], v104\n\t"
        "v_lshlrev_b32 %[gz2], 16, v101\n\t"
        "v_lshlrev_b32 %[gz3], 16, v105\n\t"
        "v_and_b32 v105, %[msk], v105\n\t"
        "v_fmac_f32 %[gz0], %[gz2], %[gz3]\n\t"
        "v_and_b32 %[gz2], %[msk], v101\n\t"
        "v_fmac_f32 %[gz1], %[gz2], v105\n\t"
        "v_lshlrev_b32 %[gz2], 16, v102\n\t"
        "v_lshlrev_b32 %[gz3], 16, v106\n\t"
        "v_and_b32 v106, %[msk], v106\n\t"
        "v_fmac_f32 %[gz0], %[gz2], %[gz3]\n\t"
        "v_and_b32 %[gz2], %[msk], v102\n\t"
        "v_fmac_f32 %[gz1], %[gz2], v106\n\t"
        "v_lshlrev_b32 %[gz2], 16, v103\n\t"
        "v_lshlrev_b32 %[gz3], 16, v107\n\t"
        "v_and_b32 v107, %[msk], v107\n\t"
        "v_fmac_f32 %[gz0], %[gz2], %[gz3]\n\t"
        "v_and_b32 %[gz2], %[msk], v103\n\t"
        "v_fmac_f32 %[gz1], %[gz2], v107\n\t"
        "s_waitcnt vmcnt(3) lgkmcnt(0)\n\t"
        "s_barrier\n\t"
        "v_mfma_f32_32x32x16_bf16 %[a0], v[88:91], v[96:99], %[a0]\n\t"
        "s_add_u32 m0, %[wd], 18432\n\t"
        "s_mul_i32 %[st], %[ku], 98304\n\t"
        "s_add_u32 %[st], %[st], %[w0]\n\t"
        "buffer_load_dwordx4 %[wv], %[rw], %[st] offen lds\n\t"
        "s_add_u32 %[ku], %[ku], 1\n\t"
        "s_and_b32 %[ku], %[ku], 15\n\t"
        "ds_read_b128 v[64:67], %[xb] offset:33280\n\t"
        "ds_read_b64_tr_b16 v[72:73], %[wb] offset:0\n\t"
        "ds_read_b64_tr_b16 v[74:75], %[wb] offset:256\n\t"
        "ds_read_b128 v[68:71], %[gb] offset:768\n\t"
        "v_mfma_f32_32x32x16_bf16 %[a0], v[100:103], v[108:111], %[a0]\n\t"
        "ds_read_b128 v[76:79], %[xb] offset:33312\n\t"
        "ds_read_b64_tr_b16 v[84:85], %[wb] offset:1024\n\t"
        "ds_read_b64_tr_b16 v[86:87], %[wb] offset:1280\n\t"
        "ds_read_b128 v[80:83], %[gb] offset:800\n\t"
        "s_waitcnt lgkmcnt(4)\n\t"
        "v_lshlrev_b32 %[gz2], 16, v64\n\t"
        "v_lshlrev_b32 %[gz3], 16, v68\n\t"
        "v_and_b32 v68, %[msk], v68\n\t"
        "v_fmac_f32 %[gz0], %[gz2], %[gz3]\n\t"
        "v_and_b32 %[gz2], %[msk], v64\n\t"
        "v_fmac_f32 %[gz1], %[gz2], v68\n\t"
        "v_lshlrev_b32 %[gz2], 16, v65\n\t"
        "v_lshlrev_b32 %[gz3], 16, v69\n\t"
        "v_and_b32 v69, %[msk], v69\n\t"
        "v_fmac_f32 %[gz0], %[gz2], %[gz3]\n\t"
        "v_and_b32 %[gz2], %[msk], v65\n\t"
        "v_fmac_f32 %[gz1], %[gz2], v69\n\t"
        "v_lshlrev_b32 %[gz2], 16, v66\n\t"
        "v_lshlrev_b32 %[gz3], 16, v70\n\t"
        "v_and_b32 v70, %[msk], v70\n\t"
        "v_fmac_f32 %[gz0], %[gz2], %[gz3]\n\t"
        "v_and_b32 %[gz2], %[msk], v66\n\t"
        "v_fmac_f32 %[gz1], %[gz2], v70\n\t"
        "v_lshlrev_b32 %[gz2], 16, v67\n\t"
        "v_lshlrev_b32 %[gz3], 16, v71\n\t"
        "v_and_b32 v71, %[msk], v71\n\t"
        "v_fmac_f32 %[gz0], %[gz2], %[gz3]\n\t"
        "v_and_b32 %[gz2], %[msk], v67\n\t"
        "v_fmac_f32 %[gz1], %[gz2], v71\n\t"
        "s_waitcnt lgkmcnt(0)\n\t"
        "v_lshlrev_b32 %[gz2], 16, v76\n\t"
        "v_lshlrev_b32 %[gz3], 16, v80\n\t"
        "v_and_b32 v80, %[msk], v80\n\t"
        "v_fmac_f32 %[gz0], %[gz2], %[gz3]\n\t"
        "v_and_b32 %[gz2], %[msk], v76\n\t"
        "v_fmac_f32 %[gz1], %[gz2], v80\n\t"
        "v_lshlrev_b32 %[gz2], 16, v77\n\t"
        "v_lshlrev_b32 %[gz3], 16, v81\n\t"
        "v_and_b32 v81, %[msk], v81\n\t"
        "v_fmac_f32 %[gz0], %[gz2], %[gz3]\n\t"
        "v_and_b32 %[gz2], %[msk], v77\n\t"
        "v_fmac_f32 %[gz1], %[gz2], v81\n\t"
        "v_lshlrev_b32 %[gz2], 16, v78\n\t"
        "v_lshlrev_b32 %[gz3], 16, v82\n\t"
        "v_and_b32 v82, %[msk], v82\n\t"
        "v_fmac_f32 %[gz0], %[gz2], %[gz3]\n\t"
        "v_and_b32 %[gz2], %[msk], v78\n\t"
        "v_fmac_f32 %[gz1], %[gz2], v82\n\t"
        "v_lshlrev_b32 %[gz2], 16, v79\n\t"
        "v_lshlrev_b32 %[gz3], 16, v83\n\t"
        "v_and_b32 v83, %[msk], v83\n\t"
        "v_fmac_f32 %[gz0], %[gz2], %[gz3]\n\t"
        "v_and_b32 %[gz2], %[msk], v79\n\t"
        "v_fmac_f32 %[gz1], %[gz2], v83\n\t"
        "s_waitcnt vmcnt(3) lgkmcnt(0)\n\t"
        "s_barrier\n\t"
        "v_mfma_f32_32x32x16_bf16 %[a0], v[64:67], v[72:75], %[a0]\n\t"
        "ds_read_b128 v[88:91], %[xb] offset:33344\n\t"
        "ds_read_b64_tr_b16 v[96:97], %[wb] offset:6144\n\t"
        "ds_read_b64_tr_b16 v[98:99], %[wb] offset:6400\n\t"
        "ds_read_b128 v[92:95], %[gb] offset:832\n\t"
        "v_mfma_f32_32x32x16_bf16 %[a0], v[76:79], v[84:87], %[a0]\n\t"
        "ds_read_b128 v[100:103], %[xb] offset:33376\n\t"
        "ds_read_b64_tr_b16 v[108:109], %[wb] offset:7168\n\t"
        "ds_read_b64_tr_b16 v[110:111], %[wb] offset:7424\n\t"
        "ds_read_b128 v[104:107], %[gb] offset:864\n\t"
        "s_waitcnt lgkmcnt(4)\n\t"
        "v_lshlrev_b32 %[gz2], 16, v88\n\t"
        "v_lshlrev_b32 %[gz3], 16, v92\n\t"
        "v_and_b32 v92, %[msk], v92\n\t"
        "v_fmac_f32 %[gz0], %[gz2], %[gz3]\n\t"
        "v_and_b32 %[gz2], %[msk], v88\n\t"
        "v_fmac_f32 %[gz1], %[gz2], v92\n\t"
        "v_lshlrev_b32 %[gz2], 16, v89\n\t"
        "v_lshlrev_b32 %[gz3], 16, v93\n\t"
        "v_and_b32 v93, %[msk], v93\n\t"
        "v_fmac_f32 %[gz0], %[gz2], %[gz3]\n\t"
        "v_and_b32 %[gz2], %[msk], v89\n\t"
        "v_fmac_f32 %[gz1], %[gz2], v93\n\t"
        "v_lshlrev_b32 %[gz2], 16, v90\n\t"
        "v_lshlrev_b32 %[gz3], 16, v94\n\t"
        "v_and_b32 v94, %[msk], v94\n\t"
        "v_fmac_f32 %[gz0], %[gz2], %[gz3]\n\t"
        "v_and_b32 %[gz2], %[msk], v90\n\t"
        "v_fmac_f32 %[gz1], %[gz2], v94\n\t"
        "v_lshlrev_b32 %[gz2], 16, v91\n\t"
        "v_lshlrev_b32 %[gz3], 16, v95\n\t"
        "v_and_b32 v95, %[msk], v95\n\t"
        "v_fmac_f32 %[gz0], %[gz2], %[gz3]\n\t"
        "v_and_b32 %[gz2], %[msk], v91\n\t"
        "v_fmac_f32 %[gz1], %[gz2], v95\n\t"
        "s_waitcnt lgkmcnt(0)\n\t"
        "v_lshlrev_b32 %[gz2], 16, v100\n\t"
        "v_lshlrev_b32 %[gz3], 16, v104\n\t"
        "v_and_b32 v104, %[msk], v104\n\t"
        "v_fmac_f32 %[gz0], %[gz2], %[gz3]\n\t"
        "v_and_b32 %[gz2], %[msk], v100\n\t"
        "v_fmac_f32 %[gz1], %[gz2], v104\n\t"
        "v_lshlrev_b32 %[gz2], 16, v101\n\t"
        "v_lshlrev_b32 %[gz3], 16, v105\n\t"
        "v_and_b32 v105, %[msk], v105\n\t"
        "v_fmac_f32 %[gz0], %[gz2], %[gz3]\n\t"
        "v_and_b32 %[gz2], %[msk], v101\n\t"
        "v_fmac_f32 %[gz1], %[gz2], v105\n\t"
        "v_lshlrev_b32 %[gz2], 16, v102\n\t"
        "v_lshlrev_b32 %[gz3], 16, v106\n\t"
        "v_and_b32 v106, %[msk], v106\n\t"
        "v_fmac_f32 %[gz0], %[gz2], %[gz3]\n\t"
        "v_and_b32 %[gz2], %[msk], v102\n\t"
        "v_fmac_f32 %[gz1], %[gz2], v106\n\t"
        "v_lshlrev_b32 %[gz2], 16, v103\n\t"
        "v_lshlrev_b32 %[gz3], 16, v107\n\t"
        "v_and_b32 v107, %[msk], v107\n\t"
        "v_fmac_f32 %[gz0], %[gz2], %[gz3]\n\t"
        "v_and_b32 %[gz2], %[msk], v103\n\t"
        "v_fmac_f32 %[gz1], %[gz2], v107\n\t"
        "s_waitcnt vmcnt(1) lgkmcnt(0)\n\t"
        "s_barrier\n\t"
        "v_mfma_f32_32x32x16_bf16 %[a0], v[88:91], v[96:99], %[a0]\n\t"
        "ds_read_b128 v[64:67], %[xb] offset:49920\n\t"
        "ds_read_b64_tr_b16 v[72:73], %[wb] offset:12288\n\t"
        "ds_read_b64_tr_b16 v[74:75], %[wb] offset:12544\n\t"
        "ds_read_b128 v[68:71], %[gb] offset:896\n\t"
        "v_mfma_f32_32x32x16_bf16 %[a0], v[100:103], v[108:111], %[a0]\n\t"
        "ds_read_b128 v[76:79], %[xb] offset:49952\n\t"
        "ds_read_b64_tr_b16 v[84:85], %[wb] offset:13312\n\t"
        "ds_read_b64_tr_b16 v[86:87], %[wb] offset:13568\n\t"
        "ds_read_b128 v[80:83], %[gb] offset:928\n\t"
        "s_waitcnt lgkmcnt(4)\n\t"
        "v_lshlrev_b32 %[gz2], 16, v64\n\t"
        "v_lshlrev_b32 %[gz3], 16, v68\n\t"
        "v_and_b32 v68, %[msk], v68\n\t"
        "v_fmac_f32 %[gz0], %[gz2], %[gz3]\n\t"
        "v_and_b32 %[gz2], %[msk], v64\n\t"
        "v_fmac_f32 %[gz1], %[gz2], v68\n\t"
        "v_lshlrev_b32 %[gz2], 16, v65\n\t"
        "v_lshlrev_b32 %[gz3], 16, v69\n\t"
        "v_and_b32 v69, %[msk], v69\n\t"
        "v_fmac_f32 %[gz0], %[gz2], %[gz3]\n\t"
        "v_and_b32 %[gz2], %[msk], v65\n\t"
        "v_fmac_f32 %[gz1], %[gz2], v69\n\t"
        "v_lshlrev_b32 %[gz2], 16, v66\n\t"
        "v_lshlrev_b32 %[gz3], 16, v70\n\t"
        "v_and_b32 v70, %[msk], v70\n\t"
        "v_fmac_f32 %[gz0], %[gz2], %[gz3]\n\t"
        "v_and_b32 %[gz2], %[msk], v66\n\t"
        "v_fmac_f32 %[gz1], %[gz2], v70\n\t"
        "v_lshlrev_b32 %[gz2], 16, v67\n\t"
        "v_lshlrev_b32 %[gz3], 16, v71\n\t"
        "v_and_b32 v71, %[msk], v71\n\t"
        "v_fmac_f32 %[gz0], %[gz2], %[gz3]\n\t"
        "v_and_b32 %[gz2], %[msk], v67\n\t"
        "v_fmac_f32 %[gz1], %[gz2], v71\n\t"
        "s_waitcnt lgkmcnt(0)\n\t"
        "v_lshlrev_b32 %[gz2], 16, v76\n\t"
        "v_lshlrev_b32 %[gz3], 16, v80\n\t"
        "v_and_b32 v80, %[msk], v80\n\t"
        "v_fmac_f32 %[gz0], %[gz2], %[gz3]\n\t"
        "v_and_b32 %[gz2], %[msk], v76\n\t"
        "v_fmac_f32 %[gz1], %[gz2], v80\n\t"
        "v_lshlrev_b32 %[gz2], 16, v77\n\t"
        "v_lshlrev_b32 %[gz3], 16, v81\n\t"
        "v_and_b32 v81, %[msk], v81\n\t"
        "v_fmac_f32 %[gz0], %[gz2], %[gz3]\n\t"
        "v_and_b32 %[gz2], %[msk], v77\n\t"
        "v_fmac_f32 %[gz1], %[gz2], v81\n\t"
        "v_lshlrev_b32 %[gz2], 16, v78\n\t"
        "v_lshlrev_b32 %[gz3], 16, v82\n\t"
        "v_and_b32 v82, %[msk], v82\n\t"
        "v_fmac_f32 %[gz0], %[gz2], %[gz3]\n\t"
        "v_and_b32 %[gz2], %[msk], v78\n\t"
        "v_fmac_f32 %[gz1], %[gz2], v82\n\t"
        "v_lshlrev_b32 %[gz2], 16, v79\n\t"
        "v_lshlrev_b32 %[gz3], 16, v83\n\t"
        "v_and_b32 v83, %[msk], v83\n\t"
        "v_fmac_f32 %[gz0], %[gz2], %[gz3]\n\t"
        "v_and_b32 %[gz2], %[msk], v79\n\t"
        "v_fmac_f32 %[gz1], %[gz2], v83\n\t"
        "s_waitcnt vmcnt(0) lgkmcnt(0)\n\t"
        "s_barrier\n\t"
        "v_mfma_f32_32x32x16_bf16 %[a0], v[64:67], v[72:75], %[a0]\n\t"
        "ds_read_b128 v[88:91], %[xb] offset:49984\n\t"
        "ds_read_b64_tr_b16 v[96:97], %[wb] offset:18432\n\t"
        "ds_read_b64_tr_b16 v[98:99], %[wb] offset:18688\n\t"
        "ds_read_b128 v[92:95], %[gb] offset:960\n\t"
        "v_mfma_f32_32x32x16_bf16 %[a0], v[76:79], v[84:87], %[a0]\n\t"
        "ds_read_b128 v[100:103], %[xb] offset:50016\n\t"
        "ds_read_b64_tr_b16 v[108:109], %[wb] offset:19456\n\t"
        "ds_read_b64_tr_b16 v[110:111], %[wb] offset:19712\n\t"
        "ds_read_b128 v[104:107], %[gb] offset:992\n\t"
        "s_waitcnt lgkmcnt(4)\n\t"
        "v_lshlrev_b32 %[gz2], 16, v88\n\t"
        "v_lshlrev_b32 %[gz3], 16, v92\n\t"
        "v_and_b32 v92, %[msk], v92\n\t"
        "v_fmac_f32 %[gz0], %[gz2], %[gz3]\n\t"
        "v_and_b32 %[gz2], %[msk], v88\n\t"
        "v_fmac_f32 %[gz1], %[gz2], v92\n\t"
        "v_lshlrev_b32 %[gz2], 16, v89\n\t"
        "v_lshlrev_b32 %[gz3], 16, v93\n\t"
        "v_and_b32 v93, %[msk], v93\n\t"
        "v_fmac_f32 %[gz0], %[gz2], %[gz3]\n\t"
        "v_and_b32 %[gz2], %[msk], v89\n\t"
        "v_fmac_f32 %[gz1], %[gz2], v93\n\t"
        "v_lshlrev_b32 %[gz2], 16, v90\n\t"
        "v_lshlrev_b32 %[gz3], 16, v94\n\t"
        "v_and_b32 v94, %[msk], v94\n\t"
        "v_fmac_f32 %[gz0], %[gz2], %[gz3]\n\t"
        "v_and_b32 %[gz2], %[msk], v90\n\t"
        "v_fmac_f32 %[gz1], %[gz2], v94\n\t"
        "v_lshlrev_b32 %[gz2], 16, v91\n\t"
        "v_lshlrev_b32 %[gz3], 16, v95\n\t"
        "v_and_b32 v95, %[msk], v95\n\t"
        "v_fmac_f32 %[gz0], %[gz2], %[gz3]\n\t"
        "v_and_b32 %[gz2], %[msk], v91\n\t"
        "v_fmac_f32 %[gz1], %[gz2], v95\n\t"
        "s_waitcnt lgkmcnt(0)\n\t"
        "v_lshlrev_b32 %[gz2], 16, v100\n\t"
        "v_lshlrev_b32 %[gz3], 16, v104\n\t"
        "v_and_b32 v104, %[msk], v104\n\t"
        "v_fmac_f32 %[gz0], %[gz2], %[gz3]\n\t"
        "v_and_b32 %[gz2], %[msk], v100\n\t"
        "v_fmac_f32 %[gz1], %[gz2], v104\n\t"
        "v_lshlrev_b32 %[gz2], 16, v101\n\t"
        "v_lshlrev_b32 %[gz3], 16, v105\n\t"
        "v_and_b32 v105, %[msk], v105\n\t"
        "v_fmac_f32 %[gz0], %[gz2], %[gz3]\n\t"
        "v_and_b32 %[gz2], %[msk], v101\n\t"
        "v_fmac_f32 %[gz1], %[gz2], v105\n\t"
        "v_lshlrev_b32 %[gz2], 16, v102\n\t"
        "v_lshlrev_b32 %[gz3], 16, v106\n\t"
        "v_and_b32 v106, %[msk], v106\n\t"
        "v_fmac_f32 %[gz0], %[gz2], %[gz3]\n\t"
        "v_and_b32 %[gz2], %[msk], v102\n\t"
        "v_fmac_f32 %[gz1], %[gz2], v106\n\t"
        "v_lshlrev_b32 %[gz2], 16, v103\n\t"
        "v_lshlrev_b32 %[gz3], 16, v107\n\t"
        "v_and_b32 v107, %[msk], v107\n\t"
        "v_fmac_f32 %[gz0], %[gz2], %[gz3]\n\t"
        "v_and_b32 %[gz2], %[msk], v103\n\t"
        "v_fmac_f32 %[gz1], %[gz2], v107\n\t"
        "s_waitcnt vmcnt(0) lgkmcnt(0)\n\t"
        "s_barrier\n\t"
        "v_mfma_f32_32x32x16_bf16 %[a0], v[88:91], v[96:99], %[a0]\n\t"
        "v_mfma_f32_32x32x16_bf16 %[a0], v[100:103], v[108:111], %[a0]\n\t"
        "s_mov_b32 m0, %[sv]\n\t"
        "s_nop 10\n\t"
                : [a0] "=&v"(a0), [gz0] "+v"(gz0), [gz1] "+v"(gz1), [gz2] "+v"(gz2), [gz3] "+v"(gz3), [ep] "=&v"(epoch), [sv] "=&s"(sv), [st] "=&s"(st), [kx] "+s"(sg.kx), [ku] "+s"(sg.ku)
                : [xv] "v"(sg.xv), [wv] "v"(sg.wv), [rx] "s"(sg.rx), [rw] "s"(sg.rw), [xs] "s"(sg.xs), [xd] "s"(sg.xd), [wd] "s"(sg.wd), [w0] "s"(sg.w0),
                  [xb] "v"(xb + tv * 256), [wb] "v"(wb + 4096 + ch * 32), [gb] "v"(lds0 + L_GI + hf * 16), [msk] "s"(0xffff0000u), [re] "s"(mk_rsrc(ws, WS_END))
                : "memory", "scc", "v64", "v65", "v66", "v67", "v68", "v69", "v70", "v71", "v72", "v73", "v74", "v75", "v76", "v77", "v78", "v79", "v80", "v81", "v82", "v83", "v84", "v85", "v86", "v87", "v88", "v89", "v90", "v91", "v92", "v93", "v94", "v95", "v96", "v97", "v98", "v99", "v100", "v101", "v102", "v103", "v104", "v105", "v106", "v107", "v108", "v109", "v110", "v111");
                const float gz = gz0 + gz1;
                const auto gg = __builtin_amdgcn_permlane32_swap(__float_as_uint(gz), __float_as_uint(gz), false, false);
                const float z = __uint_as_float(gg[0]) + __uint_as_float(gg[1]);
                if (hf == 0) __hip_atomic_store((GAS unsigned long long*)(ws + WS_G) + rb * 128 + tv * 32 + r,
                                                            ((unsigned long long)(epoch + 1u) << 32) | __float_as_uint(z), __ATOMIC_RELAXED, __HIP_MEMORY_SCOPE_AGENT);
            }

            const u32x4 v0 = not_sentinel(pack8<0>(a0)), v1 = not_sentinel(pack8<8>(a0));
            __builtin_amdgcn_raw_buffer_store_b128(v0, rs, vg, gkv + tv * 2048, AUX_SC1);
            __builtin_amdgcn_raw_buffer_store_b128(v1, rs, vg + 256, gkv + tv * 2048, AUX_SC1);
            *(LAS u32x4*)(uintptr_t)vl = v0; *(LAS u32x4*)(uintptr_t)(vl + 256) = v1;
        }
    }
    *(LAS unsigned*)(uintptr_t)a_pad = (a_pad & 16) ? 0u : 0x3F803F80u;
    asm volatile("s_mov_b64 exec, 1\n\tds_add_u32 %0, %1\n\ts_mov_b64 exec, -1" :: "v"(lds0 + L_CNT + C_PROJ * 4), "v"(1) : "memory");

    lds_wait_ge<1>((volatile LAS unsigned*)(L + L_CNT + C_PROJ * 4), 8u);
    const u32x4 qpk = *(const LAS u32x4*)(uintptr_t)(a_q + kh * 1024), q1 = *(const LAS u32x4*)(uintptr_t)(a_q + (kh ^ 1) * 1024);
    const bf16x8 qf = __builtin_bit_cast(bf16x8, qpk);
    const float nm = *(const LAS float*)(L + L_XM + (hs * 2 + qp) * 256 + lane * 4);
    if (hs) __builtin_amdgcn_s_setprio(1); else __builtin_amdgcn_s_setprio(0);

    f32x16 o, o1;
    {
        u32x4 kf, v0, v1; unsigned tm = __float_as_uint(nm), st, sp, wt;
        asm volatile(
        "s_mov_b32 %[sp], 0x40000\n\t"
        "s_mov_b32 %[wt], 0\n\t"
        "ds_read_b128 %[kf], %[ak] offset:0\n\t"
        "ds_read_b128 %[v0], %[a0] offset:0\n\t"
        "v_mov_b32 v64, %[tm]\n\t"
        "v_mov_b32 v65, %[tm]\n\t"
        "v_mov_b32 v66, %[tm]\n\t"
        "v_mov_b32 v67, %[tm]\n\t"
        "v_mov_b32 v68, %[tm]\n\t"
        "v_mov_b32 v69, %[tm]\n\t"
        "v_mov_b32 v70, %[tm]\n\t"
        "v_mov_b32 v71, %[tm]\n\t"
        "v_mov_b32 v72, %[tm]\n\t"
        "v_mov_b32 v73, %[tm]\n\t"
        "v_mov_b32 v74, %[tm]\n\t"
        "v_mov_b32 v75, %[tm]\n\t"
        "v_mov_b32 v76, %[tm]\n\t"
        "v_mov_b32 v77, %[tm]\n\t"
        "v_mov_b32 v78, %[tm]\n\t"
        "v_mov_b32 v79, %[tm]\n\t"
        "s_waitcnt lgkmcnt(1)\n\t"
        "s_nop 0\n\t"
        "v_mfma_f32_32x32x16_bf16 v[32:47], %[kf], %[q0], v[64:79]\n\t"
        "v_mov_b32 v0, 0\n\t"
        "v_mov_b32 v1, 0\n\t"
        "v_mov_b32 v2, 0\n\t"
        "v_mov_b32 v3, 0\n\t"
        "v_mov_b32 v4, 0\n\t"
        "v_mov_b32 v5, 0\n\t"
        "v_mov_b32 v6, 0\n\t"
        "v_mov_b32 v7, 0\n\t"
        "v_mov_b32 v8, 0\n\t"
        "v_mov_b32 v9, 0\n\t"
        "v_mov_b32 v10, 0\n\t"
        "v_mov_b32 v11, 0\n\t"
        "v_mov_b32 v12, 0\n\t"
        "v_mov_b32 v13, 0\n\t"
        "v_mov_b32 v14, 0\n\t"
        "v_mov_b32 v15, 0\n\t"
        "v_mov_b32 v16, 0\n\t"
        "v_mov_b32 v17, 0\n\t"
        "v_mov_b32 v18, 0\n\t"
        "v_mov_b32 v19, 0\n\t"
        "v_mov_b32 v20, 0\n\t"
        "v_mov_b32 v21, 0\n\t"
        "v_mov_b32 v22, 0\n\t"
        "v_mov_b32 v23, 0\n\t"
        "v_mov_b32 v24, 0\n\t"
        "v_mov_b32 v25, 0\n\t"
        "v_mov_b32 v26, 0\n\t"
        "v_mov_b32 v27, 0\n\t"
        "v_mov_b32 v28, 0\n\t"
        "v_mov_b32 v29, 0\n\t"
        "v_mov_b32 v30, 0\n\t"
        "v_mov_b32 v31, 0\n\t"
        "v_exp_f32 v32, v32\n\t"
        "v_exp_f32 v33, v33\n\t"
        "v_exp_f32 v34, v34\n\t"
        "s_branch .Lin_%=\n\t"
        ".p2align 6\n\t"
        ".Lloop_%=:\n\t"
        "s_cmp_lg_u32 %[wt], 13\n\t"
        "s_cbranch_scc0 .Lpr_%=\n\t"
        ".Lpb_%=:\n\t"
        "v_exp_f32 v32, v32\n\t"
        "v_exp_f32 v33, v33\n\t"
        "v_exp_f32 v34, v34\n\t"
        "v_smfmac_f32_32x32x32_bf16 v[16:31], %[v1], v[56:63], %[ix]\n\t"
        ".Lin_%=:\n\t"
        "ds_read_b32 %[tm], %[ac] offset:16\n\t"
        "ds_read_b128 %[v1], %[a1] offset:0\n\t"
        "v_exp_f32 v35, v35\n\t"
        "v_exp_f32 v36, v36\n\t"
        "v_exp_f32 v37, v37\n\t"
        "v_mfma_f32_32x32x16_bf16 v[48:63], %[kf], %[q1], v[64:79]\n\t"
        "ds_read_b128 %[kf], %[ak] offset:2080\n\t"
        "v_exp_f32 v38, v38\n\t"
        "v_exp_f32 v39, v39\n\t"
        "v_exp_f32 v40, v40\n\t"
        "v_exp_f32 v41, v41\n\t"
        "v_exp_f32 v42, v42\n\t"
        "s_waitcnt lgkmcnt(2)\n\t"
        "v_smfmac_f32_32x32x32_bf16 v[0:15], %[v0], v[32:39], %[ix]\n\t"
        "v_exp_f32 v43, v43\n\t"
        "v_exp_f32 v44, v44\n\t"
        "v_exp_f32 v45, v45\n\t"
        "v_exp_f32 v46, v46\n\t"
        "v_exp_f32 v47, v47\n\t"
        "v_exp_f32 v48, v48\n\t"
        "v_exp_f32 v49, v49\n\t"
        "v_exp_f32 v50, v50\n\t"
        "s_waitcnt lgkmcnt(0)\n\t"
        "v_smfmac_f32_32x32x32_bf16 v[0:15], %[v1], v[40:47], %[ix]\n\t"
        "v_exp_f32 v51, v51\n\t"
        "v_exp_f32 v52, v52\n\t"
        "v_exp_f32 v53, v53\n\t"
        "v_mfma_f32_32x32x16_bf16 v[32:47], %[kf], %[q0], v[64:79]\n\t"
        "v_exp_f32 v54, v54\n\t"
        "v_exp_f32 v55, v55\n\t"
        "v_exp_f32 v56, v56\n\t"
        "v_exp_f32 v57, v57\n\t"
        "v_exp_f32 v58, v58\n\t"
        "v_smfmac_f32_32x32x32_bf16 v[16:31], %[v0], v[48:55], %[ix]\n\t"
        "ds_read_b128 %[v0], %[a0] offset:2080\n\t"
        "v_exp_f32 v59, v59\n\t"
        "v_exp_f32 v60, v60\n\t"
        "v_exp_f32 v61, v61\n\t"
        "v_exp_f32 v62, v62\n\t"
        "v_exp_f32 v63, v63\n\t"
        "s_waitcnt vmcnt(0)\n\t"
        "s_mov_b64 exec, 1\n\t"
        "ds_add_u32 %[ac], %[ix]\n\t"
        "s_mov_b64 exec, -1\n\t"
        ".Lsd0_%=:\n\t"
        "v_readfirstlane_b32 %[st], %[tm]\n\t"
        "s_nop 0\n\t"
        "s_cmp_ge_u32 %[st], %[wt]\n\t"
        "s_cbranch_scc0 .Lsl0_%=\n\t"
        "s_add_u32 %[wt], %[wt], 1\n\t"
        "v_exp_f32 v32, v32\n\t"
        "v_exp_f32 v33, v33\n\t"
        "v_exp_f32 v34, v34\n\t"
        "v_smfmac_f32_32x32x32_bf16 v[16:31], %[v1], v[56:63], %[ix]\n\t"
        "ds_read_b128 %[v1], %[a1] offset:2080\n\t"
        "v_exp_f32 v35, v35\n\t"
        "v_exp_f32 v36, v36\n\t"
        "v_exp_f32 v37, v37\n\t"
        "v_mfma_f32_32x32x16_bf16 v[48:63], %[kf], %[q1], v[64:79]\n\t"
        "ds_read_b128 %[kf], %[ak] offset:16640\n\t"
        "v_exp_f32 v38, v38\n\t"
        "v_exp_f32 v39, v39\n\t"
        "v_exp_f32 v40, v40\n\t"
        "v_exp_f32 v41, v41\n\t"
        "v_exp_f32 v42, v42\n\t"
        "s_waitcnt lgkmcnt(2)\n\t"
        "v_smfmac_f32_32x32x32_bf16 v[0:15], %[v0], v[32:39], %[ix]\n\t"
        "v_exp_f32 v43, v43\n\t"
        "v_exp_f32 v44, v44\n\t"
        "v_exp_f32 v45, v45\n\t"
        "v_exp_f32 v46, v46\n\t"
        "v_exp_f32 v47, v47\n\t"
        "v_exp_f32 v48, v48\n\t"
        "v_exp_f32 v49, v49\n\t"
        "v_exp_f32 v50, v50\n\t"
        "s_waitcnt lgkmcnt(0)\n\t"
        "v_smfmac_f32_32x32x32_bf16 v[0:15], %[v1], v[40:47], %[ix]\n\t"
        "v_exp_f32 v51, v51\n\t"
        "v_exp_f32 v52, v52\n\t"
        "v_exp_f32 v53, v53\n\t"
        "v_mfma_f32_32x32x16_bf16 v[32:47], %[kf], %[q0], v[64:79]\n\t"
        "v_exp_f32 v54, v54\n\t"
        "v_exp_f32 v55, v55\n\t"
        "v_exp_f32 v56, v56\n\t"
        "v_exp_f32 v57, v57\n\t"
        "v_exp_f32 v58, v58\n\t"
        "v_smfmac_f32_32x32x32_bf16 v[16:31], %[v0], v[48:55], %[ix]\n\t"
        "ds_read_b128 %[v0], %[a0] offset:16640\n\t"
        "v_exp_f32 v59, v59\n\t"
        "v_exp_f32 v60, v60\n\t"
        "v_exp_f32 v61, v61\n\t"
        "v_exp_f32 v62, v62\n\t"
        "v_exp_f32 v63, v63\n\t"
        "s_cmp_eq_u32 %[wt], 1\n\t"
        "s_cbranch_scc1 .Lfx_%=\n\t"
        ".Lfb_%=:\n\t"
        "v_exp_f32 v32, v32\n\t"
        "v_exp_f32 v33, v33\n\t"
        "v_exp_f32 v34, v34\n\t"
        "v_smfmac_f32_32x32x32_bf16 v[16:31], %[v1], v[56:63], %[ix]\n\t"
        "ds_read_b32 %[tm], %[ac] offset:16\n\t"
        "ds_read_b128 %[v1], %[a1] offset:16640\n\t"
        "v_exp_f32 v35, v35\n\t"
        "v_exp_f32 v36, v36\n\t"
        "v_exp_f32 v37, v37\n\t"
        "v_mfma_f32_32x32x16_bf16 v[48:63], %[kf], %[q1], v[64:79]\n\t"
        "ds_read_b128 %[kf], %[ak] offset:18720\n\t"
        "v_exp_f32 v38, v38\n\t"
        "v_exp_f32 v39, v39\n\t"
        "v_exp_f32 v40, v40\n\t"
        "v_exp_f32 v41, v41\n\t"
        "v_exp_f32 v42, v42\n\t"
        "s_waitcnt lgkmcnt(2)\n\t"
        "v_smfmac_f32_32x32x32_bf16 v[0:15], %[v0], v[32:39], %[ix]\n\t"
        "v_exp_f32 v43, v43\n\t"
        "v_exp_f32 v44, v44\n\t"
        "v_exp_f32 v45, v45\n\t"
        "v_exp_f32 v46, v46\n\t"
        "v_exp_f32 v47, v47\n\t"
        "v_exp_f32 v48, v48\n\t"
        "v_exp_f32 v49, v49\n\t"
        "v_exp_f32 v50, v50\n\t"
        "s_waitcnt lgkmcnt(0)\n\t"
        "v_smfmac_f32_32x32x32_bf16 v[0:15], %[v1], v[40:47], %[ix]\n\t"
        "v_exp_f32 v51, v51\n\t"
        "v_exp_f32 v52, v52\n\t"
        "v_exp_f32 v53, v53\n\t"
        "v_mfma_f32_32x32x16_bf16 v[32:47], %[kf], %[q0], v[64:79]\n\t"
        "v_exp_f32 v54, v54\n\t"
        "v_exp_f32 v55, v55\n\t"
        "v_exp_f32 v56, v56\n\t"
        "v_exp_f32 v57, v57\n\t"
        "v_exp_f32 v58, v58\n\t"
        "v_smfmac_f32_32x32x32_bf16 v[16:31], %[v0], v[48:55], %[ix]\n\t"
        "ds_read_b128 %[v0], %[a0] offset:18720\n\t"
        "v_exp_f32 v59, v59\n\t"
        "v_exp_f32 v60, v60\n\t"
        "v_exp_f32 v61, v61\n\t"
        "v_exp_f32 v62, v62\n\t"
        "v_exp_f32 v63, v63\n\t"
        "s_mov_b64 exec, 1\n\t"
        "ds_add_u32 %[ac], %[ix]\n\t"
        "s_mov_b64 exec, -1\n\t"
        ".Lsd1_%=:\n\t"
        "v_readfirstlane_b32 %[st], %[tm]\n\t"
        "s_nop 0\n\t"
        "s_cmp_ge_u32 %[st], %[wt]\n\t"
        "s_cbranch_scc0 .Lsl1_%=\n\t"
        "s_add_u32 %[wt], %[wt], 1\n\t"
        "v_exp_f32 v32, v32\n\t"
        "v_exp_f32 v33, v33\n\t"
        "v_exp_f32 v34, v34\n\t"
        "v_smfmac_f32_32x32x32_bf16 v[16:31], %[v1], v[56:63], %[ix]\n\t"
        "ds_read_b128 %[v1], %[a1] offset:18720\n\t"
        "v_exp_f32 v35, v35\n\t"
        "v_exp_f32 v36, v36\n\t"
        "v_exp_f32 v37, v37\n\t"
        "v_mfma_f32_32x32x16_bf16 v[48:63], %[kf], %[q1], v[64:79]\n\t"
        "ds_read_b128 %[kf], %[ak] offset:33280\n\t"
        "v_exp_f32 v38, v38\n\t"
        "v_exp_f32 v39, v39\n\t"
        "v_exp_f32 v40, v40\n\t"
        "v_exp_f32 v41, v41\n\t"
        "v_exp_f32 v42, v42\n\t"
        "s_waitcnt lgkmcnt(2)\n\t"
        "v_smfmac_f32_32x32x32_bf16 v[0:15], %[v0], v[32:39], %[ix]\n\t"
        "v_exp_f32 v43, v43\n\t"
        "v_exp_f32 v44, v44\n\t"
        "v_exp_f32 v45, v45\n\t"
        "v_exp_f32 v46, v46\n\t"
        "v_exp_f32 v47, v47\n\t"
        "v_exp_f32 v48, v48\n\t"
        "v_exp_f32 v49, v49\n\t"
        "v_exp_f32 v50, v50\n\t"
        "s_waitcnt lgkmcnt(0)\n\t"
        "v_smfmac_f32_32x32x32_bf16 v[0:15], %[v1], v[40:47], %[ix]\n\t"
        "v_exp_f32 v51, v51\n\t"
        "v_exp_f32 v52, v52\n\t"
        "v_exp_f32 v53, v53\n\t"
        "v_mfma_f32_32x32x16_bf16 v[32:47], %[kf], %[q0], v[64:79]\n\t"
        "v_exp_f32 v54, v54\n\t"
        "v_exp_f32 v55, v55\n\t"
        "v_exp_f32 v56, v56\n\t"
        "v_exp_f32 v57, v57\n\t"
        "v_exp_f32 v58, v58\n\t"
        "v_smfmac_f32_32x32x32_bf16 v[16:31], %[v0], v[48:55], %[ix]\n\t"
        "ds_read_b128 %[v0], %[a0] offset:33280\n\t"
        "v_exp_f32 v59, v59\n\t"
        "v_exp_f32 v60, v60\n\t"
        "v_exp_f32 v61, v61\n\t"
        "v_exp_f32 v62, v62\n\t"
        "v_exp_f32 v63, v63\n\t"
        "v_exp_f32 v32, v32\n\t"
        "v_exp_f32 v33, v33\n\t"
        "v_exp_f32 v34, v34\n\t"
        "v_smfmac_f32_32x32x32_bf16 v[16:31], %[v1], v[56:63], %[ix]\n\t"
        "ds_read_b32 %[tm], %[ac] offset:16\n\t"
        "ds_read_b128 %[v1], %[a1] offset:33280\n\t"
        "v_exp_f32 v35, v35\n\t"
        "v_exp_f32 v36, v36\n\t"
        "v_exp_f32 v37, v37\n\t"
        "v_mfma_f32_32x32x16_bf16 v[48:63], %[kf], %[q1], v[64:79]\n\t"
        "ds_read_b128 %[kf], %[ak] offset:35360\n\t"
        "v_exp_f32 v38, v38\n\t"
        "v_exp_f32 v39, v39\n\t"
        "v_exp_f32 v40, v40\n\t"
        "v_exp_f32 v41, v41\n\t"
        "v_exp_f32 v42, v42\n\t"
        "s_waitcnt lgkmcnt(2)\n\t"
        "v_smfmac_f32_32x32x32_bf16 v[0:15], %[v0], v[32:39], %[ix]\n\t"
        "v_exp_f32 v43, v43\n\t"
        "v_exp_f32 v44, v44\n\t"
        "v_exp_f32 v45, v45\n\t"
        "v_exp_f32 v46, v46\n\t"
        "v_exp_f32 v47, v47\n\t"
        "v_exp_f32 v48, v48\n\t"
        "v_exp_f32 v49, v49\n\t"
        "v_exp_f32 v50, v50\n\t"
        "s_waitcnt lgkmcnt(0)\n\t"
        "v_smfmac_f32_32x32x32_bf16 v[0:15], %[v1], v[40:47], %[ix]\n\t"
        "v_exp_f32 v51, v51\n\t"
        "v_exp_f32 v52, v52\n\t"
        "v_exp_f32 v53, v53\n\t"
        "v_mfma_f32_32x32x16_bf16 v[32:47], %[kf], %[q0], v[64:79]\n\t"
        "v_exp_f32 v54, v54\n\t"
        "v_exp_f32 v55, v55\n\t"
        "v_exp_f32 v56, v56\n\t"
        "v_exp_f32 v57, v57\n\t"
        "v_exp_f32 v58, v58\n\t"
        "v_smfmac_f32_32x32x32_bf16 v[16:31], %[v0], v[48:55], %[ix]\n\t"
        "ds_read_b128 %[v0], %[a0] offset:35360\n\t"
        "v_exp_f32 v59, v59\n\t"
        "v_exp_f32 v60, v60\n\t"
        "v_exp_f32 v61, v61\n\t"
        "v_exp_f32 v62, v62\n\t"
        "v_exp_f32 v63, v63\n\t"
        "s_mov_b64 exec, 1\n\t"
        "ds_add_u32 %[ac], %[ix]\n\t"
        "s_mov_b64 exec, -1\n\t"
        ".Lsd2_%=:\n\t"
        "v_readfirstlane_b32 %[st], %[tm]\n\t"
        "s_nop 0\n\t"
        "s_cmp_ge_u32 %[st], %[wt]\n\t"
        "s_cbranch_scc0 .Lsl2_%=\n\t"
        "s_add_u32 %[wt], %[wt], 1\n\t"
        "v_exp_f32 v32, v32\n\t"
        "v_exp_f32 v33, v33\n\t"
        "v_exp_f32 v34, v34\n\t"
        "v_smfmac_f32_32x32x32_bf16 v[16:31], %[v1], v[56:63], %[ix]\n\t"
        "ds_read_b128 %[v1], %[a1] offset:35360\n\t"
        "v_exp_f32 v35, v35\n\t"
        "v_exp_f32 v36, v36\n\t"
        "v_exp_f32 v37, v37\n\t"
        "v_mfma_f32_32x32x16_bf16 v[48:63], %[kf], %[q1], v[64:79]\n\t"
        "ds_read_b128 %[kf], %[ak] offset:49920\n\t"
        "v_exp_f32 v38, v38\n\t"
        "v_exp_f32 v39, v39\n\t"
        "v_exp_f32 v40, v40\n\t"
        "v_exp_f32 v41, v41\n\t"
        "v_exp_f32 v42, v42\n\t"
        "s_waitcnt lgkmcnt(2)\n\t"
        "v_smfmac_f32_32x32x32_bf16 v[0:15], %[v0], v[32:39], %[ix]\n\t"
        "v_exp_f32 v43, v43\n\t"
        "v_exp_f32 v44, v44\n\t"
        "v_exp_f32 v45, v45\n\t"
        "v_exp_f32 v46, v46\n\t"
        "v_exp_f32 v47, v47\n\t"
        "v_exp_f32 v48, v48\n\t"
        "v_exp_f32 v49, v49\n\t"
        "v_exp_f32 v50, v50\n\t"
        "s_waitcnt lgkmcnt(0)\n\t"
        "v_smfmac_f32_32x32x32_bf16 v[0:15], %[v1], v[40:47], %[ix]\n\t"
        "v_exp_f32 v51, v51\n\t"
        "v_exp_f32 v52, v52\n\t"
        "v_exp_f32 v53, v53\n\t"
        "v_mfma_f32_32x32x16_bf16 v[32:47], %[kf], %[q0], v[64:79]\n\t"
        "v_exp_f32 v54, v54\n\t"
        "v_exp_f32 v55, v55\n\t"
        "v_exp_f32 v56, v56\n\t"
        "v_exp_f32 v57, v57\n\t"
        "v_exp_f32 v58, v58\n\t"
        "v_smfmac_f32_32x32x32_bf16 v[16:31], %[v0], v[48:55], %[ix]\n\t"
        "ds_read_b128 %[v0], %[a0] offset:49920\n\t"
        "v_exp_f32 v59, v59\n\t"
        "v_exp_f32 v60, v60\n\t"
        "v_exp_f32 v61, v61\n\t"
        "v_exp_f32 v62, v62\n\t"
        "v_exp_f32 v63, v63\n\t"
        "v_exp_f32 v32, v32\n\t"
        "v_exp_f32 v33, v33\n\t"
        "v_exp_f32 v34, v34\n\t"
        "v_smfmac_f32_32x32x32_bf16 v[16:31], %[v1], v[56:63], %[ix]\n\t"
        "ds_read_b32 %[tm], %[ac] offset:16\n\t"
        "ds_read_b128 %[v1], %[a1] offset:49920\n\t"
        "v_exp_f32 v35, v35\n\t"
        "v_exp_f32 v36, v36\n\t"
        "v_exp_f32 v37, v37\n\t"
        "v_mfma_f32_32x32x16_bf16 v[48:63], %[kf], %[q1], v[64:79]\n\t"
        "ds_read_b128 %[kf], %[ak] offset:52000\n\t"
        "v_exp_f32 v38, v38\n\t"
        "v_exp_f32 v39, v39\n\t"
        "v_exp_f32 v40, v40\n\t"
        "v_exp_f32 v41, v41\n\t"
        "v_exp_f32 v42, v42\n\t"
        "s_waitcnt lgkmcnt(2)\n\t"
        "v_smfmac_f32_32x32x32_bf16 v[0:15], %[v0], v[32:39], %[ix]\n\t"
        "v_exp_f32 v43, v43\n\t"
        "v_exp_f32 v44, v44\n\t"
        "v_exp_f32 v45, v45\n\t"
        "v_exp_f32 v46, v46\n\t"
        "v_exp_f32 v47, v47\n\t"
        "v_exp_f32 v48, v48\n\t"
        "v_exp_f32 v49, v49\n\t"
        "v_exp_f32 v50, v50\n\t"
        "s_waitcnt lgkmcnt(0)\n\t"
        "v_smfmac_f32_32x32x32_bf16 v[0:15], %[v1], v[40:47], %[ix]\n\t"
        "v_exp_f32 v51, v51\n\t"
        "v_exp_f32 v52, v52\n\t"
        "v_exp_f32 v53, v53\n\t"
        "v_mfma_f32_32x32x16_bf16 v[32:47], %[kf], %[q0], v[64:79]\n\t"
        "v_exp_f32 v54, v54\n\t"
        "v_exp_f32 v55, v55\n\t"
        "v_exp_f32 v56, v56\n\t"
        "v_exp_f32 v57, v57\n\t"
        "v_exp_f32 v58, v58\n\t"
        "v_smfmac_f32_32x32x32_bf16 v[16:31], %[v0], v[48:55], %[ix]\n\t"
        "ds_read_b128 %[v0], %[a0] offset:52000\n\t"
        "v_exp_f32 v59, v59\n\t"
        "v_exp_f32 v60, v60\n\t"
        "v_exp_f32 v61, v61\n\t"
        "v_exp_f32 v62, v62\n\t"
        "v_exp_f32 v63, v63\n\t"
        "s_mov_b64 exec, 1\n\t"
        "ds_add_u32 %[ac], %[ix]\n\t"
        "s_mov_b64 exec, -1\n\t"
        ".Lsd3_%=:\n\t"
        "v_readfirstlane_b32 %[st], %[tm]\n\t"
        "s_nop 0\n\t"
        "s_cmp_ge_u32 %[st], %[wt]\n\t"
        "s_cbranch_scc0 .Lsl3_%=\n\t"
        "s_add_u32 %[wt], %[wt], 1\n\t"
        "v_exp_f32 v32, v32\n\t"
        "v_exp_f32 v33, v33\n\t"
        "v_exp_f32 v34, v34\n\t"
        "v_smfmac_f32_32x32x32_bf16 v[16:31], %[v1], v[56:63], %[ix]\n\t"
        "ds_read_b128 %[v1], %[a1] offset:52000\n\t"
        "v_exp_f32 v35, v35\n\t"
        "v_exp_f32 v36, v36\n\t"
        "v_exp_f32 v37, v37\n\t"
        "v_mfma_f32_32x32x16_bf16 v[48:63], %[kf], %[q1], v[64:79]\n\t"
        "ds_read_b128 %[kf], %[ak] offset:0\n\t"
        "v_exp_f32 v38, v38\n\t"
        "v_exp_f32 v39, v39\n\t"
        "v_exp_f32 v40, v40\n\t"
        "v_exp_f32 v41, v41\n\t"
        "v_exp_f32 v42, v42\n\t"
        "s_waitcnt lgkmcnt(2)\n\t"
        "v_smfmac_f32_32x32x32_bf16 v[0:15], %[v0], v[32:39], %[ix]\n\t"
        "v_exp_f32 v43, v43\n\t"
        "v_exp_f32 v44, v44\n\t"
        "v_exp_f32 v45, v45\n\t"
        "v_exp_f32 v46, v46\n\t"
        "v_exp_f32 v47, v47\n\t"
        "v_exp_f32 v48, v48\n\t"
        "v_exp_f32 v49, v49\n\t"
        "v_exp_f32 v50, v50\n\t"
        "s_waitcnt lgkmcnt(0)\n\t"
        "v_smfmac_f32_32x32x32_bf16 v[0:15], %[v1], v[40:47], %[ix]\n\t"
        "v_exp_f32 v51, v51\n\t"
        "v_exp_f32 v52, v52\n\t"
        "v_exp_f32 v53, v53\n\t"
        "v_mfma_f32_32x32x16_bf16 v[32:47], %[kf], %[q0], v[64:79]\n\t"
        "v_exp_f32 v54, v54\n\t"
        "v_exp_f32 v55, v55\n\t"
        "v_exp_f32 v56, v56\n\t"
        "v_exp_f32 v57, v57\n\t"
        "v_exp_f32 v58, v58\n\t"
        "v_smfmac_f32_32x32x32_bf16 v[16:31], %[v0], v[48:55], %[ix]\n\t"
        "ds_read_b128 %[v0], %[a0] offset:0\n\t"
        "v_exp_f32 v59, v59\n\t"
        "v_exp_f32 v60, v60\n\t"
        "v_exp_f32 v61, v61\n\t"
        "v_exp_f32 v62, v62\n\t"
        "v_exp_f32 v63, v63\n\t"
        "s_cmp_lg_u32 %[wt], 17\n\t"
        "s_cbranch_scc1 .Lloop_%=\n\t"
        "v_smfmac_f32_32x32x32_bf16 v[16:31], %[v1], v[56:63], %[ix]\n\t"
        "s_branch .Lend_%=\n\t"
        ".Lpr_%=:\n\t"
        "s_setprio 0\n\t"
        "s_branch .Lpb_%=\n\t"
        ".Lsl0_%=:\n\t"
        "s_sleep 2\n\t"
        "ds_read_b32 %[tm], %[ac] offset:16\n\t"
        "s_sub_u32 %[sp], %[sp], 1\n\t"
        "s_cbranch_scc1 .Lend_%=\n\t"
        "s_waitcnt lgkmcnt(0)\n\t"
        "s_branch .Lsd0_%=\n\t"
        ".Lfy_%=:\n\t"
        "s_sleep 1\n\t"
        ".Lfx_%=:\n\t"
        "ds_read_b32 %[tm], %[ac] offset:16\n\t"
        "s_sub_u32 %[sp], %[sp], 1\n\t"
        "s_cbranch_scc1 .Lend_%=\n\t"
        "s_waitcnt lgkmcnt(0)\n\t"
        "v_readfirstlane_b32 %[st], %[tm]\n\t"
        "s_nop 0\n\t"
        "s_cmp_ge_u32 %[st], 1\n\t"
        "s_cbranch_scc0 .Lfy_%=\n\t"
        "ds_read_b128 %[kf], %[ak] offset:16640\n\t"
        "ds_read_b128 %[v0], %[a0] offset:16640\n\t"
        "s_mov_b32 %[wt], 2\n\t"
        "s_waitcnt lgkmcnt(1)\n\t"
        "v_mfma_f32_32x32x16_bf16 v[32:47], %[kf], %[q0], v[64:79]\n\t"
        "s_nop 10\n\t"
        "s_branch .Lfb_%=\n\t"
        ".Lsl1_%=:\n\t"
        "s_sleep 2\n\t"
        "ds_read_b32 %[tm], %[ac] offset:16\n\t"
        "s_sub_u32 %[sp], %[sp], 1\n\t"
        "s_cbranch_scc1 .Lend_%=\n\t"
        "s_waitcnt lgkmcnt(0)\n\t"
        "s_branch .Lsd1_%=\n\t"
        ".Lsl2_%=:\n\t"
        "s_sleep 2\n\t"
        "ds_read_b32 %[tm], %[ac] offset:16\n\t"
        "s_sub_u32 %[sp], %[sp], 1\n\t"
        "s_cbranch_scc1 .Lend_%=\n\t"
        "s_waitcnt lgkmcnt(0)\n\t"
        "s_branch .Lsd2_%=\n\t"
        ".Lsl3_%=:\n\t"
        "s_sleep 2\n\t"
        "ds_read_b32 %[tm], %[ac] offset:16\n\t"
        "s_sub_u32 %[sp], %[sp], 1\n\t"
        "s_cbranch_scc1 .Lend_%=\n\t"
        "s_waitcnt lgkmcnt(0)\n\t"
        "s_branch .Lsd3_%=\n\t"
        ".Lend_%=:\n\t"
        "s_mov_b64 exec, -1\n\t"
        "s_waitcnt lgkmcnt(0)\n\t"
        "s_nop 11\n\t"
            : "=&{v[0:15]}"(o), "=&{v[16:31]}"(o1), [kf] "=&v"(kf), [v0] "=&v"(v0), [v1] "=&v"(v1),
              [tm] "+v"(tm), [st] "=&s"(st), [wt] "=&s"(wt), [sp] "=&s"(sp)
            : [q0] "v"(qpk), [q1] "v"(q1), [ak] "v"(a_k), [a0] "v"(a_v0), [a1] "v"(a_v1), [ac] "v"(lds0 + L_CNT + w * 32), [ix] "v"(0xDDDD)
            : "memory", "scc", "v32", "v33", "v34", "v35", "v36", "v37", "v38", "v39", "v40", "v41", "v42", "v43", "v44", "v45", "v46", "v47", "v48", "v49", "v50", "v51", "v52", "v53", "v54", "v55", "v56", "v57", "v58", "v59", "v60", "v61", "v62", "v63", "v64", "v65", "v66", "v67", "v68", "v69", "v70", "v71", "v72", "v73", "v74", "v75", "v76", "v77", "v78", "v79");
    }
    const int ln = lane_id();
    float gden;
    {
        f32x4 t0 = {o1[0], o1[1], o1[2], o1[3]}, t1 = {o1[4], o1[5], o1[6], o1[7]};
        *(LAS f32x4*)(L + L_XO + w * 2048 + ln * 16) = t0;
        *(LAS f32x4*)(L + L_XO + w * 2048 + 1024 + ln * 16) = t1;
        *(LAS float*)(L + L_XL + w * 256 + ln * 4) = o1[8];
        asm volatile("" ::: "memory");
        volatile LAS unsigned* pf = (volatile LAS unsigned*)(L + L_CNT + 8);
        if (ln == 0) pf[w * 8] = 1u;
        lds_wait_ge<1>((volatile LAS unsigned*)(L + L_CNT) + C_GATE, 2u);
        gden = 1.f + __builtin_amdgcn_exp2f(*(const LAS float*)(L + L_GX + tt * 128 + (ln & 31) * 4) * -LOG2E);
        asm volatile("" : "+v"(gden));
        lds_wait_ge<1>(pf + (w ^ 2) * 8, 1u);
        const ldsp pp = L + L_XO + (w ^ 2) * 2048 + ln * 16;
        t0 = *(const LAS f32x4*)pp; t1 = *(const LAS f32x4*)(pp + 1024);
#pragma unroll
        for (int i = 0; i < 4; ++i) { o[i] = add1(o[i], t0[i]); o[4 + i] = add1(o[4 + i], t1[i]); }
        o[8] += *(const LAS float*)(L + L_XL + (w ^ 2) * 256 + ln * 4);
    }

    {
        bool bad = !(o[8] < 1.0e18f) || !(o[8] > 1.0e-18f);
        unsigned mx = 0;
#pragma unroll
        for (int i = 0; i < 8; ++i) { const unsigned u = __float_as_uint(o[i]) & 0x7fffffffu; mx = u > mx ? u : mx; }
        bad |= mx >= 0x7f800000u;
        if (__builtin_expect(__any(bad), 0)) {
            const int gb = WS_KV + head * 64 * 2048, lane = ln, r = ln & 31, hf = ln >> 5;
            const u32x4 one = {0x3F803F80u, 0x3F803F80u, 0x3F803F80u, 0x3F803F80u};
            lds_wait_all<2>((volatile LAS unsigned*)(L + L_CNT) + (ln & 7) * 8, 0xDDDD);
            float m = -3.0e38f;
#pragma unroll 1
            for (int t = 0; t < 64; ++t) {
                const u32x4 kk = __builtin_amdgcn_raw_buffer_load_b128(rs, gb + lane * 16, t * 2048, AUX_SC1);
                m = __builtin_fmaxf(m, rowmax16(MFMA(__builtin_bit_cast(bf16x8, kk), qf, z16)));
            }
            const f32x16 cm = splat16(-xhalf_max(m));
            o = z16;
#pragma unroll 1
            for (int t = 0; t < 64; ++t) {
                const u32x4 kk = __builtin_amdgcn_raw_buffer_load_b128(rs, gb + lane * 16, t * 2048, AUX_SC1);
                u32x4 a0 = __builtin_amdgcn_raw_buffer_load_b128(rs, gb + 1024 + hf * 512 + (r & 15) * 16, t * 2048, AUX_SC1);
                u32x4 a1 = __builtin_amdgcn_raw_buffer_load_b128(rs, gb + 1280 + hf * 512 + (r & 15) * 16, t * 2048, AUX_SC1);
                if (r >= 16) { a0 = one; a1 = one; }
                f32x16 s = MFMA(__builtin_bit_cast(bf16x8, kk), qf, cm);
#pragma unroll
                for (int i = 0; i < 16; ++i) s[i] = __builtin_amdgcn_exp2f(s[i]);
                o = smf(__builtin_bit_cast(bf16x8, a0), lo8(s), o); o = smf(__builtin_bit_cast(bf16x8, a1), hi8(s), o);
            }
        }
    }
    const u32x4 opk = not_sentinel(pack8<0>(o, __builtin_amdgcn_rcpf(gden * o[8])));
    __builtin_amdgcn_raw_buffer_store_b128(opk, rs, WS_Z + (rb * 4 + tt) * ZT + (112 + head) * 1024 + ln * 16, 0, AUX_SC1);
    asm volatile("" ::: "memory");
    if (ln == 0) *(volatile LAS unsigned*)(L + L_CNT + 8 + w * 32) = 2u;

}

__device__ __forceinline__ void role_kv(Stage& sg, char* ws, ldsp L, unsigned lds0) {
    const int a = blockIdx.x, hp = sg.hp, rb = sg.rb, lane = lane_id(), w = sg.w16 & 7, cb = (hp + 8) & 15;
    const __amdgpu_buffer_rsrc_t rs = __builtin_amdgcn_make_buffer_rsrc(ws, 0, WS_END, 0x00020000);
    volatile LAS unsigned* cw = (volatile LAS unsigned*)(L + L_CNT);
    unsigned sv, st, epoch, l16 = lane * 16;
    u32x4 pz = {0xFFFFFFFFu, 0xFFFFFFFFu, 0xFFFFFFFFu, 0xFFFFFFFFu};
    asm volatile("" : "+v"(pz));
    const int pkv = WS_KV + ((2 * hp + (w >> 2)) * 64 + rb * 4 + (w & 3)) * 2048, po = WS_Z + (rb * 4 + (w & 3)) * ZT + (112 + 2 * hp + (w >> 2)) * 1024;
    const int ako = w == 4 ? WS_ACK + a * FS : 0;
    asm volatile(
        "s_mov_b32 %[sv], m0\n\t"
        "s_waitcnt vmcnt(3) lgkmcnt(0)\n\t"
        "s_barrier\n\t"
        "s_add_u32 m0, %[wd], 24576\n\t"
        "s_mul_i32 %[st], %[ku], 114688\n\t"
        "s_add_u32 %[st], %[st], %[w0]\n\t"
        "buffer_load_dwordx4 %[wv], %[rw], %[st] offen lds\n\t"
        "s_add_u32 %[ku], %[ku], 1\n\t"
        "s_and_b32 %[ku], %[ku], 15\n\t"
        "s_waitcnt vmcnt(4) lgkmcnt(0)\n\t"
        "s_barrier\n\t"
        "s_add_u32 m0, %[wd], 32768\n\t"
        "s_mul_i32 %[st], %[ku], 114688\n\t"
        "s_add_u32 %[st], %[st], %[w0]\n\t"
        "buffer_load_dwordx4 %[wv], %[rw], %[st] offen lds\n\t"
        "s_add_u32 %[ku], %[ku], 1\n\t"
        "s_and_b32 %[ku], %[ku], 15\n\t"
        "s_add_u32 m0, %[xd], 33280\n\t"
        "s_add_u32 %[st], %[xs], %[kx]\n\t"
        "buffer_load_dwordx4 %[xv], %[rx], %[st] offen lds\n\t"
        "s_add_u32 %[kx], %[kx], 128\n\t"
        "s_and_b32 %[kx], %[kx], 0x3ff\n\t"
        "s_waitcnt vmcnt(4) lgkmcnt(0)\n\t"
        "s_barrier\n\t"
        "s_add_u32 m0, %[wd], 40960\n\t"
        "s_mul_i32 %[st], %[ku], 114688\n\t"
        "s_add_u32 %[st], %[st], %[w0]\n\t"
        "buffer_load_dwordx4 %[wv], %[rw], %[st] offen lds\n\t"
        "s_add_u32 %[ku], %[ku], 1\n\t"
        "s_and_b32 %[ku], %[ku], 15\n\t"
        "s_waitcnt vmcnt(4) lgkmcnt(0)\n\t"
        "s_barrier\n\t"
        "s_add_u32 m0, %[wd], 0\n\t"
        "s_mul_i32 %[st], %[ku], 114688\n\t"
        "s_add_u32 %[st], %[st], %[w0]\n\t"
        "buffer_load_dwordx4 %[wv], %[rw], %[st] offen lds\n\t"
        "s_add_u32 %[ku], %[ku], 1\n\t"
        "s_and_b32 %[ku], %[ku], 15\n\t"
        "buffer_load_dword %[ep], off, %[re], 0 sc1\n\t"
        "s_add_u32 m0, %[xd], 49920\n\t"
        "s_add_u32 %[st], %[xs], %[kx]\n\t"
        "buffer_load_dwordx4 %[xv], %[rx], %[st] offen lds\n\t"
        "s_add_u32 %[kx], %[kx], 128\n\t"
        "s_and_b32 %[kx], %[kx], 0x3ff\n\t"
        "s_waitcnt vmcnt(4) lgkmcnt(0)\n\t"
        "s_barrier\n\t"
        "s_add_u32 m0, %[wd], 8192\n\t"
        "s_mul_i32 %[st], %[ku], 114688\n\t"
        "s_add_u32 %[st], %[st], %[w0]\n\t"
        "buffer_load_dwordx4 %[wv], %[rw], %[st] offen lds\n\t"
        "s_add_u32 %[ku], %[ku], 1\n\t"
        "s_and_b32 %[ku], %[ku], 15\n\t"
        "s_waitcnt vmcnt(5) lgkmcnt(0)\n\t"
        "s_barrier\n\t"
        "s_add_u32 m0, %[wd], 16384\n\t"
        "s_mul_i32 %[st], %[ku], 114688\n\t"
        "s_add_u32 %[st], %[st], %[w0]\n\t"
        "buffer_load_dwordx4 %[wv], %[rw], %[st] offen lds\n\t"
        "s_add_u32 %[ku], %[ku], 1\n\t"
        "s_and_b32 %[ku], %[ku], 15\n\t"
        "s_add_u32 m0, %[xd], 0\n\t"
        "s_add_u32 %[st], %[xs], %[kx]\n\t"
        "buffer_load_dwordx4 %[xv], %[rx], %[st] offen lds\n\t"
        "s_add_u32 %[kx], %[kx], 128\n\t"
        "s_and_b32 %[kx], %[kx], 0x3ff\n\t"
        "s_waitcnt vmcnt(3) lgkmcnt(0)\n\t"
        "s_barrier\n\t"
        "s_add_u32 m0, %[wd], 24576\n\t"
        "s_mul_i32 %[st], %[ku], 114688\n\t"
        "s_add_u32 %[st], %[st], %[w0]\n\t"
        "buffer_load_dwordx4 %[wv], %[rw], %[st] offen lds\n\t"
        "s_add_u32 %[ku], %[ku], 1\n\t"
        "s_and_b32 %[ku], %[ku], 15\n\t"
        "s_waitcnt vmcnt(4) lgkmcnt(0)\n\t"
        "s_barrier\n\t"
        "s_add_u32 m0, %[wd], 32768\n\t"
        "s_mul_i32 %[st], %[ku], 114688\n\t"
        "s_add_u32 %[st], %[st], %[w0]\n\t"
        "buffer_load_dwordx4 %[wv], %[rw], %[st] offen lds\n\t"
        "s_add_u32 %[ku], %[ku], 1\n\t"
        "s_and_b32 %[ku], %[ku], 15\n\t"
        "s_add_u32 m0, %[xd], 16640\n\t"
        "s_add_u32 %[st], %[xs], %[kx]\n\t"
        "buffer_load_dwordx4 %[xv], %[rx], %[st] offen lds\n\t"
        "s_add_u32 %[kx], %[kx], 128\n\t"
        "s_and_b32 %[kx], %[kx], 0x3ff\n\t"
        "s_waitcnt vmcnt(3) lgkmcnt(0)\n\t"
        "s_barrier\n\t"
        "s_add_u32 m0, %[wd], 40960\n\t"
        "s_mul_i32 %[st], %[ku], 114688\n\t"
        "s_add_u32 %[st], %[st], %[w0]\n\t"
        "buffer_load_dwordx4 %[wv], %[rw], %[st] offen lds\n\t"
        "s_add_u32 %[ku], %[ku], 1\n\t"
        "s_and_b32 %[ku], %[ku], 15\n\t"
        "s_waitcnt vmcnt(4) lgkmcnt(0)\n\t"
        "s_barrier\n\t"
        "s_add_u32 m0, %[wd], 0\n\t"
        "s_mul_i32 %[st], %[ku], 114688\n\t"
        "s_add_u32 %[st], %[st], %[w0]\n\t"
        "buffer_load_dwordx4 %[wv], %[rw], %[st] offen lds\n\t"
        "s_add_u32 %[ku], %[ku], 1\n\t"
        "s_and_b32 %[ku], %[ku], 15\n\t"
        "s_add_u32 m0, %[xd], 33280\n\t"
        "s_add_u32 %[st], %[xs], %[kx]\n\t"
        "buffer_load_dwordx4 %[xv], %[rx], %[st] offen lds\n\t"
        "s_add_u32 %[kx], %[kx], 128\n\t"
        "s_and_b32 %[kx], %[kx], 0x3ff\n\t"
        "s_waitcnt vmcnt(3) lgkmcnt(0)\n\t"
        "s_barrier\n\t"
        "buffer_store_dwordx4 %[pz], %[l16], %[re], %[pkv] offen sc1\n\t"
        "s_add_u32 m0, %[wd], 8192\n\t"
        "s_mul_i32 %[st], %[ku], 114688\n\t"
        "s_add_u32 %[st], %[st], %[w0]\n\t"
        "buffer_load_dwordx4 %[wv], %[rw], %[st] offen lds\n\t"
        "s_add_u32 %[ku], %[ku], 1\n\t"
        "s_and_b32 %[ku], %[ku], 15\n\t"
        "s_waitcnt vmcnt(5) lgkmcnt(0)\n\t"
        "s_barrier\n\t"
        "buffer_store_dwordx4 %[pz], %[l16], %[re], %[pkv] offen offset:1024 sc1\n\t"
        "s_add_u32 m0, %[wd], 16384\n\t"
        "s_mul_i32 %[st], %[ku], 114688\n\t"
        "s_add_u32 %[st], %[st], %[w0]\n\t"
        "buffer_load_dwordx4 %[wv], %[rw], %[st] offen lds\n\t"
        "s_add_u32 %[ku], %[ku], 1\n\t"
        "s_and_b32 %[ku], %[ku], 15\n\t"
        "s_add_u32 m0, %[xd], 49920\n\t"
        "s_add_u32 %[st], %[xs], %[kx]\n\t"
        "buffer_load_dwordx4 %[xv], %[rx], %[st] offen lds\n\t"
        "s_add_u32 %[kx], %[kx], 128\n\t"
        "s_and_b32 %[kx], %[kx], 0x3ff\n\t"
        "s_waitcnt vmcnt(5) lgkmcnt(0)\n\t"
        "s_barrier\n\t"
        "buffer_store_dwordx4 %[pz], %[l16], %[re], %[po] offen sc1\n\t"
        "s_add_u32 m0, %[wd], 24576\n\t"
        "s_mul_i32 %[st], %[ku], 114688\n\t"
        "s_add_u32 %[st], %[st], %[w0]\n\t"
        "buffer_load_dwordx4 %[wv], %[rw], %[st] offen lds\n\t"
        "s_add_u32 %[ku], %[ku], 1\n\t"
        "s_and_b32 %[ku], %[ku], 15\n\t"
        "s_waitcnt vmcnt(7) lgkmcnt(0)\n\t"
        "s_barrier\n\t"
        "s_waitcnt vmcnt(2) lgkmcnt(0)\n\t"
        "s_barrier\n\t"
        "s_waitcnt vmcnt(2) lgkmcnt(0)\n\t"
        "s_barrier\n\t"
        "s_waitcnt vmcnt(0) lgkmcnt(0)\n\t"
        "s_barrier\n\t"
        "s_cmp_eq_u32 %[ako], 0\n\t"
        "s_cbranch_scc1 .Lna_%=\n\t"
        "v_add_u32 %[l16], 1, %[ep]\n\t"
        "s_mov_b64 exec, 1\n\t"
        "buffer_store_dword %[l16], off, %[re], %[ako] sc1\n\t"
        "s_mov_b64 exec, -1\n\t"
        ".Lna_%=:\n\t"
        "s_mov_b32 m0, %[sv]\n\t"
        "s_nop 10\n\t"
        : [sv] "=&s"(sv), [st] "=&s"(st), [ep] "=&v"(epoch), [kx] "+s"(sg.kx), [ku] "+s"(sg.ku), [l16] "+v"(l16)
        : [pz] "v"(pz), [pkv] "s"(pkv), [po] "s"(po), [ako] "s"(ako), [xv] "v"(sg.xv), [wv] "v"(sg.wv), [rx] "s"(sg.rx), [rw] "s"(sg.rw), [xs] "s"(sg.xs),
          [xd] "s"(sg.xd), [wd] "s"(sg.wd), [w0] "s"(sg.w0), [re] "s"(mk_rsrc(ws, WS_END))
        : "memory", "scc");
    const unsigned tag = __builtin_amdgcn_readfirstlane(epoch) + 1u;
    __builtin_amdgcn_s_setprio(3);
    const int hs = (w >> 1) & 1, kh = w & 1;
    const int src = WS_KV + ((2 * hp + hs) * 64 + 2 * kh) * 2048 + lane * 16, fl = WS_ACK + blk_of(hp, (lane >> 2) ^ rb) * FS;
    const ldsp dst = L + (hs * 4 + 2 * kh) * TILEB + lane * 16;
    volatile LAS unsigned* cons = cw + (4 * hs + 2 * (kh ^ hs) + (lane & 1)) * 8;
    u32x4 buf[4][4];
    auto fetch = [&](int c) {
#pragma unroll
        for (int i = 0; i < 4; ++i) buf[c & 3][i] = __builtin_amdgcn_raw_buffer_load_b128(rs, src + i * 1024, (rb ^ c) * 8192, AUX_SC1);
    };
    lds_wait_ge<2>(cw + C_PROJ, 8u);
    wait_flags<2>(rs, lane < 32 ? fl : WS_ACK + a * FS, tag);

    fetch(1);
    __builtin_amdgcn_s_sleep(2);
    fetch(2);
    __builtin_amdgcn_s_sleep(2);
    fetch(3); fetch(4);
    if (lane == 0) __hip_atomic_fetch_add((LAS unsigned*)(cw + C_REQ), 1u, __ATOMIC_RELAXED, __HIP_MEMORY_SCOPE_WORKGROUP);
    unsigned chk = 0xF;
    bool done = false;
#pragma unroll
    for (int c = 1; c < 16; ++c) {
        auto open = [&]() {
            unsigned m = 0;
#pragma unroll
            for (int i = 0; i < 16; ++i) { const unsigned u = buf[c & 3][i >> 2][i & 3]; m = u > m ? u : m; }
            return __any(m == 0xFFFFFFFFu);
        };
        if (((chk >> (c & 3)) & 1) && __builtin_expect(open(), 0)) {
#pragma unroll 1
            for (unsigned spins = 0; spins < SPIN_CAP; ++spins) {
                asm volatile("" ::: "memory");
                fetch(c);
                if (!open()) break;
            }
        }
        if (c == 3) {
            if (cb < 14) lds_wait_ge<2>(cw + C_STAGE, 4u);
            if (lane < 16) *(LAS unsigned*)(L + 3 * SLOTB + (hs * 4 + 2 * kh + (lane >> 3)) * TILEB + 2048 + (lane & 7) * 4) = (lane & 4) ? 0u : 0x3F803F80u;
        }
        if (c >= 4) lds_wait_all<12>(cons, (c - 2) * 0xDDDD);
#pragma unroll
        for (int i = 0; i < 4; ++i) *(LAS u32x4*)(dst + (c & 3) * SLOTB + (i >> 1) * TILEB + (i & 1) * 1024) = buf[c & 3][i];
        asm volatile("" ::: "memory");
        if (lane < 2) cons[4] = c == 15 ? 16u : (unsigned)c;
        if (c == 2 && w == 4) {
            lds_wait_all<2>(cw + (lane & 7) * 8, 0xDDDD);
            if (lane == 0) st_flag(rs, WS_ACK + a * FS + 64, tag);
        }
        if (c + 4 < 16) {
            if (c >= 4 && !done) {
                done = __all(ld_flag(rs, fl + 64) == tag); __builtin_amdgcn_fence(__ATOMIC_ACQUIRE, "wavefront");
                if (c == 4 && !done) wait_flags<2>(rs, fl, tag);
            }
            if (done) chk &= ~(1u << (c & 3));
            fetch(c + 4);
        }
    }

}
__device__ __forceinline__ float get_gate(char* ws, ldsp L, int rb, int w, int lane, unsigned tag) {
    unsigned long long x = 0;
#pragma unroll 1
    for (unsigned spins = 0; spins < SPIN_CAP; ++spins) {
        x = __hip_atomic_load((GAS unsigned long long*)(ws + WS_G) + rb * 128 + (w & 1) * 64 + lane, __ATOMIC_RELAXED, __HIP_MEMORY_SCOPE_AGENT);
        if (__all((unsigned)(x >> 32) == tag)) break;
        __builtin_amdgcn_s_sleep(4);
    }
    if (w < 2) {
        *(LAS unsigned*)(L + L_GX + w * 256 + lane * 4) = (unsigned)x;
        asm volatile("" ::: "memory");
        if (lane == 0) __hip_atomic_fetch_add((LAS unsigned*)(L + L_CNT) + C_GATE, 1u, __ATOMIC_RELAXED, __HIP_MEMORY_SCOPE_WORKGROUP);
    }
    return __uint_as_float((unsigned)x);
}
__device__ __forceinline__ void role_f(Stage& sg, const void* wo, const void* w2, void* out, char* ws, ldsp L, unsigned lds0) {
    const int a = blockIdx.x, hp = sg.hp, rb = sg.rb, lane = lane_id(), w = sg.w16 & 7, cb = (hp + 8) & 15;
    const __amdgpu_buffer_rsrc_t rs = __builtin_amdgcn_make_buffer_rsrc(ws, 0, WS_END, 0x00020000);
    volatile LAS unsigned* cw = (volatile LAS unsigned*)(L + L_CNT);
    unsigned sv, st, epoch, l16 = lane * 16;
    u32x4 pz = {0xFFFFFFFFu, 0xFFFFFFFFu, 0xFFFFFFFFu, 0xFFFFFFFFu};
    asm volatile("" : "+v"(pz));
    const int pkv = WS_KV + ((2 * hp + (w >> 2)) * 64 + rb * 4 + (w & 3)) * 2048, po = WS_Z + (rb * 4 + (w & 3)) * ZT + (112 + 2 * hp + (w >> 2)) * 1024;
    const int ako = w == 4 ? WS_ACK + a * FS : 0;
    const int g8 = a & 7, idx = a >> 3, r = lane & 31, hf = lane >> 5, q = (lane >> 2) & 3, p = lane & 3, ch = (lane >> 4) & 1;
    unsigned tag;
    if (cb < 14) {
        const int tp = w & 1, sel = w >> 1;
        f32x16 h0, h1, h2, h3;
        asm volatile(
        "s_mov_b32 %[sv], m0\n\t"
        "s_waitcnt vmcnt(3)\n\t"
        "s_barrier\n\t"
        "s_add_u32 m0, %[wd], 24576\n\t"
        "s_mul_i32 %[st], %[ku], 114688\n\t"
        "s_add_u32 %[st], %[st], %[w0]\n\t"
        "buffer_load_dwordx4 %[wv], %[rw], %[st] offen lds\n\t"
        "s_add_u32 %[ku], %[ku], 1\n\t"
        "s_and_b32 %[ku], %[ku], 15\n\t"
        "s_waitcnt vmcnt(4)\n\t"
        "s_barrier\n\t"
        "s_add_u32 m0, %[wd], 32768\n\t"
        "s_mul_i32 %[st], %[ku], 114688\n\t"
        "s_add_u32 %[st], %[st], %[w0]\n\t"
        "buffer_load_dwordx4 %[wv], %[rw], %[st] offen lds\n\t"
        "s_add_u32 %[ku], %[ku], 1\n\t"
        "s_and_b32 %[ku], %[ku], 15\n\t"
        "ds_read_b128 v[64:67], %[xb] offset:0\n\t"
        "ds_read_b64_tr_b16 v[72:73], %[wb] offset:0\n\t"
        "ds_read_b64_tr_b16 v[74:75], %[wb] offset:1024\n\t"
        "ds_read_b128 v[68:71], %[xb] offset:256\n\t"
        "ds_read_b64_tr_b16 v[76:77], %[wb] offset:256\n\t"
        "ds_read_b64_tr_b16 v[78:79], %[wb] offset:1280\n\t"
        "s_add_u32 m0, %[xd], 33280\n\t"
        "s_add_u32 %[st], %[xs], %[kx]\n\t"
        "buffer_load_dwordx4 %[xv], %[rx], %[st] offen lds\n\t"
        "s_add_u32 %[kx], %[kx], 128\n\t"
        "s_and_b32 %[kx], %[kx], 0x3ff\n\t"
        "s_waitcnt vmcnt(4)\n\t"
        "s_barrier\n\t"
        "s_waitcnt lgkmcnt(0)\n\t"
        "v_mfma_f32_32x32x16_bf16 v[0:15], v[72:75], v[64:67], 0\n\t"
        "ds_read_b128 v[80:83], %[xb] offset:32\n\t"
        "ds_read_b64_tr_b16 v[88:89], %[wb] offset:4096\n\t"
        "ds_read_b64_tr_b16 v[90:91], %[wb] offset:5120\n\t"
        "v_mfma_f32_32x32x16_bf16 v[16:31], v[76:79], v[64:67], 0\n\t"
        "ds_read_b128 v[84:87], %[xb] offset:288\n\t"
        "ds_read_b64_tr_b16 v[92:93], %[wb] offset:4352\n\t"
        "ds_read_b64_tr_b16 v[94:95], %[wb] offset:5376\n\t"
        "v_mfma_f32_32x32x16_bf16 v[32:47], v[72:75], v[68:71], 0\n\t"
        "s_add_u32 m0, %[wd], 40960\n\t"
        "s_mul_i32 %[st], %[ku], 114688\n\t"
        "s_add_u32 %[st], %[st], %[w0]\n\t"
        "buffer_load_dwordx4 %[wv], %[rw], %[st] offen lds\n\t"
        "s_add_u32 %[ku], %[ku], 1\n\t"
        "s_and_b32 %[ku], %[ku], 15\n\t"
        "v_mfma_f32_32x32x16_bf16 v[48:63], v[76:79], v[68:71], 0\n\t"
        "s_waitcnt lgkmcnt(0)\n\t"
        "v_mfma_f32_32x32x16_bf16 v[0:15], v[88:91], v[80:83], v[0:15]\n\t"
        "ds_read_b128 v[64:67], %[xb] offset:64\n\t"
        "ds_read_b64_tr_b16 v[72:73], %[wb] offset:8192\n\t"
        "ds_read_b64_tr_b16 v[74:75], %[wb] offset:9216\n\t"
        "v_mfma_f32_32x32x16_bf16 v[16:31], v[92:95], v[80:83], v[16:31]\n\t"
        "ds_read_b128 v[68:71], %[xb] offset:320\n\t"
        "ds_read_b64_tr_b16 v[76:77], %[wb] offset:8448\n\t"
        "ds_read_b64_tr_b16 v[78:79], %[wb] offset:9472\n\t"
        "v_mfma_f32_32x32x16_bf16 v[32:47], v[88:91], v[84:87], v[32:47]\n\t"
        "v_mfma_f32_32x32x16_bf16 v[48:63], v[92:95], v[84:87], v[48:63]\n\t"
        "s_waitcnt vmcnt(4)\n\t"
        "s_barrier\n\t"
        "s_waitcnt lgkmcnt(0)\n\t"
        "v_mfma_f32_32x32x16_bf16 v[0:15], v[72:75], v[64:67], v[0:15]\n\t"
        "ds_read_b128 v[80:83], %[xb] offset:96\n\t"
        "ds_read_b64_tr_b16 v[88:89], %[wb] offset:12288\n\t"
        "ds_read_b64_tr_b16 v[90:91], %[wb] offset:13312\n\t"
        "v_mfma_f32_32x32x16_bf16 v[16:31], v[76:79], v[64:67], v[16:31]\n\t"
        "ds_read_b128 v[84:87], %[xb] offset:352\n\t"
        "ds_read_b64_tr_b16 v[92:93], %[wb] offset:12544\n\t"
        "ds_read_b64_tr_b16 v[94:95], %[wb] offset:13568\n\t"
        "v_mfma_f32_32x32x16_bf16 v[32:47], v[72:75], v[68:71], v[32:47]\n\t"
        "s_add_u32 m0, %[wd], 0\n\t"
        "s_mul_i32 %[st], %[ku], 114688\n\t"
        "s_add_u32 %[st], %[st], %[w0]\n\t"
        "buffer_load_dwordx4 %[wv], %[rw], %[st] offen lds\n\t"
        "s_add_u32 %[ku], %[ku], 1\n\t"
        "s_and_b32 %[ku], %[ku], 15\n\t"
        "buffer_load_dword %[ep], off, %[re], 0 sc1\n\t"
        "v_mfma_f32_32x32x16_bf16 v[48:63], v[76:79], v[68:71], v[48:63]\n\t"
        "s_waitcnt lgkmcnt(0)\n\t"
        "v_mfma_f32_32x32x16_bf16 v[0:15], v[88:91], v[80:83], v[0:15]\n\t"
        "ds_read_b128 v[64:67], %[xb] offset:16640\n\t"
        "ds_read_b64_tr_b16 v[72:73], %[wb] offset:16384\n\t"
        "ds_read_b64_tr_b16 v[74:75], %[wb] offset:17408\n\t"
        "v_mfma_f32_32x32x16_bf16 v[16:31], v[92:95], v[80:83], v[16:31]\n\t"
        "ds_read_b128 v[68:71], %[xb] offset:16896\n\t"
        "ds_read_b64_tr_b16 v[76:77], %[wb] offset:16640\n\t"
        "ds_read_b64_tr_b16 v[78:79], %[wb] offset:17664\n\t"
        "v_mfma_f32_32x32x16_bf16 v[32:47], v[88:91], v[84:87], v[32:47]\n\t"
        "s_add_u32 m0, %[xd], 49920\n\t"
        "s_add_u32 %[st], %[xs], %[kx]\n\t"
        "buffer_load_dwordx4 %[xv], %[rx], %[st] offen lds\n\t"
        "s_add_u32 %[kx], %[kx], 128\n\t"
        "s_and_b32 %[kx], %[kx], 0x3ff\n\t"
        "v_mfma_f32_32x32x16_bf16 v[48:63], v[92:95], v[84:87], v[48:63]\n\t"
        "s_waitcnt vmcnt(4)\n\t"
        "s_barrier\n\t"
        "s_waitcnt lgkmcnt(0)\n\t"
        "v_mfma_f32_32x32x16_bf16 v[0:15], v[72:75], v[64:67], v[0:15]\n\t"
        "ds_read_b128 v[80:83], %[xb] offset:16672\n\t"
        "ds_read_b64_tr_b16 v[88:89], %[wb] offset:20480\n\t"
        "ds_read_b64_tr_b16 v[90:91], %[wb] offset:21504\n\t"
        "v_mfma_f32_32x32x16_bf16 v[16:31], v[76:79], v[64:67], v[16:31]\n\t"
        "ds_read_b128 v[84:87], %[xb] offset:16928\n\t"
        "ds_read_b64_tr_b16 v[92:93], %[wb] offset:20736\n\t"
        "ds_read_b64_tr_b16 v[94:95], %[wb] offset:21760\n\t"
        "v_mfma_f32_32x32x16_bf16 v[32:47], v[72:75], v[68:71], v[32:47]\n\t"
        "s_add_u32 m0, %[wd], 8192\n\t"
        "s_mul_i32 %[st], %[ku], 114688\n\t"
        "s_add_u32 %[st], %[st], %[w0]\n\t"
        "buffer_load_dwordx4 %[wv], %[rw], %[st] offen lds\n\t"
        "s_add_u32 %[ku], %[ku], 1\n\t"
        "s_and_b32 %[ku], %[ku], 15\n\t"
        "v_mfma_f32_32x32x16_bf16 v[48:63], v[76:79], v[68:71], v[48:63]\n\t"
        "s_waitcnt lgkmcnt(0)\n\t"
        "v_mfma_f32_32x32x16_bf16 v[0:15], v[88:91], v[80:83], v[0:15]\n\t"
        "ds_read_b128 v[64:67], %[xb] offset:16704\n\t"
        "ds_read_b64_tr_b16 v[72:73], %[wb] offset:24576\n\t"
        "ds_read_b64_tr_b16 v[74:75], %[wb] offset:25600\n\t"
        "v_mfma_f32_32x32x16_bf16 v[16:31], v[92:95], v[80:83], v[16:31]\n\t"
        "ds_read_b128 v[68:71], %[xb] offset:16960\n\t"
        "ds_read_b64_tr_b16 v[76:77], %[wb] offset:24832\n\t"
        "ds_read_b64_tr_b16 v[78:79], %[wb] offset:25856\n\t"
        "v_mfma_f32_32x32x16_bf16 v[32:47], v[88:91], v[84:87], v[32:47]\n\t"
        "v_mfma_f32_32x32x16_bf16 v[48:63], v[92:95], v[84:87], v[48:63]\n\t"
        "s_waitcnt vmcnt(5)\n\t"
        "s_barrier\n\t"
        "s_waitcnt lgkmcnt(0)\n\t"
        "v_mfma_f32_32x32x16_bf16 v[0:15], v[72:75], v[64:67], v[0:15]\n\t"
        "ds_read_b128 v[80:83], %[xb] offset:16736\n\t"
        "ds_read_b64_tr_b16 v[88:89], %[wb] offset:28672\n\t"
        "ds_read_b64_tr_b16 v[90:91], %[wb] offset:29696\n\t"
        "v_mfma_f32_32x32x16_bf16 v[16:31], v[76:79], v[64:67], v[16:31]\n\t"
        "ds_read_b128 v[84:87], %[xb] offset:16992\n\t"
        "ds_read_b64_tr_b16 v[92:93], %[wb] offset:28928\n\t"
        "ds_read_b64_tr_b16 v[94:95], %[wb] offset:29952\n\t"
        "v_mfma_f32_32x32x16_bf16 v[32:47], v[72:75], v[68:71], v[32:47]\n\t"
        "s_add_u32 m0, %[wd], 16384\n\t"
        "s_mul_i32 %[st], %[ku], 114688\n\t"
        "s_add_u32 %[st], %[st], %[w0]\n\t"
        "buffer_load_dwordx4 %[wv], %[rw], %[st] offen lds\n\t"
        "s_add_u32 %[ku], %[ku], 1\n\t"
        "s_and_b32 %[ku], %[ku], 15\n\t"
        "v_mfma_f32_32x32x16_bf16 v[48:63], v[76:79], v[68:71], v[48:63]\n\t"
        "s_waitcnt lgkmcnt(0)\n\t"
        "v_mfma_f32_32x32x16_bf16 v[0:15], v[88:91], v[80:83], v[0:15]\n\t"
        "ds_read_b128 v[64:67], %[xb] offset:33280\n\t"
        "ds_read_b64_tr_b16 v[72:73], %[wb] offset:32768\n\t"
        "ds_read_b64_tr_b16 v[74:75], %[wb] offset:33792\n\t"
        "v_mfma_f32_32x32x16_bf16 v[16:31], v[92:95], v[80:83], v[16:31]\n\t"
        "ds_read_b128 v[68:71], %[xb] offset:33536\n\t"
        "ds_read_b64_tr_b16 v[76:77], %[wb] offset:33024\n\t"
        "ds_read_b64_tr_b16 v[78:79], %[wb] offset:34048\n\t"
        "v_mfma_f32_32x32x16_bf16 v[32:47], v[88:91], v[84:87], v[32:47]\n\t"
        "s_add_u32 m0, %[xd], 0\n\t"
        "s_add_u32 %[st], %[xs], %[kx]\n\t"
        "buffer_load_dwordx4 %[xv], %[rx], %[st] offen lds\n\t"
        "s_add_u32 %[kx], %[kx], 128\n\t"
        "s_and_b32 %[kx], %[kx], 0x3ff\n\t"
        "v_mfma_f32_32x32x16_bf16 v[48:63], v[92:95], v[84:87], v[48:63]\n\t"
        "s_waitcnt vmcnt(3)\n\t"
        "s_barrier\n\t"
        "s_waitcnt lgkmcnt(0)\n\t"
        "v_mfma_f32_32x32x16_bf16 v[0:15], v[72:75], v[64:67], v[0:15]\n\t"
        "ds_read_b128 v[80:83], %[xb] offset:33312\n\t"
        "ds_read_b64_tr_b16 v[88:89], %[wb] offset:36864\n\t"
        "ds_read_b64_tr_b16 v[90:91], %[wb] offset:37888\n\t"
        "v_mfma_f32_32x32x16_bf16 v[16:31], v[76:79], v[64:67], v[16:31]\n\t"
        "ds_read_b128 v[84:87], %[xb] offset:33568\n\t"
        "ds_read_b64_tr_b16 v[92:93], %[wb] offset:37120\n\t"
        "ds_read_b64_tr_b16 v[94:95], %[wb] offset:38144\n\t"
        "v_mfma_f32_32x32x16_bf16 v[32:47], v[72:75], v[68:71], v[32:47]\n\t"
        "s_add_u32 m0, %[wd], 24576\n\t"
        "s_mul_i32 %[st], %[ku], 114688\n\t"
        "s_add_u32 %[st], %[st], %[w0]\n\t"
        "buffer_load_dwordx4 %[wv], %[rw], %[st] offen lds\n\t"
        "s_add_u32 %[ku], %[ku], 1\n\t"
        "s_and_b32 %[ku], %[ku], 15\n\t"
        "v_mfma_f32_32x32x16_bf16 v[48:63], v[76:79], v[68:71], v[48:63]\n\t"
        "s_waitcnt lgkmcnt(0)\n\t"
        "v_mfma_f32_32x32x16_bf16 v[0:15], v[88:91], v[80:83], v[0:15]\n\t"
        "ds_read_b128 v[64:67], %[xb] offset:33344\n\t"
        "ds_read_b64_tr_b16 v[72:73], %[wb] offset:40960\n\t"
        "ds_read_b64_tr_b16 v[74:75], %[wb] offset:41984\n\t"
        "v_mfma_f32_32x32x16_bf16 v[16:31], v[92:95], v[80:83], v[16:31]\n\t"
        "ds_read_b128 v[68:71], %[xb] offset:33600\n\t"
        "ds_read_b64_tr_b16 v[76:77], %[wb] offset:41216\n\t"
        "ds_read_b64_tr_b16 v[78:79], %[wb] offset:42240\n\t"
        "v_mfma_f32_32x32x16_bf16 v[32:47], v[88:91], v[84:87], v[32:47]\n\t"
        "v_mfma_f32_32x32x16_bf16 v[48:63], v[92:95], v[84:87], v[48:63]\n\t"
        "s_waitcnt vmcnt(4)\n\t"
        "s_barrier\n\t"
        "s_waitcnt lgkmcnt(0)\n\t"
        "v_mfma_f32_32x32x16_bf16 v[0:15], v[72:75], v[64:67], v[0:15]\n\t"
        "ds_read_b128 v[80:83], %[xb] offset:33376\n\t"
        "ds_read_b64_tr_b16 v[88:89], %[wb] offset:45056\n\t"
        "ds_read_b64_tr_b16 v[90:91], %[wb] offset:46080\n\t"
        "v_mfma_f32_32x32x16_bf16 v[16:31], v[76:79], v[64:67], v[16:31]\n\t"
        "ds_read_b128 v[84:87], %[xb] offset:33632\n\t"
        "ds_read_b64_tr_b16 v[92:93], %[wb] offset:45312\n\t"
        "ds_read_b64_tr_b16 v[94:95], %[wb] offset:46336\n\t"
        "v_mfma_f32_32x32x16_bf16 v[32:47], v[72:75], v[68:71], v[32:47]\n\t"
        "s_add_u32 m0, %[wd], 32768\n\t"
        "s_mul_i32 %[st], %[ku], 114688\n\t"
        "s_add_u32 %[st], %[st], %[w0]\n\t"
        "buffer_load_dwordx4 %[wv], %[rw], %[st] offen lds\n\t"
        "s_add_u32 %[ku], %[ku], 1\n\t"
        "s_and_b32 %[ku], %[ku], 15\n\t"
        "v_mfma_f32_32x32x16_bf16 v[48:63], v[76:79], v[68:71], v[48:63]\n\t"
        "s_waitcnt lgkmcnt(0)\n\t"
        "v_mfma_f32_32x32x16_bf16 v[0:15], v[88:91], v[80:83], v[0:15]\n\t"
        "ds_read_b128 v[64:67], %[xb] offset:49920\n\t"
        "ds_read_b64_tr_b16 v[72:73], %[wb] offset:0\n\t"
        "ds_read_b64_tr_b16 v[74:75], %[wb] offset:1024\n\t"
        "v_mfma_f32_32x32x16_bf16 v[16:31], v[92:95], v[80:83], v[16:31]\n\t"
        "ds_read_b128 v[68:71], %[xb] offset:50176\n\t"
        "ds_read_b64_tr_b16 v[76:77], %[wb] offset:256\n\t"
        "ds_read_b64_tr_b16 v[78:79], %[wb] offset:1280\n\t"
        "v_mfma_f32_32x32x16_bf16 v[32:47], v[88:91], v[84:87], v[32:47]\n\t"
        "s_add_u32 m0, %[xd], 16640\n\t"
        "s_add_u32 %[st], %[xs], %[kx]\n\t"
        "buffer_load_dwordx4 %[xv], %[rx], %[st] offen lds\n\t"
        "s_add_u32 %[kx], %[kx], 128\n\t"
        "s_and_b32 %[kx], %[kx], 0x3ff\n\t"
        "v_mfma_f32_32x32x16_bf16 v[48:63], v[92:95], v[84:87], v[48:63]\n\t"
        "s_waitcnt vmcnt(3)\n\t"
        "s_barrier\n\t"
        "s_waitcnt lgkmcnt(0)\n\t"
        "v_mfma_f32_32x32x16_bf16 v[0:15], v[72:75], v[64:67], v[0:15]\n\t"
        "ds_read_b128 v[80:83], %[xb] offset:49952\n\t"
        "ds_read_b64_tr_b16 v[88:89], %[wb] offset:4096\n\t"
        "ds_read_b64_tr_b16 v[90:91], %[wb] offset:5120\n\t"
        "v_mfma_f32_32x32x16_bf16 v[16:31], v[76:79], v[64:67], v[16:31]\n\t"
        "ds_read_b128 v[84:87], %[xb] offset:50208\n\t"
        "ds_read_b64_tr_b16 v[92:93], %[wb] offset:4352\n\t"
        "ds_read_b64_tr_b16 v[94:95], %[wb] offset:5376\n\t"
        "v_mfma_f32_32x32x16_bf16 v[32:47], v[72:75], v[68:71], v[32:47]\n\t"
        "s_add_u32 m0, %[wd], 40960\n\t"
        "s_mul_i32 %[st], %[ku], 114688\n\t"
        "s_add_u32 %[st], %[st], %[w0]\n\t"
        "buffer_load_dwordx4 %[wv], %[rw], %[st] offen lds\n\t"
        "s_add_u32 %[ku], %[ku], 1\n\t"
        "s_and_b32 %[ku], %[ku], 15\n\t"
        "v_mfma_f32_32x32x16_bf16 v[48:63], v[76:79], v[68:71], v[48:63]\n\t"
        "s_waitcnt lgkmcnt(0)\n\t"
        "v_mfma_f32_32x32x16_bf16 v[0:15], v[88:91], v[80:83], v[0:15]\n\t"
        "ds_read_b128 v[64:67], %[xb] offset:49984\n\t"
        "ds_read_b64_tr_b16 v[72:73], %[wb] offset:8192\n\t"
        "ds_read_b64_tr_b16 v[74:75], %[wb] offset:9216\n\t"
        "v_mfma_f32_32x32x16_bf16 v[16:31], v[92:95], v[80:83], v[16:31]\n\t"
        "ds_read_b128 v[68:71], %[xb] offset:50240\n\t"
        "ds_read_b64_tr_b16 v[76:77], %[wb] offset:8448\n\t"
        "ds_read_b64_tr_b16 v[78:79], %[wb] offset:9472\n\t"
        "v_mfma_f32_32x32x16_bf16 v[32:47], v[88:91], v[84:87], v[32:47]\n\t"
        "v_mfma_f32_32x32x16_bf16 v[48:63], v[92:95], v[84:87], v[48:63]\n\t"
        "s_waitcnt vmcnt(4)\n\t"
        "s_barrier\n\t"
        "s_waitcnt lgkmcnt(0)\n\t"
        "v_mfma_f32_32x32x16_bf16 v[0:15], v[72:75], v[64:67], v[0:15]\n\t"
        "ds_read_b128 v[80:83], %[xb] offset:50016\n\t"
        "ds_read_b64_tr_b16 v[88:89], %[wb] offset:12288\n\t"
        "ds_read_b64_tr_b16 v[90:91], %[wb] offset:13312\n\t"
        "v_mfma_f32_32x32x16_bf16 v[16:31], v[76:79], v[64:67], v[16:31]\n\t"
        "ds_read_b128 v[84:87], %[xb] offset:50272\n\t"
        "ds_read_b64_tr_b16 v[92:93], %[wb] offset:12544\n\t"
        "ds_read_b64_tr_b16 v[94:95], %[wb] offset:13568\n\t"
        "v_mfma_f32_32x32x16_bf16 v[32:47], v[72:75], v[68:71], v[32:47]\n\t"
        "s_add_u32 m0, %[wd], 0\n\t"
        "s_mul_i32 %[st], %[ku], 114688\n\t"
        "s_add_u32 %[st], %[st], %[w0]\n\t"
        "buffer_load_dwordx4 %[wv], %[rw], %[st] offen lds\n\t"
        "s_add_u32 %[ku], %[ku], 1\n\t"
        "s_and_b32 %[ku], %[ku], 15\n\t"
        "v_mfma_f32_32x32x16_bf16 v[48:63], v[76:79], v[68:71], v[48:63]\n\t"
        "s_waitcnt lgkmcnt(0)\n\t"
        "v_mfma_f32_32x32x16_bf16 v[0:15], v[88:91], v[80:83], v[0:15]\n\t"
        "ds_read_b128 v[64:67], %[xb] offset:0\n\t"
        "ds_read_b64_tr_b16 v[72:73], %[wb] offset:16384\n\t"
        "ds_read_b64_tr_b16 v[74:75], %[wb] offset:17408\n\t"
        "v_mfma_f32_32x32x16_bf16 v[16:31], v[92:95], v[80:83], v[16:31]\n\t"
        "ds_read_b128 v[68:71], %[xb] offset:256\n\t"
        "ds_read_b64_tr_b16 v[76:77], %[wb] offset:16640\n\t"
        "ds_read_b64_tr_b16 v[78:79], %[wb] offset:17664\n\t"
        "v_mfma_f32_32x32x16_bf16 v[32:47], v[88:91], v[84:87], v[32:47]\n\t"
        "s_add_u32 m0, %[xd], 33280\n\t"
        "s_add_u32 %[st], %[xs], %[kx]\n\t"
        "buffer_load_dwordx4 %[xv], %[rx], %[st] offen lds\n\t"
        "s_add_u32 %[kx], %[kx], 128\n\t"
        "s_and_b32 %[kx], %[kx], 0x3ff\n\t"
        "v_mfma_f32_32x32x16_bf16 v[48:63], v[92:95], v[84:87], v[48:63]\n\t"
        "s_waitcnt vmcnt(3)\n\t"
        "s_barrier\n\t"
        "buffer_store_dwordx4 %[pz], %[l16], %[re], %[pkv] offen sc1\n\t"
        "s_waitcnt lgkmcnt(0)\n\t"
        "v_mfma_f32_32x32x16_bf16 v[0:15], v[72:75], v[64:67], v[0:15]\n\t"
        "ds_read_b128 v[80:83], %[xb] offset:32\n\t"
        "ds_read_b64_tr_b16 v[88:89], %[wb] offset:20480\n\t"
        "ds_read_b64_tr_b16 v[90:91], %[wb] offset:21504\n\t"
        "v_mfma_f32_32x32x16_bf16 v[16:31], v[76:79], v[64:67], v[16:31]\n\t"
        "ds_read_b128 v[84:87], %[xb] offset:288\n\t"
        "ds_read_b64_tr_b16 v[92:93], %[wb] offset:20736\n\t"
        "ds_read_b64_tr_b16 v[94:95], %[wb] offset:21760\n\t"
        "v_mfma_f32_32x32x16_bf16 v[32:47], v[72:75], v[68:71], v[32:47]\n\t"
        "s_add_u32 m0, %[wd], 8192\n\t"
        "s_mul_i32 %[st], %[ku], 114688\n\t"
        "s_add_u32 %[st], %[st], %[w0]\n\t"
        "buffer_load_dwordx4 %[wv], %[rw], %[st] offen lds\n\t"
        "s_add_u32 %[ku], %[ku], 1\n\t"
        "s_and_b32 %[ku], %[ku], 15\n\t"
        "v_mfma_f32_32x32x16_bf16 v[48:63], v[76:79], v[68:71], v[48:63]\n\t"
        "s_waitcnt lgkmcnt(0)\n\t"
        "v_mfma_f32_32x32x16_bf16 v[0:15], v[88:91], v[80:83], v[0:15]\n\t"
        "ds_read_b128 v[64:67], %[xb] offset:64\n\t"
        "ds_read_b64_tr_b16 v[72:73], %[wb] offset:24576\n\t"
        "ds_read_b64_tr_b16 v[74:75], %[wb] offset:25600\n\t"
        "v_mfma_f32_32x32x16_bf16 v[16:31], v[92:95], v[80:83], v[16:31]\n\t"
        "ds_read_b128 v[68:71], %[xb] offset:320\n\t"
        "ds_read_b64_tr_b16 v[76:77], %[wb] offset:24832\n\t"
        "ds_read_b64_tr_b16 v[78:79], %[wb] offset:25856\n\t"
        "v_mfma_f32_32x32x16_bf16 v[32:47], v[88:91], v[84:87], v[32:47]\n\t"
        "v_mfma_f32_32x32x16_bf16 v[48:63], v[92:95], v[84:87], v[48:63]\n\t"
        "s_waitcnt vmcnt(5)\n\t"
        "s_barrier\n\t"
        "buffer_store_dwordx4 %[pz], %[l16], %[re], %[pkv] offen offset:1024 sc1\n\t"
        "s_waitcnt lgkmcnt(0)\n\t"
        "v_mfma_f32_32x32x16_bf16 v[0:15], v[72:75], v[64:67], v[0:15]\n\t"
        "ds_read_b128 v[80:83], %[xb] offset:96\n\t"
        "ds_read_b64_tr_b16 v[88:89], %[wb] offset:28672\n\t"
        "ds_read_b64_tr_b16 v[90:91], %[wb] offset:29696\n\t"
        "v_mfma_f32_32x32x16_bf16 v[16:31], v[76:79], v[64:67], v[16:31]\n\t"
        "ds_read_b128 v[84:87], %[xb] offset:352\n\t"
        "ds_read_b64_tr_b16 v[92:93], %[wb] offset:28928\n\t"
        "ds_read_b64_tr_b16 v[94:95], %[wb] offset:29952\n\t"
        "v_mfma_f32_32x32x16_bf16 v[32:47], v[72:75], v[68:71], v[32:47]\n\t"
        "s_add_u32 m0, %[wd], 16384\n\t"
        "s_mul_i32 %[st], %[ku], 114688\n\t"
        "s_add_u32 %[st], %[st], %[w0]\n\t"
        "buffer_load_dwordx4 %[wv], %[rw], %[st] offen lds\n\t"
        "s_add_u32 %[ku], %[ku], 1\n\t"
        "s_and_b32 %[ku], %[ku], 15\n\t"
        "v_mfma_f32_32x32x16_bf16 v[48:63], v[76:79], v[68:71], v[48:63]\n\t"
        "s_waitcnt lgkmcnt(0)\n\t"
        "v_mfma_f32_32x32x16_bf16 v[0:15], v[88:91], v[80:83], v[0:15]\n\t"
        "ds_read_b128 v[64:67], %[xb] offset:16640\n\t"
        "ds_read_b64_tr_b16 v[72:73], %[wb] offset:32768\n\t"
        "ds_read_b64_tr_b16 v[74:75], %[wb] offset:33792\n\t"
        "v_mfma_f32_32x32x16_bf16 v[16:31], v[92:95], v[80:83], v[16:31]\n\t"
        "ds_read_b128 v[68:71], %[xb] offset:16896\n\t"
        "ds_read_b64_tr_b16 v[76:77], %[wb] offset:33024\n\t"
        "ds_read_b64_tr_b16 v[78:79], %[wb] offset:34048\n\t"
        "v_mfma_f32_32x32x16_bf16 v[32:47], v[88:91], v[84:87], v[32:47]\n\t"
        "s_add_u32 m0, %[xd], 49920\n\t"
        "s_add_u32 %[st], %[xs], %[kx]\n\t"
        "buffer_load_dwordx4 %[xv], %[rx], %[st] offen lds\n\t"
        "s_add_u32 %[kx], %[kx], 128\n\t"
        "s_and_b32 %[kx], %[kx], 0x3ff\n\t"
        "v_mfma_f32_32x32x16_bf16 v[48:63], v[92:95], v[84:87], v[48:63]\n\t"
        "s_waitcnt vmcnt(5)\n\t"
        "s_barrier\n\t"
        "buffer_store_dwordx4 %[pz], %[l16], %[re], %[po] offen sc1\n\t"
        "s_waitcnt lgkmcnt(0)\n\t"
        "v_mfma_f32_32x32x16_bf16 v[0:15], v[72:75], v[64:67], v[0:15]\n\t"
        "ds_read_b128 v[80:83], %[xb] offset:16672\n\t"
        "ds_read_b64_tr_b16 v[88:89], %[wb] offset:36864\n\t"
        "ds_read_b64_tr_b16 v[90:91], %[wb] offset:37888\n\t"
        "v_mfma_f32_32x32x16_bf16 v[16:31], v[76:79], v[64:67], v[16:31]\n\t"
        "ds_read_b128 v[84:87], %[xb] offset:16928\n\t"
        "ds_read_b64_tr_b16 v[92:93], %[wb] offset:37120\n\t"
        "ds_read_b64_tr_b16 v[94:95], %[wb] offset:38144\n\t"
        "v_mfma_f32_32x32x16_bf16 v[32:47], v[72:75], v[68:71], v[32:47]\n\t"
        "s_add_u32 m0, %[wd], 24576\n\t"
        "s_mul_i32 %[st], %[ku], 114688\n\t"
        "s_add_u32 %[st], %[st], %[w0]\n\t"
        "buffer_load_dwordx4 %[wv], %[rw], %[st] offen lds\n\t"
        "s_add_u32 %[ku], %[ku], 1\n\t"
        "s_and_b32 %[ku], %[ku], 15\n\t"
        "v_mfma_f32_32x32x16_bf16 v[48:63], v[76:79], v[68:71], v[48:63]\n\t"
        "s_waitcnt lgkmcnt(0)\n\t"
        "v_mfma_f32_32x32x16_bf16 v[0:15], v[88:91], v[80:83], v[0:15]\n\t"
        "ds_read_b128 v[64:67], %[xb] offset:16704\n\t"
        "ds_read_b64_tr_b16 v[72:73], %[wb] offset:40960\n\t"
        "ds_read_b64_tr_b16 v[74:75], %[wb] offset:41984\n\t"
        "v_mfma_f32_32x32x16_bf16 v[16:31], v[92:95], v[80:83], v[16:31]\n\t"
        "ds_read_b128 v[68:71], %[xb] offset:16960\n\t"
        "ds_read_b64_tr_b16 v[76:77], %[wb] offset:41216\n\t"
        "ds_read_b64_tr_b16 v[78:79], %[wb] offset:42240\n\t"
        "v_mfma_f32_32x32x16_bf16 v[32:47], v[88:91], v[84:87], v[32:47]\n\t"
        "v_mfma_f32_32x32x16_bf16 v[48:63], v[92:95], v[84:87], v[48:63]\n\t"
        "s_waitcnt vmcnt(7)\n\t"
        "s_barrier\n\t"
        "s_waitcnt lgkmcnt(0)\n\t"
        "v_mfma_f32_32x32x16_bf16 v[0:15], v[72:75], v[64:67], v[0:15]\n\t"
        "ds_read_b128 v[80:83], %[xb] offset:16736\n\t"
        "ds_read_b64_tr_b16 v[88:89], %[wb] offset:45056\n\t"
        "ds_read_b64_tr_b16 v[90:91], %[wb] offset:46080\n\t"
        "v_mfma_f32_32x32x16_bf16 v[16:31], v[76:79], v[64:67], v[16:31]\n\t"
        "ds_read_b128 v[84:87], %[xb] offset:16992\n\t"
        "ds_read_b64_tr_b16 v[92:93], %[wb] offset:45312\n\t"
        "ds_read_b64_tr_b16 v[94:95], %[wb] offset:46336\n\t"
        "v_mfma_f32_32x32x16_bf16 v[32:47], v[72:75], v[68:71], v[32:47]\n\t"
        "v_mfma_f32_32x32x16_bf16 v[48:63], v[76:79], v[68:71], v[48:63]\n\t"
        "s_waitcnt lgkmcnt(0)\n\t"
        "v_mfma_f32_32x32x16_bf16 v[0:15], v[88:91], v[80:83], v[0:15]\n\t"
        "ds_read_b128 v[64:67], %[xb] offset:33280\n\t"
        "ds_read_b64_tr_b16 v[72:73], %[wb] offset:0\n\t"
        "ds_read_b64_tr_b16 v[74:75], %[wb] offset:1024\n\t"
        "v_mfma_f32_32x32x16_bf16 v[16:31], v[92:95], v[80:83], v[16:31]\n\t"
        "ds_read_b128 v[68:71], %[xb] offset:33536\n\t"
        "ds_read_b64_tr_b16 v[76:77], %[wb] offset:256\n\t"
        "ds_read_b64_tr_b16 v[78:79], %[wb] offset:1280\n\t"
        "v_mfma_f32_32x32x16_bf16 v[32:47], v[88:91], v[84:87], v[32:47]\n\t"
        "v_mfma_f32_32x32x16_bf16 v[48:63], v[92:95], v[84:87], v[48:63]\n\t"
        "s_waitcnt vmcnt(2)\n\t"
        "s_barrier\n\t"
        "s_waitcnt lgkmcnt(0)\n\t"
        "v_mfma_f32_32x32x16_bf16 v[0:15], v[72:75], v[64:67], v[0:15]\n\t"
        "ds_read_b128 v[80:83], %[xb] offset:33312\n\t"
        "ds_read_b64_tr_b16 v[88:89], %[wb] offset:4096\n\t"
        "ds_read_b64_tr_b16 v[90:91], %[wb] offset:5120\n\t"
        "v_mfma_f32_32x32x16_bf16 v[16:31], v[76:79], v[64:67], v[16:31]\n\t"
        "ds_read_b128 v[84:87], %[xb] offset:33568\n\t"
        "ds_read_b64_tr_b16 v[92:93], %[wb] offset:4352\n\t"
        "ds_read_b64_tr_b16 v[94:95], %[wb] offset:5376\n\t"
        "v_mfma_f32_32x32x16_bf16 v[32:47], v[72:75], v[68:71], v[32:47]\n\t"
        "v_mfma_f32_32x32x16_bf16 v[48:63], v[76:79], v[68:71], v[48:63]\n\t"
        "s_waitcnt lgkmcnt(0)\n\t"
        "v_mfma_f32_32x32x16_bf16 v[0:15], v[88:91], v[80:83], v[0:15]\n\t"
        "ds_read_b128 v[64:67], %[xb] offset:33344\n\t"
        "ds_read_b64_tr_b16 v[72:73], %[wb] offset:8192\n\t"
        "ds_read_b64_tr_b16 v[74:75], %[wb] offset:9216\n\t"
        "v_mfma_f32_32x32x16_bf16 v[16:31], v[92:95], v[80:83], v[16:31]\n\t"
        "ds_read_b128 v[68:71], %[xb] offset:33600\n\t"
        "ds_read_b64_tr_b16 v[76:77], %[wb] offset:8448\n\t"
        "ds_read_b64_tr_b16 v[78:79], %[wb] offset:9472\n\t"
        "v_mfma_f32_32x32x16_bf16 v[32:47], v[88:91], v[84:87], v[32:47]\n\t"
        "v_mfma_f32_32x32x16_bf16 v[48:63], v[92:95], v[84:87], v[48:63]\n\t"
        "s_waitcnt vmcnt(2)\n\t"
        "s_barrier\n\t"
        "s_waitcnt lgkmcnt(0)\n\t"
        "v_mfma_f32_32x32x16_bf16 v[0:15], v[72:75], v[64:67], v[0:15]\n\t"
        "ds_read_b128 v[80:83], %[xb] offset:33376\n\t"
        "ds_read_b64_tr_b16 v[88:89], %[wb] offset:12288\n\t"
        "ds_read_b64_tr_b16 v[90:91], %[wb] offset:13312\n\t"
        "v_mfma_f32_32x32x16_bf16 v[16:31], v[76:79], v[64:67], v[16:31]\n\t"
        "ds_read_b128 v[84:87], %[xb] offset:33632\n\t"
        "ds_read_b64_tr_b16 v[92:93], %[wb] offset:12544\n\t"
        "ds_read_b64_tr_b16 v[94:95], %[wb] offset:13568\n\t"
        "v_mfma_f32_32x32x16_bf16 v[32:47], v[72:75], v[68:71], v[32:47]\n\t"
        "v_mfma_f32_32x32x16_bf16 v[48:63], v[76:79], v[68:71], v[48:63]\n\t"
        "s_waitcnt lgkmcnt(0)\n\t"
        "v_mfma_f32_32x32x16_bf16 v[0:15], v[88:91], v[80:83], v[0:15]\n\t"
        "ds_read_b128 v[64:67], %[xb] offset:49920\n\t"
        "ds_read_b64_tr_b16 v[72:73], %[wb] offset:16384\n\t"
        "ds_read_b64_tr_b16 v[74:75], %[wb] offset:17408\n\t"
        "v_mfma_f32_32x32x16_bf16 v[16:31], v[92:95], v[80:83], v[16:31]\n\t"
        "ds_read_b128 v[68:71], %[xb] offset:50176\n\t"
        "ds_read_b64_tr_b16 v[76:77], %[wb] offset:16640\n\t"
        "ds_read_b64_tr_b16 v[78:79], %[wb] offset:17664\n\t"
        "v_mfma_f32_32x32x16_bf16 v[32:47], v[88:91], v[84:87], v[32:47]\n\t"
        "v_mfma_f32_32x32x16_bf16 v[48:63], v[92:95], v[84:87], v[48:63]\n\t"
        "s_waitcnt vmcnt(0)\n\t"
        "s_barrier\n\t"
        "s_cmp_eq_u32 %[ako], 0\n\t"
        "s_cbranch_scc1 .Lna_%=\n\t"
        "v_add_u32 %[l16], 1, %[ep]\n\t"
        "s_mov_b64 exec, 1\n\t"
        "buffer_store_dword %[l16], off, %[re], %[ako] sc1\n\t"
        "s_mov_b64 exec, -1\n\t"
        ".Lna_%=:\n\t"
        "s_waitcnt lgkmcnt(0)\n\t"
        "v_mfma_f32_32x32x16_bf16 v[0:15], v[72:75], v[64:67], v[0:15]\n\t"
        "ds_read_b128 v[80:83], %[xb] offset:49952\n\t"
        "ds_read_b64_tr_b16 v[88:89], %[wb] offset:20480\n\t"
        "ds_read_b64_tr_b16 v[90:91], %[wb] offset:21504\n\t"
        "v_mfma_f32_32x32x16_bf16 v[16:31], v[76:79], v[64:67], v[16:31]\n\t"
        "ds_read_b128 v[84:87], %[xb] offset:50208\n\t"
        "ds_read_b64_tr_b16 v[92:93], %[wb] offset:20736\n\t"
        "ds_read_b64_tr_b16 v[94:95], %[wb] offset:21760\n\t"
        "v_mfma_f32_32x32x16_bf16 v[32:47], v[72:75], v[68:71], v[32:47]\n\t"
        "v_mfma_f32_32x32x16_bf16 v[48:63], v[76:79], v[68:71], v[48:63]\n\t"
        "s_waitcnt lgkmcnt(0)\n\t"
        "v_mfma_f32_32x32x16_bf16 v[0:15], v[88:91], v[80:83], v[0:15]\n\t"
        "ds_read_b128 v[64:67], %[xb] offset:49984\n\t"
        "ds_read_b64_tr_b16 v[72:73], %[wb] offset:24576\n\t"
        "ds_read_b64_tr_b16 v[74:75], %[wb] offset:25600\n\t"
        "v_mfma_f32_32x32x16_bf16 v[16:31], v[92:95], v[80:83], v[16:31]\n\t"
        "ds_read_b128 v[68:71], %[xb] offset:50240\n\t"
        "ds_read_b64_tr_b16 v[76:77], %[wb] offset:24832\n\t"
        "ds_read_b64_tr_b16 v[78:79], %[wb] offset:25856\n\t"
        "v_mfma_f32_32x32x16_bf16 v[32:47], v[88:91], v[84:87], v[32:47]\n\t"
        "v_mfma_f32_32x32x16_bf16 v[48:63], v[92:95], v[84:87], v[48:63]\n\t"
        "s_waitcnt lgkmcnt(0)\n\t"
        "v_mfma_f32_32x32x16_bf16 v[0:15], v[72:75], v[64:67], v[0:15]\n\t"
        "ds_read_b128 v[80:83], %[xb] offset:50016\n\t"
        "ds_read_b64_tr_b16 v[88:89], %[wb] offset:28672\n\t"
        "ds_read_b64_tr_b16 v[90:91], %[wb] offset:29696\n\t"
        "v_mfma_f32_32x32x16_bf16 v[16:31], v[76:79], v[64:67], v[16:31]\n\t"
        "ds_read_b128 v[84:87], %[xb] offset:50272\n\t"
        "ds_read_b64_tr_b16 v[92:93], %[wb] offset:28928\n\t"
        "ds_read_b64_tr_b16 v[94:95], %[wb] offset:29952\n\t"
        "v_mfma_f32_32x32x16_bf16 v[32:47], v[72:75], v[68:71], v[32:47]\n\t"
        "v_mfma_f32_32x32x16_bf16 v[48:63], v[76:79], v[68:71], v[48:63]\n\t"
        "s_waitcnt lgkmcnt(0)\n\t"
        "v_mov_b32 %[l16], 1\n\t"
        "s_mov_b64 exec, 1\n\t"
        "ds_add_u32 %[fc], %[l16]\n\t"
        "s_mov_b64 exec, -1\n\t"
        "v_mfma_f32_32x32x16_bf16 v[0:15], v[88:91], v[80:83], v[0:15]\n\t"
        "v_mfma_f32_32x32x16_bf16 v[16:31], v[92:95], v[80:83], v[16:31]\n\t"
        "v_mfma_f32_32x32x16_bf16 v[32:47], v[88:91], v[84:87], v[32:47]\n\t"
        "v_mfma_f32_32x32x16_bf16 v[48:63], v[92:95], v[84:87], v[48:63]\n\t"
        "s_mov_b32 m0, %[sv]\n\t"
        "s_nop 10\n\t"
            : "=&{v[0:15]}"(h0), "=&{v[16:31]}"(h1), "=&{v[32:47]}"(h2), "=&{v[48:63]}"(h3), [sv] "=&s"(sv), [st] "=&s"(st), [ep] "=&v"(epoch),
              [kx] "+s"(sg.kx), [ku] "+s"(sg.ku), [l16] "+v"(l16)
            : [pz] "v"(pz), [pkv] "s"(pkv), [po] "s"(po), [ako] "s"(ako), [xv] "v"(sg.xv), [wv] "v"(sg.wv), [rx] "s"(sg.rx), [rw] "s"(sg.rw), [xs] "s"(sg.xs),
              [xd] "s"(sg.xd), [wd] "s"(sg.wd), [w0] "s"(sg.w0), [xb] "v"(lds0 + L_X + (r >> 1) * 1040 + (r & 1) * 128 + hf * 16 + tp * 512),
              [wb] "v"(lds0 + L_WF + (8 * hf + 2 * sel) * 256 + q * 64 + ch * 32 + p * 8),
              [re] "s"(mk_rsrc(ws, WS_END)), [fc] "v"(lds0 + L_CNT + C_STAGE * 4)
            : "memory", "scc", "v64", "v65", "v66", "v67", "v68", "v69", "v70", "v71", "v72", "v73", "v74", "v75", "v76", "v77", "v78", "v79", "v80", "v81", "v82", "v83", "v84", "v85", "v86", "v87", "v88", "v89", "v90", "v91", "v92", "v93", "v94", "v95");
        tag = __builtin_amdgcn_readfirstlane(epoch) + 1u;

        __builtin_amdgcn_s_setprio(3);
        const float og = __builtin_amdgcn_rcpf(1.f + __builtin_amdgcn_exp2f(get_gate(ws, L, rb, w, lane, tag) * LOG2E));
        const auto gs = __builtin_amdgcn_permlane32_swap(__float_as_uint(og), __float_as_uint(og), false, false);
        const float g0 = __uint_as_float(gs[0]), g1 = __uint_as_float(gs[1]);
        u32x4 pp[8] = {relu8(pack8<0>(h0, g0)), relu8(pack8<8>(h0, g0)), relu8(pack8<0>(h1, g0)), relu8(pack8<8>(h1, g0)),
                       relu8(pack8<0>(h2, g1)), relu8(pack8<8>(h2, g1)), relu8(pack8<0>(h3, g1)), relu8(pack8<8>(h3, g1))};
        asm volatile("" : "+v"(pp[0]), "+v"(pp[1]), "+v"(pp[2]), "+v"(pp[3]), "+v"(pp[4]), "+v"(pp[5]), "+v"(pp[6]), "+v"(pp[7]));
        lds_wait_ge<4>(cw + C_REQ, 4u);

        const int zo = WS_Z + (rb * 4 + 2 * tp) * ZT + (cb * 8 + sel * 4) * 1024;
#pragma unroll
        for (int i = 0; i < 8; ++i) {
            __builtin_amdgcn_raw_buffer_store_b128(pp[i], rs, lane * 16 + (i & 3) * 1024, zo + (i >> 2) * ZT, AUX_SC1);
            if ((i & 1) == 1) drain();
            if ((i & 3) == 3 && lane == 0) st_flag(rs, WS_HF + ((rb * 4 + 2 * tp + (i >> 2)) * 32 + cb * 2 + sel) * FS, tag);
        }
        lds_wait_ge<1>(cw + C_STAGE, 4u);
    } else {
            asm volatile(
        "s_mov_b32 %[sv], m0\n\t"
        "s_waitcnt vmcnt(3) lgkmcnt(0)\n\t"
        "s_barrier\n\t"
        "s_waitcnt vmcnt(2) lgkmcnt(0)\n\t"
        "s_barrier\n\t"
        "s_add_u32 m0, %[xd], 33280\n\t"
        "s_add_u32 %[st], %[xs], %[kx]\n\t"
        "buffer_load_dwordx4 %[xv], %[rx], %[st] offen lds\n\t"
        "s_add_u32 %[kx], %[kx], 128\n\t"
        "s_and_b32 %[kx], %[kx], 0x3ff\n\t"
        "s_waitcnt vmcnt(1) lgkmcnt(0)\n\t"
        "s_barrier\n\t"
        "s_waitcnt vmcnt(1) lgkmcnt(0)\n\t"
        "s_barrier\n\t"
        "buffer_load_dword %[ep], off, %[re], 0 sc1\n\t"
        "s_add_u32 m0, %[xd], 49920\n\t"
        "s_add_u32 %[st], %[xs], %[kx]\n\t"
        "buffer_load_dwordx4 %[xv], %[rx], %[st] offen lds\n\t"
        "s_add_u32 %[kx], %[kx], 128\n\t"
        "s_and_b32 %[kx], %[kx], 0x3ff\n\t"
        "s_waitcnt vmcnt(2) lgkmcnt(0)\n\t"
        "s_barrier\n\t"
        "s_waitcnt vmcnt(2) lgkmcnt(0)\n\t"
        "s_barrier\n\t"
        "s_add_u32 m0, %[xd], 0\n\t"
        "s_add_u32 %[st], %[xs], %[kx]\n\t"
        "buffer_load_dwordx4 %[xv], %[rx], %[st] offen lds\n\t"
        "s_add_u32 %[kx], %[kx], 128\n\t"
        "s_and_b32 %[kx], %[kx], 0x3ff\n\t"
        "s_waitcnt vmcnt(1) lgkmcnt(0)\n\t"
        "s_barrier\n\t"
        "s_waitcnt vmcnt(1) lgkmcnt(0)\n\t"
        "s_barrier\n\t"
        "s_add_u32 m0, %[xd], 16640\n\t"
        "s_add_u32 %[st], %[xs], %[kx]\n\t"
        "buffer_load_dwordx4 %[xv], %[rx], %[st] offen lds\n\t"
        "s_add_u32 %[kx], %[kx], 128\n\t"
        "s_and_b32 %[kx], %[kx], 0x3ff\n\t"
        "s_waitcnt vmcnt(1) lgkmcnt(0)\n\t"
        "s_barrier\n\t"
        "s_waitcnt vmcnt(1) lgkmcnt(0)\n\t"
        "s_barrier\n\t"
        "s_add_u32 m0, %[xd], 33280\n\t"
        "s_add_u32 %[st], %[xs], %[kx]\n\t"
        "buffer_load_dwordx4 %[xv], %[rx], %[st] offen lds\n\t"
        "s_add_u32 %[kx], %[kx], 128\n\t"
        "s_and_b32 %[kx], %[kx], 0x3ff\n\t"
        "s_waitcnt vmcnt(1) lgkmcnt(0)\n\t"
        "s_barrier\n\t"
        "buffer_store_dwordx4 %[pz], %[l16], %[re], %[pkv] offen sc1\n\t"
        "s_waitcnt vmcnt(2) lgkmcnt(0)\n\t"
        "s_barrier\n\t"
        "buffer_store_dwordx4 %[pz], %[l16], %[re], %[pkv] offen offset:1024 sc1\n\t"
        "s_add_u32 m0, %[xd], 49920\n\t"
        "s_add_u32 %[st], %[xs], %[kx]\n\t"
        "buffer_load_dwordx4 %[xv], %[rx], %[st] offen lds\n\t"
        "s_add_u32 %[kx], %[kx], 128\n\t"
        "s_and_b32 %[kx], %[kx], 0x3ff\n\t"
        "s_waitcnt vmcnt(3) lgkmcnt(0)\n\t"
        "s_barrier\n\t"
        "buffer_store_dwordx4 %[pz], %[l16], %[re], %[po] offen sc1\n\t"
        "s_waitcnt vmcnt(4) lgkmcnt(0)\n\t"
        "s_barrier\n\t"
        "s_waitcnt vmcnt(1) lgkmcnt(0)\n\t"
        "s_barrier\n\t"
        "s_waitcnt vmcnt(1) lgkmcnt(0)\n\t"
        "s_barrier\n\t"
        "s_waitcnt vmcnt(0) lgkmcnt(0)\n\t"
        "s_barrier\n\t"
        "s_cmp_eq_u32 %[ako], 0\n\t"
        "s_cbranch_scc1 .Lna_%=\n\t"
        "v_add_u32 %[l16], 1, %[ep]\n\t"
        "s_mov_b64 exec, 1\n\t"
        "buffer_store_dword %[l16], off, %[re], %[ako] sc1\n\t"
        "s_mov_b64 exec, -1\n\t"
        ".Lna_%=:\n\t"
        "s_mov_b32 m0, %[sv]\n\t"
        "s_nop 10\n\t"
                : [sv] "=&s"(sv), [st] "=&s"(st), [ep] "=&v"(epoch), [kx] "+s"(sg.kx), [ku] "+s"(sg.ku), [l16] "+v"(l16)
                : [pz] "v"(pz), [pkv] "s"(pkv), [po] "s"(po), [ako] "s"(ako), [xv] "v"(sg.xv), [rx] "s"(sg.rx), [xs] "s"(sg.xs), [xd] "s"(sg.xd), [re] "s"(mk_rsrc(ws, WS_END))
                : "memory", "scc");
        tag = __builtin_amdgcn_readfirstlane(epoch) + 1u;
        __builtin_amdgcn_s_setprio(3);
        if (cb == 15 && rb == 15 && w == 0) {
#pragma unroll 1
            for (int i = 0; i < 4; ++i) wait_flags<8>(rs, WS_ACK + (i * 64 + lane) * FS, tag);
            if (lane == 0) st_flag(rs, WS_EPOCH, tag);
        }
        lds_wait_ge<8>(cw + C_REQ, 4u);
        if (w < 2) get_gate(ws, L, rb, w, lane, tag);
        lds_wait_ge<16>(cw + 32, 6 * 0xDDDD);
    }
    const int rb64 = g8 * 4 + (idx & 3), cb64 = idx >> 2, kq = w;
    const u32x4 rz = mk_rsrc(ws, WS_END), r2 = mk_rsrc(w2, DFF * DM * 2);
    const unsigned wv = (unsigned)((4 * hf + q) * 1024 + cb64 * 128 + ch * 64 + p * 16);
    const unsigned wd = lds0 + L_F + (unsigned)(kq * 16384);
    const unsigned wbl = wd + hf * 512 + q * 64 + (ch ^ (kq & 1)) * 32 + p * 8;
    const unsigned zv = (unsigned)(WS_Z + rb64 * 2 * ZT + lane * 16);
    unsigned kw = (unsigned)(cb64 * 11 + (idx & 3) * 7) % 28u, kz = kw * 1024;

    asm volatile(
        "s_mov_b32 %[sv], m0\n\t"
        "s_add_u32 m0, %[wd], 0\n\t"
        "s_lshl_b32 %[st], %[kw], 14\n\t"
        "s_add_u32 %[st], %[st], %[s2]\n\t"
        "buffer_load_dwordx4 %[wv], %[r2], %[st] offen lds\n\t"
        "s_add_u32 m0, %[wd], 1024\n\t"
        "s_add_u32 %[st], %[st], 8192\n\t"
        "buffer_load_dwordx4 %[wv], %[r2], %[st] offen lds\n\t"
        "s_add_u32 %[kw], %[kw], 1\n\t"
        "s_cmp_eq_u32 %[kw], 28\n\t"
        "s_cselect_b32 %[kw], 0, %[kw]\n\t"
        "s_add_u32 m0, %[wd], 2048\n\t"
        "s_lshl_b32 %[st], %[kw], 14\n\t"
        "s_add_u32 %[st], %[st], %[s2]\n\t"
        "buffer_load_dwordx4 %[wv], %[r2], %[st] offen lds\n\t"
        "s_add_u32 m0, %[wd], 3072\n\t"
        "s_add_u32 %[st], %[st], 8192\n\t"
        "buffer_load_dwordx4 %[wv], %[r2], %[st] offen lds\n\t"
        "s_add_u32 %[kw], %[kw], 1\n\t"
        "s_cmp_eq_u32 %[kw], 28\n\t"
        "s_cselect_b32 %[kw], 0, %[kw]\n\t"
        "s_add_u32 m0, %[wd], 4096\n\t"
        "s_lshl_b32 %[st], %[kw], 14\n\t"
        "s_add_u32 %[st], %[st], %[s2]\n\t"
        "buffer_load_dwordx4 %[wv], %[r2], %[st] offen lds\n\t"
        "s_add_u32 m0, %[wd], 5120\n\t"
        "s_add_u32 %[st], %[st], 8192\n\t"
        "buffer_load_dwordx4 %[wv], %[r2], %[st] offen lds\n\t"
        "s_add_u32 %[kw], %[kw], 1\n\t"
        "s_cmp_eq_u32 %[kw], 28\n\t"
        "s_cselect_b32 %[kw], 0, %[kw]\n\t"
        "s_add_u32 m0, %[wd], 6144\n\t"
        "s_lshl_b32 %[st], %[kw], 14\n\t"
        "s_add_u32 %[st], %[st], %[s2]\n\t"
        "buffer_load_dwordx4 %[wv], %[r2], %[st] offen lds\n\t"
        "s_add_u32 m0, %[wd], 7168\n\t"
        "s_add_u32 %[st], %[st], 8192\n\t"
        "buffer_load_dwordx4 %[wv], %[r2], %[st] offen lds\n\t"
        "s_add_u32 %[kw], %[kw], 1\n\t"
        "s_cmp_eq_u32 %[kw], 28\n\t"
        "s_cselect_b32 %[kw], 0, %[kw]\n\t"
        "s_mov_b32 m0, %[sv]\n\t"
        : [sv] "=&s"(sv), [st] "=&s"(st), [kw] "+s"(kw)
        : [wv] "v"(wv), [wd] "s"(wd), [r2] "s"(r2), [s2] "s"(448 * 1024 * kq)
        : "memory", "scc");
    wait_flags<8>(rs, WS_ACK + blk_of(4 * (lane >> 4) + kq, rb64 >> 1) * FS, tag);
    wait_flags<12>(rs, WS_HF + ((rb64 * 2 + (lane >> 5)) * 32 + 7 * kq + ((lane & 31) < 28 ? (lane & 31) >> 2 : 6)) * FS, tag);
    drain();

    __builtin_amdgcn_s_setprio(0);
    f32x16 c0, c1, c2, c3;
    {
        unsigned tm, t2, sp;
        asm volatile(
        "s_mov_b32 %[sv], m0\n\t"
        "s_add_u32 %[st], %[z2], %[kz]\n\t"
        "buffer_load_dwordx4 v[80:83], %[zv], %[rz], %[st] offen sc1\n\t"
        "buffer_load_dwordx4 v[84:87], %[zu], %[rz], %[st] offen sc1\n\t"
        "s_add_u32 %[kz], %[kz], 1024\n\t"
        "s_cmp_eq_u32 %[kz], 28672\n\t"
        "s_cselect_b32 %[kz], 0, %[kz]\n\t"
        "s_add_u32 %[st], %[z2], %[kz]\n\t"
        "buffer_load_dwordx4 v[88:91], %[zv], %[rz], %[st] offen sc1\n\t"
        "buffer_load_dwordx4 v[92:95], %[zu], %[rz], %[st] offen sc1\n\t"
        "s_add_u32 %[kz], %[kz], 1024\n\t"
        "s_cmp_eq_u32 %[kz], 28672\n\t"
        "s_cselect_b32 %[kz], 0, %[kz]\n\t"
        "s_add_u32 %[st], %[z2], %[kz]\n\t"
        "buffer_load_dwordx4 v[96:99], %[zv], %[rz], %[st] offen sc1\n\t"
        "buffer_load_dwordx4 v[100:103], %[zu], %[rz], %[st] offen sc1\n\t"
        "s_add_u32 %[kz], %[kz], 1024\n\t"
        "s_cmp_eq_u32 %[kz], 28672\n\t"
        "s_cselect_b32 %[kz], 0, %[kz]\n\t"
        "s_add_u32 %[st], %[z2], %[kz]\n\t"
        "buffer_load_dwordx4 v[104:107], %[zv], %[rz], %[st] offen sc1\n\t"
        "buffer_load_dwordx4 v[108:111], %[zu], %[rz], %[st] offen sc1\n\t"
        "s_add_u32 %[kz], %[kz], 1024\n\t"
        "s_cmp_eq_u32 %[kz], 28672\n\t"
        "s_cselect_b32 %[kz], 0, %[kz]\n\t"
        "ds_read_b64_tr_b16 v[64:65], %[wb0] offset:0\n\t"
        "ds_read_b64_tr_b16 v[66:67], %[wb0] offset:1024\n\t"
        "ds_read_b64_tr_b16 v[68:69], %[wb1] offset:0\n\t"
        "ds_read_b64_tr_b16 v[70:71], %[wb1] offset:1024\n\t"
        "s_waitcnt vmcnt(6) lgkmcnt(0)\n\t"
        "v_mfma_f32_32x32x16_bf16 v[0:15], v[64:67], v[80:83], 0\n\t"
        "ds_read_b64_tr_b16 v[72:73], %[wb0] offset:2048\n\t"
        "ds_read_b64_tr_b16 v[74:75], %[wb0] offset:3072\n\t"
        "ds_read_b64_tr_b16 v[76:77], %[wb1] offset:2048\n\t"
        "ds_read_b64_tr_b16 v[78:79], %[wb1] offset:3072\n\t"
        "v_mfma_f32_32x32x16_bf16 v[16:31], v[68:71], v[80:83], 0\n\t"
        "s_add_u32 m0, %[wd], 8192\n\t"
        "s_lshl_b32 %[st], %[kw], 14\n\t"
        "s_add_u32 %[st], %[st], %[s2]\n\t"
        "buffer_load_dwordx4 %[wv], %[r2], %[st] offen lds\n\t"
        "s_add_u32 m0, %[wd], 9216\n\t"
        "s_add_u32 %[st], %[st], 8192\n\t"
        "buffer_load_dwordx4 %[wv], %[r2], %[st] offen lds\n\t"
        "s_add_u32 %[kw], %[kw], 1\n\t"
        "s_cmp_eq_u32 %[kw], 28\n\t"
        "s_cselect_b32 %[kw], 0, %[kw]\n\t"
        "v_mfma_f32_32x32x16_bf16 v[32:47], v[64:67], v[84:87], 0\n\t"
        "v_mfma_f32_32x32x16_bf16 v[48:63], v[68:71], v[84:87], 0\n\t"
        "s_add_u32 %[st], %[z2], %[kz]\n\t"
        "buffer_load_dwordx4 v[80:83], %[zv], %[rz], %[st] offen sc1\n\t"
        "buffer_load_dwordx4 v[84:87], %[zu], %[rz], %[st] offen sc1\n\t"
        "s_add_u32 %[kz], %[kz], 1024\n\t"
        "s_cmp_eq_u32 %[kz], 28672\n\t"
        "s_cselect_b32 %[kz], 0, %[kz]\n\t"
        "s_waitcnt vmcnt(8) lgkmcnt(0)\n\t"
        "v_mfma_f32_32x32x16_bf16 v[0:15], v[72:75], v[88:91], v[0:15]\n\t"
        "ds_read_b64_tr_b16 v[64:65], %[wb0] offset:4096\n\t"
        "ds_read_b64_tr_b16 v[66:67], %[wb0] offset:5120\n\t"
        "ds_read_b64_tr_b16 v[68:69], %[wb1] offset:4096\n\t"
        "ds_read_b64_tr_b16 v[70:71], %[wb1] offset:5120\n\t"
        "v_mfma_f32_32x32x16_bf16 v[16:31], v[76:79], v[88:91], v[16:31]\n\t"
        "s_add_u32 m0, %[wd], 10240\n\t"
        "s_lshl_b32 %[st], %[kw], 14\n\t"
        "s_add_u32 %[st], %[st], %[s2]\n\t"
        "buffer_load_dwordx4 %[wv], %[r2], %[st] offen lds\n\t"
        "s_add_u32 m0, %[wd], 11264\n\t"
        "s_add_u32 %[st], %[st], 8192\n\t"
        "buffer_load_dwordx4 %[wv], %[r2], %[st] offen lds\n\t"
        "s_add_u32 %[kw], %[kw], 1\n\t"
        "s_cmp_eq_u32 %[kw], 28\n\t"
        "s_cselect_b32 %[kw], 0, %[kw]\n\t"
        "v_mfma_f32_32x32x16_bf16 v[32:47], v[72:75], v[92:95], v[32:47]\n\t"
        "v_mfma_f32_32x32x16_bf16 v[48:63], v[76:79], v[92:95], v[48:63]\n\t"
        "s_add_u32 %[st], %[z2], %[kz]\n\t"
        "buffer_load_dwordx4 v[88:91], %[zv], %[rz], %[st] offen sc1\n\t"
        "buffer_load_dwordx4 v[92:95], %[zu], %[rz], %[st] offen sc1\n\t"
        "s_add_u32 %[kz], %[kz], 1024\n\t"
        "s_cmp_eq_u32 %[kz], 28672\n\t"
        "s_cselect_b32 %[kz], 0, %[kz]\n\t"
        "s_waitcnt vmcnt(10) lgkmcnt(0)\n\t"
        "v_mfma_f32_32x32x16_bf16 v[0:15], v[64:67], v[96:99], v[0:15]\n\t"
        "ds_read_b64_tr_b16 v[72:73], %[wb0] offset:6144\n\t"
        "ds_read_b64_tr_b16 v[74:75], %[wb0] offset:7168\n\t"
        "ds_read_b64_tr_b16 v[76:77], %[wb1] offset:6144\n\t"
        "ds_read_b64_tr_b16 v[78:79], %[wb1] offset:7168\n\t"
        "v_mfma_f32_32x32x16_bf16 v[16:31], v[68:71], v[96:99], v[16:31]\n\t"
        "s_add_u32 m0, %[wd], 12288\n\t"
        "s_lshl_b32 %[st], %[kw], 14\n\t"
        "s_add_u32 %[st], %[st], %[s2]\n\t"
        "buffer_load_dwordx4 %[wv], %[r2], %[st] offen lds\n\t"
        "s_add_u32 m0, %[wd], 13312\n\t"
        "s_add_u32 %[st], %[st], 8192\n\t"
        "buffer_load_dwordx4 %[wv], %[r2], %[st] offen lds\n\t"
        "s_add_u32 %[kw], %[kw], 1\n\t"
        "s_cmp_eq_u32 %[kw], 28\n\t"
        "s_cselect_b32 %[kw], 0, %[kw]\n\t"
        "v_mfma_f32_32x32x16_bf16 v[32:47], v[64:67], v[100:103], v[32:47]\n\t"
        "v_mfma_f32_32x32x16_bf16 v[48:63], v[68:71], v[100:103], v[48:63]\n\t"
        "s_add_u32 %[st], %[z2], %[kz]\n\t"
        "buffer_load_dwordx4 v[96:99], %[zv], %[rz], %[st] offen sc1\n\t"
        "buffer_load_dwordx4 v[100:103], %[zu], %[rz], %[st] offen sc1\n\t"
        "s_add_u32 %[kz], %[kz], 1024\n\t"
        "s_cmp_eq_u32 %[kz], 28672\n\t"
        "s_cselect_b32 %[kz], 0, %[kz]\n\t"
        "s_waitcnt vmcnt(10) lgkmcnt(0)\n\t"
        "v_mfma_f32_32x32x16_bf16 v[0:15], v[72:75], v[104:107], v[0:15]\n\t"
        "ds_read_b64_tr_b16 v[64:65], %[wb0] offset:8192\n\t"
        "ds_read_b64_tr_b16 v[66:67], %[wb0] offset:9216\n\t"
        "ds_read_b64_tr_b16 v[68:69], %[wb1] offset:8192\n\t"
        "ds_read_b64_tr_b16 v[70:71], %[wb1] offset:9216\n\t"
        "v_mfma_f32_32x32x16_bf16 v[16:31], v[76:79], v[104:107], v[16:31]\n\t"
        "s_add_u32 m0, %[wd], 14336\n\t"
        "s_lshl_b32 %[st], %[kw], 14\n\t"
        "s_add_u32 %[st], %[st], %[s2]\n\t"
        "buffer_load_dwordx4 %[wv], %[r2], %[st] offen lds\n\t"
        "s_add_u32 m0, %[wd], 15360\n\t"
        "s_add_u32 %[st], %[st], 8192\n\t"
        "buffer_load_dwordx4 %[wv], %[r2], %[st] offen lds\n\t"
        "s_add_u32 %[kw], %[kw], 1\n\t"
        "s_cmp_eq_u32 %[kw], 28\n\t"
        "s_cselect_b32 %[kw], 0, %[kw]\n\t"
        "v_mfma_f32_32x32x16_bf16 v[32:47], v[72:75], v[108:111], v[32:47]\n\t"
        "v_mfma_f32_32x32x16_bf16 v[48:63], v[76:79], v[108:111], v[48:63]\n\t"
        "s_add_u32 %[st], %[z2], %[kz]\n\t"
        "buffer_load_dwordx4 v[104:107], %[zv], %[rz], %[st] offen sc1\n\t"
        "buffer_load_dwordx4 v[108:111], %[zu], %[rz], %[st] offen sc1\n\t"
        "s_add_u32 %[kz], %[kz], 1024\n\t"
        "s_cmp_eq_u32 %[kz], 28672\n\t"
        "s_cselect_b32 %[kz], 0, %[kz]\n\t"
        "s_waitcnt vmcnt(10) lgkmcnt(0)\n\t"
        "v_mfma_f32_32x32x16_bf16 v[0:15], v[64:67], v[80:83], v[0:15]\n\t"
        "ds_read_b64_tr_b16 v[72:73], %[wb0] offset:10240\n\t"
        "ds_read_b64_tr_b16 v[74:75], %[wb0] offset:11264\n\t"
        "ds_read_b64_tr_b16 v[76:77], %[wb1] offset:10240\n\t"
        "ds_read_b64_tr_b16 v[78:79], %[wb1] offset:11264\n\t"
        "v_mfma_f32_32x32x16_bf16 v[16:31], v[68:71], v[80:83], v[16:31]\n\t"
        "s_add_u32 m0, %[wd], 0\n\t"
        "s_lshl_b32 %[st], %[kw], 14\n\t"
        "s_add_u32 %[st], %[st], %[s2]\n\t"
        "buffer_load_dwordx4 %[wv], %[r2], %[st] offen lds\n\t"
        "s_add_u32 m0, %[wd], 1024\n\t"
        "s_add_u32 %[st], %[st], 8192\n\t"
        "buffer_load_dwordx4 %[wv], %[r2], %[st] offen lds\n\t"
        "s_add_u32 %[kw], %[kw], 1\n\t"
        "s_cmp_eq_u32 %[kw], 28\n\t"
        "s_cselect_b32 %[kw], 0, %[kw]\n\t"
        "v_mfma_f32_32x32x16_bf16 v[32:47], v[64:67], v[84:87], v[32:47]\n\t"
        "v_mfma_f32_32x32x16_bf16 v[48:63], v[68:71], v[84:87], v[48:63]\n\t"
        "s_add_u32 %[st], %[z2], %[kz]\n\t"
        "buffer_load_dwordx4 v[80:83], %[zv], %[rz], %[st] offen sc1\n\t"
        "buffer_load_dwordx4 v[84:87], %[zu], %[rz], %[st] offen sc1\n\t"
        "s_add_u32 %[kz], %[kz], 1024\n\t"
        "s_cmp_eq_u32 %[kz], 28672\n\t"
        "s_cselect_b32 %[kz], 0, %[kz]\n\t"
        "s_waitcnt vmcnt(10) lgkmcnt(0)\n\t"
        "v_mfma_f32_32x32x16_bf16 v[0:15], v[72:75], v[88:91], v[0:15]\n\t"
        "ds_read_b64_tr_b16 v[64:65], %[wb0] offset:12288\n\t"
        "ds_read_b64_tr_b16 v[66:67], %[wb0] offset:13312\n\t"
        "ds_read_b64_tr_b16 v[68:69], %[wb1] offset:12288\n\t"
        "ds_read_b64_tr_b16 v[70:71], %[wb1] offset:13312\n\t"
        "v_mfma_f32_32x32x16_bf16 v[16:31], v[76:79], v[88:91], v[16:31]\n\t"
        "s_add_u32 m0, %[wd], 2048\n\t"
        "s_lshl_b32 %[st], %[kw], 14\n\t"
        "s_add_u32 %[st], %[st], %[s2]\n\t"
        "buffer_load_dwordx4 %[wv], %[r2], %[st] offen lds\n\t"
        "s_add_u32 m0, %[wd], 3072\n\t"
        "s_add_u32 %[st], %[st], 8192\n\t"
        "buffer_load_dwordx4 %[wv], %[r2], %[st] offen lds\n\t"
        "s_add_u32 %[kw], %[kw], 1\n\t"
        "s_cmp_eq_u32 %[kw], 28\n\t"
        "s_cselect_b32 %[kw], 0, %[kw]\n\t"
        "v_mfma_f32_32x32x16_bf16 v[32:47], v[72:75], v[92:95], v[32:47]\n\t"
        "v_mfma_f32_32x32x16_bf16 v[48:63], v[76:79], v[92:95], v[48:63]\n\t"
        "s_add_u32 %[st], %[z2], %[kz]\n\t"
        "buffer_load_dwordx4 v[88:91], %[zv], %[rz], %[st] offen sc1\n\t"
        "buffer_load_dwordx4 v[92:95], %[zu], %[rz], %[st] offen sc1\n\t"
        "s_add_u32 %[kz], %[kz], 1024\n\t"
        "s_cmp_eq_u32 %[kz], 28672\n\t"
        "s_cselect_b32 %[kz], 0, %[kz]\n\t"
        "s_waitcnt vmcnt(10) lgkmcnt(0)\n\t"
        "v_mfma_f32_32x32x16_bf16 v[0:15], v[64:67], v[96:99], v[0:15]\n\t"
        "ds_read_b64_tr_b16 v[72:73], %[wb0] offset:14336\n\t"
        "ds_read_b64_tr_b16 v[74:75], %[wb0] offset:15360\n\t"
        "ds_read_b64_tr_b16 v[76:77], %[wb1] offset:14336\n\t"
        "ds_read_b64_tr_b16 v[78:79], %[wb1] offset:15360\n\t"
        "v_mfma_f32_32x32x16_bf16 v[16:31], v[68:71], v[96:99], v[16:31]\n\t"
        "s_add_u32 m0, %[wd], 4096\n\t"
        "s_lshl_b32 %[st], %[kw], 14\n\t"
        "s_add_u32 %[st], %[st], %[s2]\n\t"
        "buffer_load_dwordx4 %[wv], %[r2], %[st] offen lds\n\t"
        "s_add_u32 m0, %[wd], 5120\n\t"
        "s_add_u32 %[st], %[st], 8192\n\t"
        "buffer_load_dwordx4 %[wv], %[r2], %[st] offen lds\n\t"
        "s_add_u32 %[kw], %[kw], 1\n\t"
        "s_cmp_eq_u32 %[kw], 28\n\t"
        "s_cselect_b32 %[kw], 0, %[kw]\n\t"
        "v_mfma_f32_32x32x16_bf16 v[32:47], v[64:67], v[100:103], v[32:47]\n\t"
        "v_mfma_f32_32x32x16_bf16 v[48:63], v[68:71], v[100:103], v[48:63]\n\t"
        "s_add_u32 %[st], %[z2], %[kz]\n\t"
        "buffer_load_dwordx4 v[96:99], %[zv], %[rz], %[st] offen sc1\n\t"
        "buffer_load_dwordx4 v[100:103], %[zu], %[rz], %[st] offen sc1\n\t"
        "s_add_u32 %[kz], %[kz], 1024\n\t"
        "s_cmp_eq_u32 %[kz], 28672\n\t"
        "s_cselect_b32 %[kz], 0, %[kz]\n\t"
        "s_waitcnt vmcnt(10) lgkmcnt(0)\n\t"
        "v_mfma_f32_32x32x16_bf16 v[0:15], v[72:75], v[104:107], v[0:15]\n\t"
        "ds_read_b64_tr_b16 v[64:65], %[wb0] offset:0\n\t"
        "ds_read_b64_tr_b16 v[66:67], %[wb0] offset:1024\n\t"
        "ds_read_b64_tr_b16 v[68:69], %[wb1] offset:0\n\t"
        "ds_read_b64_tr_b16 v[70:71], %[wb1] offset:1024\n\t"
        "v_mfma_f32_32x32x16_bf16 v[16:31], v[76:79], v[104:107], v[16:31]\n\t"
        "s_add_u32 m0, %[wd], 6144\n\t"
        "s_lshl_b32 %[st], %[kw], 14\n\t"
        "s_add_u32 %[st], %[st], %[s2]\n\t"
        "buffer_load_dwordx4 %[wv], %[r2], %[st] offen lds\n\t"
        "s_add_u32 m0, %[wd], 7168\n\t"
        "s_add_u32 %[st], %[st], 8192\n\t"
        "buffer_load_dwordx4 %[wv], %[r2], %[st] offen lds\n\t"
        "s_add_u32 %[kw], %[kw], 1\n\t"
        "s_cmp_eq_u32 %[kw], 28\n\t"
        "s_cselect_b32 %[kw], 0, %[kw]\n\t"
        "v_mfma_f32_32x32x16_bf16 v[32:47], v[72:75], v[108:111], v[32:47]\n\t"
        "v_mfma_f32_32x32x16_bf16 v[48:63], v[76:79], v[108:111], v[48:63]\n\t"
        "s_add_u32 %[st], %[z2], %[kz]\n\t"
        "buffer_load_dwordx4 v[104:107], %[zv], %[rz], %[st] offen sc1\n\t"
        "buffer_load_dwordx4 v[108:111], %[zu], %[rz], %[st] offen sc1\n\t"
        "s_add_u32 %[kz], %[kz], 1024\n\t"
        "s_cmp_eq_u32 %[kz], 28672\n\t"
        "s_cselect_b32 %[kz], 0, %[kz]\n\t"
        "s_waitcnt vmcnt(10) lgkmcnt(0)\n\t"
        "v_mfma_f32_32x32x16_bf16 v[0:15], v[64:67], v[80:83], v[0:15]\n\t"
        "ds_read_b64_tr_b16 v[72:73], %[wb0] offset:2048\n\t"
        "ds_read_b64_tr_b16 v[74:75], %[wb0] offset:3072\n\t"
        "ds_read_b64_tr_b16 v[76:77], %[wb1] offset:2048\n\t"
        "ds_read_b64_tr_b16 v[78:79], %[wb1] offset:3072\n\t"
        "v_mfma_f32_32x32x16_bf16 v[16:31], v[68:71], v[80:83], v[16:31]\n\t"
        "s_add_u32 m0, %[wd], 8192\n\t"
        "s_lshl_b32 %[st], %[kw], 14\n\t"
        "s_add_u32 %[st], %[st], %[s2]\n\t"
        "buffer_load_dwordx4 %[wv], %[r2], %[st] offen lds\n\t"
        "s_add_u32 m0, %[wd], 9216\n\t"
        "s_add_u32 %[st], %[st], 8192\n\t"
        "buffer_load_dwordx4 %[wv], %[r2], %[st] offen lds\n\t"
        "s_add_u32 %[kw], %[kw], 1\n\t"
        "s_cmp_eq_u32 %[kw], 28\n\t"
        "s_cselect_b32 %[kw], 0, %[kw]\n\t"
        "v_mfma_f32_32x32x16_bf16 v[32:47], v[64:67], v[84:87], v[32:47]\n\t"
        "v_mfma_f32_32x32x16_bf16 v[48:63], v[68:71], v[84:87], v[48:63]\n\t"
        "s_add_u32 %[st], %[z2], %[kz]\n\t"
        "buffer_load_dwordx4 v[80:83], %[zv], %[rz], %[st] offen sc1\n\t"
        "buffer_load_dwordx4 v[84:87], %[zu], %[rz], %[st] offen sc1\n\t"
        "s_add_u32 %[kz], %[kz], 1024\n\t"
        "s_cmp_eq_u32 %[kz], 28672\n\t"
        "s_cselect_b32 %[kz], 0, %[kz]\n\t"
        "s_waitcnt vmcnt(10) lgkmcnt(0)\n\t"
        "v_mfma_f32_32x32x16_bf16 v[0:15], v[72:75], v[88:91], v[0:15]\n\t"
        "ds_read_b64_tr_b16 v[64:65], %[wb0] offset:4096\n\t"
        "ds_read_b64_tr_b16 v[66:67], %[wb0] offset:5120\n\t"
        "ds_read_b64_tr_b16 v[68:69], %[wb1] offset:4096\n\t"
        "ds_read_b64_tr_b16 v[70:71], %[wb1] offset:5120\n\t"
        "v_mfma_f32_32x32x16_bf16 v[16:31], v[76:79], v[88:91], v[16:31]\n\t"
        "s_add_u32 m0, %[wd], 10240\n\t"
        "s_lshl_b32 %[st], %[kw], 14\n\t"
        "s_add_u32 %[st], %[st], %[s2]\n\t"
        "buffer_load_dwordx4 %[wv], %[r2], %[st] offen lds\n\t"
        "s_add_u32 m0, %[wd], 11264\n\t"
        "s_add_u32 %[st], %[st], 8192\n\t"
        "buffer_load_dwordx4 %[wv], %[r2], %[st] offen lds\n\t"
        "s_add_u32 %[kw], %[kw], 1\n\t"
        "s_cmp_eq_u32 %[kw], 28\n\t"
        "s_cselect_b32 %[kw], 0, %[kw]\n\t"
        "v_mfma_f32_32x32x16_bf16 v[32:47], v[72:75], v[92:95], v[32:47]\n\t"
        "v_mfma_f32_32x32x16_bf16 v[48:63], v[76:79], v[92:95], v[48:63]\n\t"
        "s_add_u32 %[st], %[z2], %[kz]\n\t"
        "buffer_load_dwordx4 v[88:91], %[zv], %[rz], %[st] offen sc1\n\t"
        "buffer_load_dwordx4 v[92:95], %[zu], %[rz], %[st] offen sc1\n\t"
        "s_add_u32 %[kz], %[kz], 1024\n\t"
        "s_cmp_eq_u32 %[kz], 28672\n\t"
        "s_cselect_b32 %[kz], 0, %[kz]\n\t"
        "s_waitcnt vmcnt(10) lgkmcnt(0)\n\t"
        "v_mfma_f32_32x32x16_bf16 v[0:15], v[64:67], v[96:99], v[0:15]\n\t"
        "ds_read_b64_tr_b16 v[72:73], %[wb0] offset:6144\n\t"
        "ds_read_b64_tr_b16 v[74:75], %[wb0] offset:7168\n\t"
        "ds_read_b64_tr_b16 v[76:77], %[wb1] offset:6144\n\t"
        "ds_read_b64_tr_b16 v[78:79], %[wb1] offset:7168\n\t"
        "v_mfma_f32_32x32x16_bf16 v[16:31], v[68:71], v[96:99], v[16:31]\n\t"
        "s_add_u32 m0, %[wd], 12288\n\t"
        "s_lshl_b32 %[st], %[kw], 14\n\t"
        "s_add_u32 %[st], %[st], %[s2]\n\t"
        "buffer_load_dwordx4 %[wv], %[r2], %[st] offen lds\n\t"
        "s_add_u32 m0, %[wd], 13312\n\t"
        "s_add_u32 %[st], %[st], 8192\n\t"
        "buffer_load_dwordx4 %[wv], %[r2], %[st] offen lds\n\t"
        "s_add_u32 %[kw], %[kw], 1\n\t"
        "s_cmp_eq_u32 %[kw], 28\n\t"
        "s_cselect_b32 %[kw], 0, %[kw]\n\t"
        "v_mfma_f32_32x32x16_bf16 v[32:47], v[64:67], v[100:103], v[32:47]\n\t"
        "v_mfma_f32_32x32x16_bf16 v[48:63], v[68:71], v[100:103], v[48:63]\n\t"
        "s_add_u32 %[st], %[z2], %[kz]\n\t"
        "buffer_load_dwordx4 v[96:99], %[zv], %[rz], %[st] offen sc1\n\t"
        "buffer_load_dwordx4 v[100:103], %[zu], %[rz], %[st] offen sc1\n\t"
        "s_add_u32 %[kz], %[kz], 1024\n\t"
        "s_cmp_eq_u32 %[kz], 28672\n\t"
        "s_cselect_b32 %[kz], 0, %[kz]\n\t"
        "s_waitcnt vmcnt(10) lgkmcnt(0)\n\t"
        "v_mfma_f32_32x32x16_bf16 v[0:15], v[72:75], v[104:107], v[0:15]\n\t"
        "ds_read_b64_tr_b16 v[64:65], %[wb0] offset:8192\n\t"
        "ds_read_b64_tr_b16 v[66:67], %[wb0] offset:9216\n\t"
        "ds_read_b64_tr_b16 v[68:69], %[wb1] offset:8192\n\t"
        "ds_read_b64_tr_b16 v[70:71], %[wb1] offset:9216\n\t"
        "v_mfma_f32_32x32x16_bf16 v[16:31], v[76:79], v[104:107], v[16:31]\n\t"
        "s_add_u32 m0, %[wd], 14336\n\t"
        "s_lshl_b32 %[st], %[kw], 14\n\t"
        "s_add_u32 %[st], %[st], %[s2]\n\t"
        "buffer_load_dwordx4 %[wv], %[r2], %[st] offen lds\n\t"
        "s_add_u32 m0, %[wd], 15360\n\t"
        "s_add_u32 %[st], %[st], 8192\n\t"
        "buffer_load_dwordx4 %[wv], %[r2], %[st] offen lds\n\t"
        "s_add_u32 %[kw], %[kw], 1\n\t"
        "s_cmp_eq_u32 %[kw], 28\n\t"
        "s_cselect_b32 %[kw], 0, %[kw]\n\t"
        "v_mfma_f32_32x32x16_bf16 v[32:47], v[72:75], v[108:111], v[32:47]\n\t"
        "v_mfma_f32_32x32x16_bf16 v[48:63], v[76:79], v[108:111], v[48:63]\n\t"
        "s_add_u32 %[st], %[z2], %[kz]\n\t"
        "buffer_load_dwordx4 v[104:107], %[zv], %[rz], %[st] offen sc1\n\t"
        "buffer_load_dwordx4 v[108:111], %[zu], %[rz], %[st] offen sc1\n\t"
        "s_add_u32 %[kz], %[kz], 1024\n\t"
        "s_cmp_eq_u32 %[kz], 28672\n\t"
        "s_cselect_b32 %[kz], 0, %[kz]\n\t"
        "s_waitcnt vmcnt(10) lgkmcnt(0)\n\t"
        "v_mfma_f32_32x32x16_bf16 v[0:15], v[64:67], v[80:83], v[0:15]\n\t"
        "ds_read_b64_tr_b16 v[72:73], %[wb0] offset:10240\n\t"
        "ds_read_b64_tr_b16 v[74:75], %[wb0] offset:11264\n\t"
        "ds_read_b64_tr_b16 v[76:77], %[wb1] offset:10240\n\t"
        "ds_read_b64_tr_b16 v[78:79], %[wb1] offset:11264\n\t"
        "v_mfma_f32_32x32x16_bf16 v[16:31], v[68:71], v[80:83], v[16:31]\n\t"
        "s_add_u32 m0, %[wd], 0\n\t"
        "s_lshl_b32 %[st], %[kw], 14\n\t"
        "s_add_u32 %[st], %[st], %[s2]\n\t"
        "buffer_load_dwordx4 %[wv], %[r2], %[st] offen lds\n\t"
        "s_add_u32 m0, %[wd], 1024\n\t"
        "s_add_u32 %[st], %[st], 8192\n\t"
        "buffer_load_dwordx4 %[wv], %[r2], %[st] offen lds\n\t"
        "s_add_u32 %[kw], %[kw], 1\n\t"
        "s_cmp_eq_u32 %[kw], 28\n\t"
        "s_cselect_b32 %[kw], 0, %[kw]\n\t"
        "v_mfma_f32_32x32x16_bf16 v[32:47], v[64:67], v[84:87], v[32:47]\n\t"
        "v_mfma_f32_32x32x16_bf16 v[48:63], v[68:71], v[84:87], v[48:63]\n\t"
        "s_add_u32 %[st], %[z2], %[kz]\n\t"
        "buffer_load_dwordx4 v[80:83], %[zv], %[rz], %[st] offen sc1\n\t"
        "buffer_load_dwordx4 v[84:87], %[zu], %[rz], %[st] offen sc1\n\t"
        "s_add_u32 %[kz], %[kz], 1024\n\t"
        "s_cmp_eq_u32 %[kz], 28672\n\t"
        "s_cselect_b32 %[kz], 0, %[kz]\n\t"
        "s_waitcnt vmcnt(10) lgkmcnt(0)\n\t"
        "v_mfma_f32_32x32x16_bf16 v[0:15], v[72:75], v[88:91], v[0:15]\n\t"
        "ds_read_b64_tr_b16 v[64:65], %[wb0] offset:12288\n\t"
        "ds_read_b64_tr_b16 v[66:67], %[wb0] offset:13312\n\t"
        "ds_read_b64_tr_b16 v[68:69], %[wb1] offset:12288\n\t"
        "ds_read_b64_tr_b16 v[70:71], %[wb1] offset:13312\n\t"
        "v_mfma_f32_32x32x16_bf16 v[16:31], v[76:79], v[88:91], v[16:31]\n\t"
        "s_add_u32 m0, %[wd], 2048\n\t"
        "s_lshl_b32 %[st], %[kw], 14\n\t"
        "s_add_u32 %[st], %[st], %[s2]\n\t"
        "buffer_load_dwordx4 %[wv], %[r2], %[st] offen lds\n\t"
        "s_add_u32 m0, %[wd], 3072\n\t"
        "s_add_u32 %[st], %[st], 8192\n\t"
        "buffer_load_dwordx4 %[wv], %[r2], %[st] offen lds\n\t"
        "s_add_u32 %[kw], %[kw], 1\n\t"
        "s_cmp_eq_u32 %[kw], 28\n\t"
        "s_cselect_b32 %[kw], 0, %[kw]\n\t"
        "v_mfma_f32_32x32x16_bf16 v[32:47], v[72:75], v[92:95], v[32:47]\n\t"
        "v_mfma_f32_32x32x16_bf16 v[48:63], v[76:79], v[92:95], v[48:63]\n\t"
        "s_add_u32 %[st], %[z2], %[kz]\n\t"
        "buffer_load_dwordx4 v[88:91], %[zv], %[rz], %[st] offen sc1\n\t"
        "buffer_load_dwordx4 v[92:95], %[zu], %[rz], %[st] offen sc1\n\t"
        "s_add_u32 %[kz], %[kz], 1024\n\t"
        "s_cmp_eq_u32 %[kz], 28672\n\t"
        "s_cselect_b32 %[kz], 0, %[kz]\n\t"
        "s_waitcnt vmcnt(10) lgkmcnt(0)\n\t"
        "v_mfma_f32_32x32x16_bf16 v[0:15], v[64:67], v[96:99], v[0:15]\n\t"
        "ds_read_b64_tr_b16 v[72:73], %[wb0] offset:14336\n\t"
        "ds_read_b64_tr_b16 v[74:75], %[wb0] offset:15360\n\t"
        "ds_read_b64_tr_b16 v[76:77], %[wb1] offset:14336\n\t"
        "ds_read_b64_tr_b16 v[78:79], %[wb1] offset:15360\n\t"
        "v_mfma_f32_32x32x16_bf16 v[16:31], v[68:71], v[96:99], v[16:31]\n\t"
        "s_add_u32 m0, %[wd], 4096\n\t"
        "s_lshl_b32 %[st], %[kw], 14\n\t"
        "s_add_u32 %[st], %[st], %[s2]\n\t"
        "buffer_load_dwordx4 %[wv], %[r2], %[st] offen lds\n\t"
        "s_add_u32 m0, %[wd], 5120\n\t"
        "s_add_u32 %[st], %[st], 8192\n\t"
        "buffer_load_dwordx4 %[wv], %[r2], %[st] offen lds\n\t"
        "s_add_u32 %[kw], %[kw], 1\n\t"
        "s_cmp_eq_u32 %[kw], 28\n\t"
        "s_cselect_b32 %[kw], 0, %[kw]\n\t"
        "v_mfma_f32_32x32x16_bf16 v[32:47], v[64:67], v[100:103], v[32:47]\n\t"
        "v_mfma_f32_32x32x16_bf16 v[48:63], v[68:71], v[100:103], v[48:63]\n\t"
        "s_add_u32 %[st], %[z2], %[kz]\n\t"
        "buffer_load_dwordx4 v[96:99], %[zv], %[rz], %[st] offen sc1\n\t"
        "buffer_load_dwordx4 v[100:103], %[zu], %[rz], %[st] offen sc1\n\t"
        "s_add_u32 %[kz], %[kz], 1024\n\t"
        "s_cmp_eq_u32 %[kz], 28672\n\t"
        "s_cselect_b32 %[kz], 0, %[kz]\n\t"
        "s_waitcnt vmcnt(10) lgkmcnt(0)\n\t"
        "v_mfma_f32_32x32x16_bf16 v[0:15], v[72:75], v[104:107], v[0:15]\n\t"
        "ds_read_b64_tr_b16 v[64:65], %[wb0] offset:0\n\t"
        "ds_read_b64_tr_b16 v[66:67], %[wb0] offset:1024\n\t"
        "ds_read_b64_tr_b16 v[68:69], %[wb1] offset:0\n\t"
        "ds_read_b64_tr_b16 v[70:71], %[wb1] offset:1024\n\t"
        "v_mfma_f32_32x32x16_bf16 v[16:31], v[76:79], v[104:107], v[16:31]\n\t"
        "s_add_u32 m0, %[wd], 6144\n\t"
        "s_lshl_b32 %[st], %[kw], 14\n\t"
        "s_add_u32 %[st], %[st], %[s2]\n\t"
        "buffer_load_dwordx4 %[wv], %[r2], %[st] offen lds\n\t"
        "s_add_u32 m0, %[wd], 7168\n\t"
        "s_add_u32 %[st], %[st], 8192\n\t"
        "buffer_load_dwordx4 %[wv], %[r2], %[st] offen lds\n\t"
        "s_add_u32 %[kw], %[kw], 1\n\t"
        "s_cmp_eq_u32 %[kw], 28\n\t"
        "s_cselect_b32 %[kw], 0, %[kw]\n\t"
        "v_mfma_f32_32x32x16_bf16 v[32:47], v[72:75], v[108:111], v[32:47]\n\t"
        "v_mfma_f32_32x32x16_bf16 v[48:63], v[76:79], v[108:111], v[48:63]\n\t"
        "s_add_u32 %[st], %[z2], %[kz]\n\t"
        "buffer_load_dwordx4 v[104:107], %[zv], %[rz], %[st] offen sc1\n\t"
        "buffer_load_dwordx4 v[108:111], %[zu], %[rz], %[st] offen sc1\n\t"
        "s_add_u32 %[kz], %[kz], 1024\n\t"
        "s_cmp_eq_u32 %[kz], 28672\n\t"
        "s_cselect_b32 %[kz], 0, %[kz]\n\t"
        "s_waitcnt vmcnt(10) lgkmcnt(0)\n\t"
        "v_mfma_f32_32x32x16_bf16 v[0:15], v[64:67], v[80:83], v[0:15]\n\t"
        "ds_read_b64_tr_b16 v[72:73], %[wb0] offset:2048\n\t"
        "ds_read_b64_tr_b16 v[74:75], %[wb0] offset:3072\n\t"
        "ds_read_b64_tr_b16 v[76:77], %[wb1] offset:2048\n\t"
        "ds_read_b64_tr_b16 v[78:79], %[wb1] offset:3072\n\t"
        "v_mfma_f32_32x32x16_bf16 v[16:31], v[68:71], v[80:83], v[16:31]\n\t"
        "s_add_u32 m0, %[wd], 8192\n\t"
        "s_lshl_b32 %[st], %[kw], 14\n\t"
        "s_add_u32 %[st], %[st], %[s2]\n\t"
        "buffer_load_dwordx4 %[wv], %[r2], %[st] offen lds\n\t"
        "s_add_u32 m0, %[wd], 9216\n\t"
        "s_add_u32 %[st], %[st], 8192\n\t"
        "buffer_load_dwordx4 %[wv], %[r2], %[st] offen lds\n\t"
        "s_add_u32 %[kw], %[kw], 1\n\t"
        "s_cmp_eq_u32 %[kw], 28\n\t"
        "s_cselect_b32 %[kw], 0, %[kw]\n\t"
        "v_mfma_f32_32x32x16_bf16 v[32:47], v[64:67], v[84:87], v[32:47]\n\t"
        "v_mfma_f32_32x32x16_bf16 v[48:63], v[68:71], v[84:87], v[48:63]\n\t"
        "s_add_u32 %[st], %[z2], %[kz]\n\t"
        "buffer_load_dwordx4 v[80:83], %[zv], %[rz], %[st] offen sc1\n\t"
        "buffer_load_dwordx4 v[84:87], %[zu], %[rz], %[st] offen sc1\n\t"
        "s_add_u32 %[kz], %[kz], 1024\n\t"
        "s_cmp_eq_u32 %[kz], 28672\n\t"
        "s_cselect_b32 %[kz], 0, %[kz]\n\t"
        "s_waitcnt vmcnt(10) lgkmcnt(0)\n\t"
        "v_mfma_f32_32x32x16_bf16 v[0:15], v[72:75], v[88:91], v[0:15]\n\t"
        "ds_read_b64_tr_b16 v[64:65], %[wb0] offset:4096\n\t"
        "ds_read_b64_tr_b16 v[66:67], %[wb0] offset:5120\n\t"
        "ds_read_b64_tr_b16 v[68:69], %[wb1] offset:4096\n\t"
        "ds_read_b64_tr_b16 v[70:71], %[wb1] offset:5120\n\t"
        "v_mfma_f32_32x32x16_bf16 v[16:31], v[76:79], v[88:91], v[16:31]\n\t"
        "s_add_u32 m0, %[wd], 10240\n\t"
        "s_lshl_b32 %[st], %[kw], 14\n\t"
        "s_add_u32 %[st], %[st], %[s2]\n\t"
        "buffer_load_dwordx4 %[wv], %[r2], %[st] offen lds\n\t"
        "s_add_u32 m0, %[wd], 11264\n\t"
        "s_add_u32 %[st], %[st], 8192\n\t"
        "buffer_load_dwordx4 %[wv], %[r2], %[st] offen lds\n\t"
        "s_add_u32 %[kw], %[kw], 1\n\t"
        "s_cmp_eq_u32 %[kw], 28\n\t"
        "s_cselect_b32 %[kw], 0, %[kw]\n\t"
        "v_mfma_f32_32x32x16_bf16 v[32:47], v[72:75], v[92:95], v[32:47]\n\t"
        "v_mfma_f32_32x32x16_bf16 v[48:63], v[76:79], v[92:95], v[48:63]\n\t"
        "s_add_u32 %[st], %[z2], %[kz]\n\t"
        "buffer_load_dwordx4 v[88:91], %[zv], %[rz], %[st] offen sc1\n\t"
        "buffer_load_dwordx4 v[92:95], %[zu], %[rz], %[st] offen sc1\n\t"
        "s_add_u32 %[kz], %[kz], 1024\n\t"
        "s_cmp_eq_u32 %[kz], 28672\n\t"
        "s_cselect_b32 %[kz], 0, %[kz]\n\t"
        "s_waitcnt vmcnt(10) lgkmcnt(0)\n\t"
        "v_mfma_f32_32x32x16_bf16 v[0:15], v[64:67], v[96:99], v[0:15]\n\t"
        "ds_read_b64_tr_b16 v[72:73], %[wb0] offset:6144\n\t"
        "ds_read_b64_tr_b16 v[74:75], %[wb0] offset:7168\n\t"
        "ds_read_b64_tr_b16 v[76:77], %[wb1] offset:6144\n\t"
        "ds_read_b64_tr_b16 v[78:79], %[wb1] offset:7168\n\t"
        "v_mfma_f32_32x32x16_bf16 v[16:31], v[68:71], v[96:99], v[16:31]\n\t"
        "s_add_u32 m0, %[wd], 12288\n\t"
        "s_lshl_b32 %[st], %[kw], 14\n\t"
        "s_add_u32 %[st], %[st], %[s2]\n\t"
        "buffer_load_dwordx4 %[wv], %[r2], %[st] offen lds\n\t"
        "s_add_u32 m0, %[wd], 13312\n\t"
        "s_add_u32 %[st], %[st], 8192\n\t"
        "buffer_load_dwordx4 %[wv], %[r2], %[st] offen lds\n\t"
        "s_add_u32 %[kw], %[kw], 1\n\t"
        "s_cmp_eq_u32 %[kw], 28\n\t"
        "s_cselect_b32 %[kw], 0, %[kw]\n\t"
        "v_mfma_f32_32x32x16_bf16 v[32:47], v[64:67], v[100:103], v[32:47]\n\t"
        "v_mfma_f32_32x32x16_bf16 v[48:63], v[68:71], v[100:103], v[48:63]\n\t"
        "s_add_u32 %[st], %[z2], %[kz]\n\t"
        "buffer_load_dwordx4 v[96:99], %[zv], %[rz], %[st] offen sc1\n\t"
        "buffer_load_dwordx4 v[100:103], %[zu], %[rz], %[st] offen sc1\n\t"
        "s_add_u32 %[kz], %[kz], 1024\n\t"
        "s_cmp_eq_u32 %[kz], 28672\n\t"
        "s_cselect_b32 %[kz], 0, %[kz]\n\t"
        "s_waitcnt vmcnt(10) lgkmcnt(0)\n\t"
        "v_mfma_f32_32x32x16_bf16 v[0:15], v[72:75], v[104:107], v[0:15]\n\t"
        "ds_read_b64_tr_b16 v[64:65], %[wb0] offset:8192\n\t"
        "ds_read_b64_tr_b16 v[66:67], %[wb0] offset:9216\n\t"
        "ds_read_b64_tr_b16 v[68:69], %[wb1] offset:8192\n\t"
        "ds_read_b64_tr_b16 v[70:71], %[wb1] offset:9216\n\t"
        "v_mfma_f32_32x32x16_bf16 v[16:31], v[76:79], v[104:107], v[16:31]\n\t"
        "s_add_u32 m0, %[wd], 14336\n\t"
        "s_lshl_b32 %[st], %[kw], 14\n\t"
        "s_add_u32 %[st], %[st], %[s2]\n\t"
        "buffer_load_dwordx4 %[wv], %[r2], %[st] offen lds\n\t"
        "s_add_u32 m0, %[wd], 15360\n\t"
        "s_add_u32 %[st], %[st], 8192\n\t"
        "buffer_load_dwordx4 %[wv], %[r2], %[st] offen lds\n\t"
        "s_add_u32 %[kw], %[kw], 1\n\t"
        "s_cmp_eq_u32 %[kw], 28\n\t"
        "s_cselect_b32 %[kw], 0, %[kw]\n\t"
        "v_mfma_f32_32x32x16_bf16 v[32:47], v[72:75], v[108:111], v[32:47]\n\t"
        "v_mfma_f32_32x32x16_bf16 v[48:63], v[76:79], v[108:111], v[48:63]\n\t"
        "s_add_u32 %[st], %[z2], %[kz]\n\t"
        "buffer_load_dwordx4 v[104:107], %[zv], %[rz], %[st] offen sc1\n\t"
        "buffer_load_dwordx4 v[108:111], %[zu], %[rz], %[st] offen sc1\n\t"
        "s_add_u32 %[kz], %[kz], 1024\n\t"
        "s_cmp_eq_u32 %[kz], 28672\n\t"
        "s_cselect_b32 %[kz], 0, %[kz]\n\t"
        "s_waitcnt vmcnt(10) lgkmcnt(0)\n\t"
        "v_mfma_f32_32x32x16_bf16 v[0:15], v[64:67], v[80:83], v[0:15]\n\t"
        "ds_read_b64_tr_b16 v[72:73], %[wb0] offset:10240\n\t"
        "ds_read_b64_tr_b16 v[74:75], %[wb0] offset:11264\n\t"
        "ds_read_b64_tr_b16 v[76:77], %[wb1] offset:10240\n\t"
        "ds_read_b64_tr_b16 v[78:79], %[wb1] offset:11264\n\t"
        "v_mfma_f32_32x32x16_bf16 v[16:31], v[68:71], v[80:83], v[16:31]\n\t"
        "s_add_u32 m0, %[wd], 0\n\t"
        "s_lshl_b32 %[st], %[kw], 14\n\t"
        "s_add_u32 %[st], %[st], %[s2]\n\t"
        "buffer_load_dwordx4 %[wv], %[r2], %[st] offen lds\n\t"
        "s_add_u32 m0, %[wd], 1024\n\t"
        "s_add_u32 %[st], %[st], 8192\n\t"
        "buffer_load_dwordx4 %[wv], %[r2], %[st] offen lds\n\t"
        "s_add_u32 %[kw], %[kw], 1\n\t"
        "s_cmp_eq_u32 %[kw], 28\n\t"
        "s_cselect_b32 %[kw], 0, %[kw]\n\t"
        "v_mfma_f32_32x32x16_bf16 v[32:47], v[64:67], v[84:87], v[32:47]\n\t"
        "v_mfma_f32_32x32x16_bf16 v[48:63], v[68:71], v[84:87], v[48:63]\n\t"
        "s_add_u32 %[st], %[z2], %[kz]\n\t"
        "buffer_load_dwordx4 v[80:83], %[zv], %[rz], %[st] offen sc1\n\t"
        "buffer_load_dwordx4 v[84:87], %[zu], %[rz], %[st] offen sc1\n\t"
        "s_add_u32 %[kz], %[kz], 1024\n\t"
        "s_cmp_eq_u32 %[kz], 28672\n\t"
        "s_cselect_b32 %[kz], 0, %[kz]\n\t"
        "s_waitcnt vmcnt(10) lgkmcnt(0)\n\t"
        "v_mfma_f32_32x32x16_bf16 v[0:15], v[72:75], v[88:91], v[0:15]\n\t"
        "ds_read_b64_tr_b16 v[64:65], %[wb0] offset:12288\n\t"
        "ds_read_b64_tr_b16 v[66:67], %[wb0] offset:13312\n\t"
        "ds_read_b64_tr_b16 v[68:69], %[wb1] offset:12288\n\t"
        "ds_read_b64_tr_b16 v[70:71], %[wb1] offset:13312\n\t"
        "v_mfma_f32_32x32x16_bf16 v[16:31], v[76:79], v[88:91], v[16:31]\n\t"
        "s_add_u32 m0, %[wd], 2048\n\t"
        "s_lshl_b32 %[st], %[kw], 14\n\t"
        "s_add_u32 %[st], %[st], %[s2]\n\t"
        "buffer_load_dwordx4 %[wv], %[r2], %[st] offen lds\n\t"
        "s_add_u32 m0, %[wd], 3072\n\t"
        "s_add_u32 %[st], %[st], 8192\n\t"
        "buffer_load_dwordx4 %[wv], %[r2], %[st] offen lds\n\t"
        "s_add_u32 %[kw], %[kw], 1\n\t"
        "s_cmp_eq_u32 %[kw], 28\n\t"
        "s_cselect_b32 %[kw], 0, %[kw]\n\t"
        "v_mfma_f32_32x32x16_bf16 v[32:47], v[72:75], v[92:95], v[32:47]\n\t"
        "v_mfma_f32_32x32x16_bf16 v[48:63], v[76:79], v[92:95], v[48:63]\n\t"
        "s_add_u32 %[st], %[z2], %[kz]\n\t"
        "buffer_load_dwordx4 v[88:91], %[zv], %[rz], %[st] offen sc1\n\t"
        "buffer_load_dwordx4 v[92:95], %[zu], %[rz], %[st] offen sc1\n\t"
        "s_add_u32 %[kz], %[kz], 1024\n\t"
        "s_cmp_eq_u32 %[kz], 28672\n\t"
        "s_cselect_b32 %[kz], 0, %[kz]\n\t"
        "s_waitcnt vmcnt(10) lgkmcnt(0)\n\t"
        "v_mfma_f32_32x32x16_bf16 v[0:15], v[64:67], v[96:99], v[0:15]\n\t"
        "ds_read_b64_tr_b16 v[72:73], %[wb0] offset:14336\n\t"
        "ds_read_b64_tr_b16 v[74:75], %[wb0] offset:15360\n\t"
        "ds_read_b64_tr_b16 v[76:77], %[wb1] offset:14336\n\t"
        "ds_read_b64_tr_b16 v[78:79], %[wb1] offset:15360\n\t"
        "v_mfma_f32_32x32x16_bf16 v[16:31], v[68:71], v[96:99], v[16:31]\n\t"
        "s_add_u32 m0, %[wd], 4096\n\t"
        "s_lshl_b32 %[st], %[kw], 14\n\t"
        "s_add_u32 %[st], %[st], %[s2]\n\t"
        "buffer_load_dwordx4 %[wv], %[r2], %[st] offen lds\n\t"
        "s_add_u32 m0, %[wd], 5120\n\t"
        "s_add_u32 %[st], %[st], 8192\n\t"
        "buffer_load_dwordx4 %[wv], %[r2], %[st] offen lds\n\t"
        "s_add_u32 %[kw], %[kw], 1\n\t"
        "s_cmp_eq_u32 %[kw], 28\n\t"
        "s_cselect_b32 %[kw], 0, %[kw]\n\t"
        "v_mfma_f32_32x32x16_bf16 v[32:47], v[64:67], v[100:103], v[32:47]\n\t"
        "v_mfma_f32_32x32x16_bf16 v[48:63], v[68:71], v[100:103], v[48:63]\n\t"
        "s_add_u32 %[st], %[z2], %[kz]\n\t"
        "buffer_load_dwordx4 v[96:99], %[zv], %[rz], %[st] offen sc1\n\t"
        "buffer_load_dwordx4 v[100:103], %[zu], %[rz], %[st] offen sc1\n\t"
        "s_add_u32 %[kz], %[kz], 1024\n\t"
        "s_cmp_eq_u32 %[kz], 28672\n\t"
        "s_cselect_b32 %[kz], 0, %[kz]\n\t"
        "s_waitcnt vmcnt(10) lgkmcnt(0)\n\t"
        "v_mfma_f32_32x32x16_bf16 v[0:15], v[72:75], v[104:107], v[0:15]\n\t"
        "ds_read_b64_tr_b16 v[64:65], %[wb0] offset:0\n\t"
        "ds_read_b64_tr_b16 v[66:67], %[wb0] offset:1024\n\t"
        "ds_read_b64_tr_b16 v[68:69], %[wb1] offset:0\n\t"
        "ds_read_b64_tr_b16 v[70:71], %[wb1] offset:1024\n\t"
        "v_mfma_f32_32x32x16_bf16 v[16:31], v[76:79], v[104:107], v[16:31]\n\t"
        "s_add_u32 m0, %[wd], 6144\n\t"
        "s_lshl_b32 %[st], %[kw], 14\n\t"
        "s_add_u32 %[st], %[st], %[s2]\n\t"
        "buffer_load_dwordx4 %[wv], %[r2], %[st] offen lds\n\t"
        "s_add_u32 m0, %[wd], 7168\n\t"
        "s_add_u32 %[st], %[st], 8192\n\t"
        "buffer_load_dwordx4 %[wv], %[r2], %[st] offen lds\n\t"
        "v_mfma_f32_32x32x16_bf16 v[32:47], v[72:75], v[108:111], v[32:47]\n\t"
        "v_mfma_f32_32x32x16_bf16 v[48:63], v[76:79], v[108:111], v[48:63]\n\t"
        "s_add_u32 %[st], %[z2], %[kz]\n\t"
        "buffer_load_dwordx4 v[104:107], %[zv], %[rz], %[st] offen sc1\n\t"
        "buffer_load_dwordx4 v[108:111], %[zu], %[rz], %[st] offen sc1\n\t"
        "s_waitcnt vmcnt(10) lgkmcnt(0)\n\t"
        "v_mfma_f32_32x32x16_bf16 v[0:15], v[64:67], v[80:83], v[0:15]\n\t"
        "ds_read_b64_tr_b16 v[72:73], %[wb0] offset:2048\n\t"
        "ds_read_b64_tr_b16 v[74:75], %[wb0] offset:3072\n\t"
        "ds_read_b64_tr_b16 v[76:77], %[wb1] offset:2048\n\t"
        "ds_read_b64_tr_b16 v[78:79], %[wb1] offset:3072\n\t"
        "v_mfma_f32_32x32x16_bf16 v[16:31], v[68:71], v[80:83], v[16:31]\n\t"
        "s_add_u32 m0, %[wd], 8192\n\t"
        "s_add_u32 %[st], %[so], 0\n\t"
        "buffer_load_dwordx4 %[wv], %[ro], %[st] offen lds\n\t"
        "s_add_u32 m0, %[wd], 9216\n\t"
        "s_add_u32 %[st], %[so], 8192\n\t"
        "buffer_load_dwordx4 %[wv], %[ro], %[st] offen lds\n\t"
        "v_mfma_f32_32x32x16_bf16 v[32:47], v[64:67], v[84:87], v[32:47]\n\t"
        "s_add_u32 m0, %[wd], 10240\n\t"
        "s_add_u32 %[st], %[so], 16384\n\t"
        "buffer_load_dwordx4 %[wv], %[ro], %[st] offen lds\n\t"
        "s_add_u32 m0, %[wd], 11264\n\t"
        "s_add_u32 %[st], %[so], 24576\n\t"
        "buffer_load_dwordx4 %[wv], %[ro], %[st] offen lds\n\t"
        "v_mfma_f32_32x32x16_bf16 v[48:63], v[68:71], v[84:87], v[48:63]\n\t"
        "s_waitcnt vmcnt(10) lgkmcnt(0)\n\t"
        "v_mfma_f32_32x32x16_bf16 v[0:15], v[72:75], v[88:91], v[0:15]\n\t"
        "ds_read_b64_tr_b16 v[64:65], %[wb0] offset:4096\n\t"
        "ds_read_b64_tr_b16 v[66:67], %[wb0] offset:5120\n\t"
        "ds_read_b64_tr_b16 v[68:69], %[wb1] offset:4096\n\t"
        "ds_read_b64_tr_b16 v[70:71], %[wb1] offset:5120\n\t"
        "v_mfma_f32_32x32x16_bf16 v[16:31], v[76:79], v[88:91], v[16:31]\n\t"
        "s_add_u32 m0, %[wd], 12288\n\t"
        "s_add_u32 %[st], %[so], 262144\n\t"
        "buffer_load_dwordx4 %[wv], %[ro], %[st] offen lds\n\t"
        "s_add_u32 m0, %[wd], 13312\n\t"
        "s_add_u32 %[st], %[so], 270336\n\t"
        "buffer_load_dwordx4 %[wv], %[ro], %[st] offen lds\n\t"
        "v_mfma_f32_32x32x16_bf16 v[32:47], v[72:75], v[92:95], v[32:47]\n\t"
        "s_add_u32 m0, %[wd], 14336\n\t"
        "s_add_u32 %[st], %[so], 278528\n\t"
        "buffer_load_dwordx4 %[wv], %[ro], %[st] offen lds\n\t"
        "s_add_u32 m0, %[wd], 15360\n\t"
        "s_add_u32 %[st], %[so], 286720\n\t"
        "buffer_load_dwordx4 %[wv], %[ro], %[st] offen lds\n\t"
        "v_mfma_f32_32x32x16_bf16 v[48:63], v[76:79], v[92:95], v[48:63]\n\t"
        "s_waitcnt vmcnt(10) lgkmcnt(0)\n\t"
        "v_mfma_f32_32x32x16_bf16 v[0:15], v[64:67], v[96:99], v[0:15]\n\t"
        "ds_read_b64_tr_b16 v[72:73], %[wb0] offset:6144\n\t"
        "ds_read_b64_tr_b16 v[74:75], %[wb0] offset:7168\n\t"
        "ds_read_b64_tr_b16 v[76:77], %[wb1] offset:6144\n\t"
        "ds_read_b64_tr_b16 v[78:79], %[wb1] offset:7168\n\t"
        "v_mfma_f32_32x32x16_bf16 v[16:31], v[68:71], v[96:99], v[16:31]\n\t"
        "s_add_u32 m0, %[wd], 0\n\t"
        "s_add_u32 %[st], %[so], 131072\n\t"
        "buffer_load_dwordx4 %[wv], %[ro], %[st] offen lds\n\t"
        "s_add_u32 m0, %[wd], 1024\n\t"
        "s_add_u32 %[st], %[so], 139264\n\t"
        "buffer_load_dwordx4 %[wv], %[ro], %[st] offen lds\n\t"
        "v_mfma_f32_32x32x16_bf16 v[32:47], v[64:67], v[100:103], v[32:47]\n\t"
        "s_add_u32 m0, %[wd], 2048\n\t"
        "s_add_u32 %[st], %[so], 147456\n\t"
        "buffer_load_dwordx4 %[wv], %[ro], %[st] offen lds\n\t"
        "s_add_u32 m0, %[wd], 3072\n\t"
        "s_add_u32 %[st], %[so], 155648\n\t"
        "buffer_load_dwordx4 %[wv], %[ro], %[st] offen lds\n\t"
        "v_mfma_f32_32x32x16_bf16 v[48:63], v[68:71], v[100:103], v[48:63]\n\t"
        "s_waitcnt vmcnt(10) lgkmcnt(0)\n\t"
        "v_mfma_f32_32x32x16_bf16 v[0:15], v[72:75], v[104:107], v[0:15]\n\t"
        "ds_read_b64_tr_b16 v[64:65], %[wb0] offset:8192\n\t"
        "ds_read_b64_tr_b16 v[66:67], %[wb0] offset:9216\n\t"
        "ds_read_b64_tr_b16 v[68:69], %[wb1] offset:8192\n\t"
        "ds_read_b64_tr_b16 v[70:71], %[wb1] offset:9216\n\t"
        "v_mfma_f32_32x32x16_bf16 v[16:31], v[76:79], v[104:107], v[16:31]\n\t"
        "s_add_u32 m0, %[wd], 4096\n\t"
        "s_add_u32 %[st], %[so], 393216\n\t"
        "buffer_load_dwordx4 %[wv], %[ro], %[st] offen lds\n\t"
        "s_add_u32 m0, %[wd], 5120\n\t"
        "s_add_u32 %[st], %[so], 401408\n\t"
        "buffer_load_dwordx4 %[wv], %[ro], %[st] offen lds\n\t"
        "v_mfma_f32_32x32x16_bf16 v[32:47], v[72:75], v[108:111], v[32:47]\n\t"
        "s_add_u32 m0, %[wd], 6144\n\t"
        "s_add_u32 %[st], %[so], 409600\n\t"
        "buffer_load_dwordx4 %[wv], %[ro], %[st] offen lds\n\t"
        "s_add_u32 m0, %[wd], 7168\n\t"
        "s_add_u32 %[st], %[so], 417792\n\t"
        "buffer_load_dwordx4 %[wv], %[ro], %[st] offen lds\n\t"
        "v_mfma_f32_32x32x16_bf16 v[48:63], v[76:79], v[108:111], v[48:63]\n\t"
        "s_mov_b32 m0, %[sv]\n\t"
        "s_waitcnt vmcnt(0) lgkmcnt(0)\n\t"
        "ds_read_b64_tr_b16 v[72:73], %[wb0] offset:10240\n\t"
        "ds_read_b64_tr_b16 v[74:75], %[wb0] offset:11264\n\t"
        "ds_read_b64_tr_b16 v[76:77], %[wb1] offset:10240\n\t"
        "ds_read_b64_tr_b16 v[78:79], %[wb1] offset:11264\n\t"
            "s_setprio 3\n\t"
            "s_mov_b32 %[sp], 0x1000\n"
            ".Lo0_%=:\n\t"
            "ds_read_b32 %[tm], %[hc]\n\t"
            "s_sub_u32 %[sp], %[sp], 1\n\ts_cbranch_scc1 .Lo1_%=\n\t"
            "s_waitcnt lgkmcnt(0)\n\t"
            "v_cmp_gt_u32 vcc, %[lim], %[tm]\n\t"
            "s_cbranch_vccz .Lo1_%=\n\t"
            "s_sleep " "24" "\n\t"
            "s_branch .Lo0_%=\n"
            ".Lo1_%=:\n\t"
            "s_mov_b32 %[sp], 0x10000\n"
            ".Lop_%=:\n\t"
            "ds_read_b32 %[tm], %[hc] offset:8\n\t"
            "s_sub_u32 %[sp], %[sp], 1\n\ts_cbranch_scc1 .Lod_%=\n\t"
            "s_waitcnt lgkmcnt(0)\n\t"
            "v_cmp_gt_u32 vcc, " "2" ", %[tm]\n\t"
            "s_cbranch_vccz .Lod_%=\n\t"
            "s_sleep 3\n\t"
            "s_branch .Lop_%=\n"
            ".Lod_%=:\n\t"
            "s_waitcnt lgkmcnt(0)\n\ts_mov_b32 %[sp], 0x10000\n\t"
        "s_add_u32 %[st], %[zo], 0\n\t"
        "buffer_load_dwordx4 v[80:83], %[zv], %[rz], %[st] offen sc1\n\t"
        "buffer_load_dwordx4 v[84:87], %[zu], %[rz], %[st] offen sc1\n\t"
        "s_add_u32 %[st], %[zo], 1024\n\t"
        "buffer_load_dwordx4 v[88:91], %[zv], %[rz], %[st] offen sc1\n\t"
        "buffer_load_dwordx4 v[92:95], %[zu], %[rz], %[st] offen sc1\n\t"
        "s_add_u32 %[st], %[zo], 16384\n\t"
        "buffer_load_dwordx4 v[96:99], %[zv], %[rz], %[st] offen sc1\n\t"
        "buffer_load_dwordx4 v[100:103], %[zu], %[rz], %[st] offen sc1\n\t"
        "s_add_u32 %[st], %[zo], 17408\n\t"
        "buffer_load_dwordx4 v[104:107], %[zv], %[rz], %[st] offen sc1\n\t"
        "buffer_load_dwordx4 v[108:111], %[zu], %[rz], %[st] offen sc1\n\t"
        "s_waitcnt vmcnt(6)\n\t"
        ".Lz28_%=:\n\t"
        "v_max3_u32 %[tm], v80, v81, v82\n\t"
        "v_max3_u32 %[t2], v83, v84, v85\n\t"
        "v_max3_u32 %[tm], %[tm], v86, v87\n\t"
        "v_max_u32 %[tm], %[tm], %[t2]\n\t"
        "v_cmp_eq_u32 vcc, -1, %[tm]\n\t"
        "s_cbranch_vccz .Ly28_%=\n\t"
        "s_sub_u32 %[sp], %[sp], 1\n\t"
        "s_cbranch_scc1 .Lzx_%=\n\t"
        "s_waitcnt vmcnt(0)\n\t"
        "s_sleep 1\n\t"
        "s_add_u32 %[st], %[zo], 0\n\t"
        "buffer_load_dwordx4 v[80:83], %[zv], %[rz], %[st] offen sc1\n\t"
        "buffer_load_dwordx4 v[84:87], %[zu], %[rz], %[st] offen sc1\n\t"
        "v_max3_u32 %[tm], v88, v89, v90\n\t"
        "v_max3_u32 %[t2], v91, v92, v93\n\t"
        "v_max3_u32 %[tm], %[tm], v94, v95\n\t"
        "v_max_u32 %[tm], %[tm], %[t2]\n\t"
        "v_cmp_eq_u32 vcc, -1, %[tm]\n\t"
        "s_cbranch_vccz .Lr2829_%=\n\t"
        "s_add_u32 %[st], %[zo], 1024\n\t"
        "buffer_load_dwordx4 v[88:91], %[zv], %[rz], %[st] offen sc1\n\t"
        "buffer_load_dwordx4 v[92:95], %[zu], %[rz], %[st] offen sc1\n\t"
        ".Lr2829_%=:\n\t"
        "v_max3_u32 %[tm], v96, v97, v98\n\t"
        "v_max3_u32 %[t2], v99, v100, v101\n\t"
        "v_max3_u32 %[tm], %[tm], v102, v103\n\t"
        "v_max_u32 %[tm], %[tm], %[t2]\n\t"
        "v_cmp_eq_u32 vcc, -1, %[tm]\n\t"
        "s_cbranch_vccz .Lr2830_%=\n\t"
        "s_add_u32 %[st], %[zo], 16384\n\t"
        "buffer_load_dwordx4 v[96:99], %[zv], %[rz], %[st] offen sc1\n\t"
        "buffer_load_dwordx4 v[100:103], %[zu], %[rz], %[st] offen sc1\n\t"
        ".Lr2830_%=:\n\t"
        "v_max3_u32 %[tm], v104, v105, v106\n\t"
        "v_max3_u32 %[t2], v107, v108, v109\n\t"
        "v_max3_u32 %[tm], %[tm], v110, v111\n\t"
        "v_max_u32 %[tm], %[tm], %[t2]\n\t"
        "v_cmp_eq_u32 vcc, -1, %[tm]\n\t"
        "s_cbranch_vccz .Lr2831_%=\n\t"
        "s_add_u32 %[st], %[zo], 17408\n\t"
        "buffer_load_dwordx4 v[104:107], %[zv], %[rz], %[st] offen sc1\n\t"
        "buffer_load_dwordx4 v[108:111], %[zu], %[rz], %[st] offen sc1\n\t"
        ".Lr2831_%=:\n\t"
        "s_waitcnt vmcnt(0)\n\t"
        "s_branch .Lz28_%=\n\t"
        ".Ly28_%=:\n\t"
        "s_waitcnt lgkmcnt(4)\n\t"
        "v_mfma_f32_32x32x16_bf16 v[0:15], v[64:67], v[80:83], v[0:15]\n\t"
        "v_mfma_f32_32x32x16_bf16 v[16:31], v[68:71], v[80:83], v[16:31]\n\t"
        "v_mfma_f32_32x32x16_bf16 v[32:47], v[64:67], v[84:87], v[32:47]\n\t"
        "v_mfma_f32_32x32x16_bf16 v[48:63], v[68:71], v[84:87], v[48:63]\n\t"
        "ds_read_b64_tr_b16 v[64:65], %[wb0] offset:12288\n\t"
        "ds_read_b64_tr_b16 v[66:67], %[wb0] offset:13312\n\t"
        "ds_read_b64_tr_b16 v[68:69], %[wb1] offset:12288\n\t"
        "ds_read_b64_tr_b16 v[70:71], %[wb1] offset:13312\n\t"
        "s_add_u32 %[st], %[zo], 8192\n\t"
        "buffer_load_dwordx4 v[80:83], %[zv], %[rz], %[st] offen sc1\n\t"
        "buffer_load_dwordx4 v[84:87], %[zu], %[rz], %[st] offen sc1\n\t"
        "s_waitcnt vmcnt(6)\n\t"
        ".Lz29_%=:\n\t"
        "v_max3_u32 %[tm], v88, v89, v90\n\t"
        "v_max3_u32 %[t2], v91, v92, v93\n\t"
        "v_max3_u32 %[tm], %[tm], v94, v95\n\t"
        "v_max_u32 %[tm], %[tm], %[t2]\n\t"
        "v_cmp_eq_u32 vcc, -1, %[tm]\n\t"
        "s_cbranch_vccz .Ly29_%=\n\t"
        "s_sub_u32 %[sp], %[sp], 1\n\t"
        "s_cbranch_scc1 .Lzx_%=\n\t"
        "s_waitcnt vmcnt(0)\n\t"
        "s_sleep 1\n\t"
        "s_add_u32 %[st], %[zo], 1024\n\t"
        "buffer_load_dwordx4 v[88:91], %[zv], %[rz], %[st] offen sc1\n\t"
        "buffer_load_dwordx4 v[92:95], %[zu], %[rz], %[st] offen sc1\n\t"
        "v_max3_u32 %[tm], v96, v97, v98\n\t"
        "v_max3_u32 %[t2], v99, v100, v101\n\t"
        "v_max3_u32 %[tm], %[tm], v102, v103\n\t"
        "v_max_u32 %[tm], %[tm], %[t2]\n\t"
        "v_cmp_eq_u32 vcc, -1, %[tm]\n\t"
        "s_cbranch_vccz .Lr2930_%=\n\t"
        "s_add_u32 %[st], %[zo], 16384\n\t"
        "buffer_load_dwordx4 v[96:99], %[zv], %[rz], %[st] offen sc1\n\t"
        "buffer_load_dwordx4 v[100:103], %[zu], %[rz], %[st] offen sc1\n\t"
        ".Lr2930_%=:\n\t"
        "v_max3_u32 %[tm], v104, v105, v106\n\t"
        "v_max3_u32 %[t2], v107, v108, v109\n\t"
        "v_max3_u32 %[tm], %[tm], v110, v111\n\t"
        "v_max_u32 %[tm], %[tm], %[t2]\n\t"
        "v_cmp_eq_u32 vcc, -1, %[tm]\n\t"
        "s_cbranch_vccz .Lr2931_%=\n\t"
        "s_add_u32 %[st], %[zo], 17408\n\t"
        "buffer_load_dwordx4 v[104:107], %[zv], %[rz], %[st] offen sc1\n\t"
        "buffer_load_dwordx4 v[108:111], %[zu], %[rz], %[st] offen sc1\n\t"
        ".Lr2931_%=:\n\t"
        "v_max3_u32 %[tm], v80, v81, v82\n\t"
        "v_max3_u32 %[t2], v83, v84, v85\n\t"
        "v_max3_u32 %[tm], %[tm], v86, v87\n\t"
        "v_max_u32 %[tm], %[tm], %[t2]\n\t"
        "v_cmp_eq_u32 vcc, -1, %[tm]\n\t"
        "s_cbranch_vccz .Lr2932_%=\n\t"
        "s_add_u32 %[st], %[zo], 8192\n\t"
        "buffer_load_dwordx4 v[80:83], %[zv], %[rz], %[st] offen sc1\n\t"
        "buffer_load_dwordx4 v[84:87], %[zu], %[rz], %[st] offen sc1\n\t"
        ".Lr2932_%=:\n\t"
        "s_waitcnt vmcnt(0)\n\t"
        "s_branch .Lz29_%=\n\t"
        ".Ly29_%=:\n\t"
        "s_waitcnt lgkmcnt(4)\n\t"
        "v_mfma_f32_32x32x16_bf16 v[0:15], v[72:75], v[88:91], v[0:15]\n\t"
        "v_mfma_f32_32x32x16_bf16 v[16:31], v[76:79], v[88:91], v[16:31]\n\t"
        "v_mfma_f32_32x32x16_bf16 v[32:47], v[72:75], v[92:95], v[32:47]\n\t"
        "v_mfma_f32_32x32x16_bf16 v[48:63], v[76:79], v[92:95], v[48:63]\n\t"
        "ds_read_b64_tr_b16 v[72:73], %[wb0] offset:14336\n\t"
        "ds_read_b64_tr_b16 v[74:75], %[wb0] offset:15360\n\t"
        "ds_read_b64_tr_b16 v[76:77], %[wb1] offset:14336\n\t"
        "ds_read_b64_tr_b16 v[78:79], %[wb1] offset:15360\n\t"
        "s_add_u32 %[st], %[zo], 9216\n\t"
        "buffer_load_dwordx4 v[88:91], %[zv], %[rz], %[st] offen sc1\n\t"
        "buffer_load_dwordx4 v[92:95], %[zu], %[rz], %[st] offen sc1\n\t"
        "s_waitcnt vmcnt(6)\n\t"
        ".Lz30_%=:\n\t"
        "v_max3_u32 %[tm], v96, v97, v98\n\t"
        "v_max3_u32 %[t2], v99, v100, v101\n\t"
        "v_max3_u32 %[tm], %[tm], v102, v103\n\t"
        "v_max_u32 %[tm], %[tm], %[t2]\n\t"
        "v_cmp_eq_u32 vcc, -1, %[tm]\n\t"
        "s_cbranch_vccz .Ly30_%=\n\t"
        "s_sub_u32 %[sp], %[sp], 1\n\t"
        "s_cbranch_scc1 .Lzx_%=\n\t"
        "s_waitcnt vmcnt(0)\n\t"
        "s_sleep 1\n\t"
        "s_add_u32 %[st], %[zo], 16384\n\t"
        "buffer_load_dwordx4 v[96:99], %[zv], %[rz], %[st] offen sc1\n\t"
        "buffer_load_dwordx4 v[100:103], %[zu], %[rz], %[st] offen sc1\n\t"
        "v_max3_u32 %[tm], v104, v105, v106\n\t"
        "v_max3_u32 %[t2], v107, v108, v109\n\t"
        "v_max3_u32 %[tm], %[tm], v110, v111\n\t"
        "v_max_u32 %[tm], %[tm], %[t2]\n\t"
        "v_cmp_eq_u32 vcc, -1, %[tm]\n\t"
        "s_cbranch_vccz .Lr3031_%=\n\t"
        "s_add_u32 %[st], %[zo], 17408\n\t"
        "buffer_load_dwordx4 v[104:107], %[zv], %[rz], %[st] offen sc1\n\t"
        "buffer_load_dwordx4 v[108:111], %[zu], %[rz], %[st] offen sc1\n\t"
        ".Lr3031_%=:\n\t"
        "v_max3_u32 %[tm], v80, v81, v82\n\t"
        "v_max3_u32 %[t2], v83, v84, v85\n\t"
        "v_max3_u32 %[tm], %[tm], v86, v87\n\t"
        "v_max_u32 %[tm], %[tm], %[t2]\n\t"
        "v_cmp_eq_u32 vcc, -1, %[tm]\n\t"
        "s_cbranch_vccz .Lr3032_%=\n\t"
        "s_add_u32 %[st], %[zo], 8192\n\t"
        "buffer_load_dwordx4 v[80:83], %[zv], %[rz], %[st] offen sc1\n\t"
        "buffer_load_dwordx4 v[84:87], %[zu], %[rz], %[st] offen sc1\n\t"
        ".Lr3032_%=:\n\t"
        "v_max3_u32 %[tm], v88, v89, v90\n\t"
        "v_max3_u32 %[t2], v91, v92, v93\n\t"
        "v_max3_u32 %[tm], %[tm], v94, v95\n\t"
        "v_max_u32 %[tm], %[tm], %[t2]\n\t"
        "v_cmp_eq_u32 vcc, -1, %[tm]\n\t"
        "s_cbranch_vccz .Lr3033_%=\n\t"
        "s_add_u32 %[st], %[zo], 9216\n\t"
        "buffer_load_dwordx4 v[88:91], %[zv], %[rz], %[st] offen sc1\n\t"
        "buffer_load_dwordx4 v[92:95], %[zu], %[rz], %[st] offen sc1\n\t"
        ".Lr3033_%=:\n\t"
        "s_waitcnt vmcnt(0)\n\t"
        "s_branch .Lz30_%=\n\t"
        ".Ly30_%=:\n\t"
        "s_waitcnt lgkmcnt(4)\n\t"
        "v_mfma_f32_32x32x16_bf16 v[0:15], v[64:67], v[96:99], v[0:15]\n\t"
        "v_mfma_f32_32x32x16_bf16 v[16:31], v[68:71], v[96:99], v[16:31]\n\t"
        "v_mfma_f32_32x32x16_bf16 v[32:47], v[64:67], v[100:103], v[32:47]\n\t"
        "v_mfma_f32_32x32x16_bf16 v[48:63], v[68:71], v[100:103], v[48:63]\n\t"
        "ds_read_b64_tr_b16 v[64:65], %[wb0] offset:0\n\t"
        "ds_read_b64_tr_b16 v[66:67], %[wb0] offset:1024\n\t"
        "ds_read_b64_tr_b16 v[68:69], %[wb1] offset:0\n\t"
        "ds_read_b64_tr_b16 v[70:71], %[wb1] offset:1024\n\t"
        "s_add_u32 %[st], %[zo], 24576\n\t"
        "buffer_load_dwordx4 v[96:99], %[zv], %[rz], %[st] offen sc1\n\t"
        "buffer_load_dwordx4 v[100:103], %[zu], %[rz], %[st] offen sc1\n\t"
        "s_waitcnt vmcnt(6)\n\t"
        ".Lz31_%=:\n\t"
        "v_max3_u32 %[tm], v104, v105, v106\n\t"
        "v_max3_u32 %[t2], v107, v108, v109\n\t"
        "v_max3_u32 %[tm], %[tm], v110, v111\n\t"
        "v_max_u32 %[tm], %[tm], %[t2]\n\t"
        "v_cmp_eq_u32 vcc, -1, %[tm]\n\t"
        "s_cbranch_vccz .Ly31_%=\n\t"
        "s_sub_u32 %[sp], %[sp], 1\n\t"
        "s_cbranch_scc1 .Lzx_%=\n\t"
        "s_waitcnt vmcnt(0)\n\t"
        "s_sleep 1\n\t"
        "s_add_u32 %[st], %[zo], 17408\n\t"
        "buffer_load_dwordx4 v[104:107], %[zv], %[rz], %[st] offen sc1\n\t"
        "buffer_load_dwordx4 v[108:111], %[zu], %[rz], %[st] offen sc1\n\t"
        "v_max3_u32 %[tm], v80, v81, v82\n\t"
        "v_max3_u32 %[t2], v83, v84, v85\n\t"
        "v_max3_u32 %[tm], %[tm], v86, v87\n\t"
        "v_max_u32 %[tm], %[tm], %[t2]\n\t"
        "v_cmp_eq_u32 vcc, -1, %[tm]\n\t"
        "s_cbranch_vccz .Lr3132_%=\n\t"
        "s_add_u32 %[st], %[zo], 8192\n\t"
        "buffer_load_dwordx4 v[80:83], %[zv], %[rz], %[st] offen sc1\n\t"
        "buffer_load_dwordx4 v[84:87], %[zu], %[rz], %[st] offen sc1\n\t"
        ".Lr3132_%=:\n\t"
        "v_max3_u32 %[tm], v88, v89, v90\n\t"
        "v_max3_u32 %[t2], v91, v92, v93\n\t"
        "v_max3_u32 %[tm], %[tm], v94, v95\n\t"
        "v_max_u32 %[tm], %[tm], %[t2]\n\t"
        "v_cmp_eq_u32 vcc, -1, %[tm]\n\t"
        "s_cbranch_vccz .Lr3133_%=\n\t"
        "s_add_u32 %[st], %[zo], 9216\n\t"
        "buffer_load_dwordx4 v[88:91], %[zv], %[rz], %[st] offen sc1\n\t"
        "buffer_load_dwordx4 v[92:95], %[zu], %[rz], %[st] offen sc1\n\t"
        ".Lr3133_%=:\n\t"
        "v_max3_u32 %[tm], v96, v97, v98\n\t"
        "v_max3_u32 %[t2], v99, v100, v101\n\t"
        "v_max3_u32 %[tm], %[tm], v102, v103\n\t"
        "v_max_u32 %[tm], %[tm], %[t2]\n\t"
        "v_cmp_eq_u32 vcc, -1, %[tm]\n\t"
        "s_cbranch_vccz .Lr3134_%=\n\t"
        "s_add_u32 %[st], %[zo], 24576\n\t"
        "buffer_load_dwordx4 v[96:99], %[zv], %[rz], %[st] offen sc1\n\t"
        "buffer_load_dwordx4 v[100:103], %[zu], %[rz], %[st] offen sc1\n\t"
        ".Lr3134_%=:\n\t"
        "s_waitcnt vmcnt(0)\n\t"
        "s_branch .Lz31_%=\n\t"
        ".Ly31_%=:\n\t"
        "s_waitcnt lgkmcnt(4)\n\t"
        "v_mfma_f32_32x32x16_bf16 v[0:15], v[72:75], v[104:107], v[0:15]\n\t"
        "v_mfma_f32_32x32x16_bf16 v[16:31], v[76:79], v[104:107], v[16:31]\n\t"
        "v_mfma_f32_32x32x16_bf16 v[32:47], v[72:75], v[108:111], v[32:47]\n\t"
        "v_mfma_f32_32x32x16_bf16 v[48:63], v[76:79], v[108:111], v[48:63]\n\t"
        "ds_read_b64_tr_b16 v[72:73], %[wb0] offset:2048\n\t"
        "ds_read_b64_tr_b16 v[74:75], %[wb0] offset:3072\n\t"
        "ds_read_b64_tr_b16 v[76:77], %[wb1] offset:2048\n\t"
        "ds_read_b64_tr_b16 v[78:79], %[wb1] offset:3072\n\t"
        "s_add_u32 %[st], %[zo], 25600\n\t"
        "buffer_load_dwordx4 v[104:107], %[zv], %[rz], %[st] offen sc1\n\t"
        "buffer_load_dwordx4 v[108:111], %[zu], %[rz], %[st] offen sc1\n\t"
        "s_waitcnt vmcnt(6)\n\t"
        ".Lz32_%=:\n\t"
        "v_max3_u32 %[tm], v80, v81, v82\n\t"
        "v_max3_u32 %[t2], v83, v84, v85\n\t"
        "v_max3_u32 %[tm], %[tm], v86, v87\n\t"
        "v_max_u32 %[tm], %[tm], %[t2]\n\t"
        "v_cmp_eq_u32 vcc, -1, %[tm]\n\t"
        "s_cbranch_vccz .Ly32_%=\n\t"
        "s_sub_u32 %[sp], %[sp], 1\n\t"
        "s_cbranch_scc1 .Lzx_%=\n\t"
        "s_waitcnt vmcnt(0)\n\t"
        "s_sleep 1\n\t"
        "s_add_u32 %[st], %[zo], 8192\n\t"
        "buffer_load_dwordx4 v[80:83], %[zv], %[rz], %[st] offen sc1\n\t"
        "buffer_load_dwordx4 v[84:87], %[zu], %[rz], %[st] offen sc1\n\t"
        "v_max3_u32 %[tm], v88, v89, v90\n\t"
        "v_max3_u32 %[t2], v91, v92, v93\n\t"
        "v_max3_u32 %[tm], %[tm], v94, v95\n\t"
        "v_max_u32 %[tm], %[tm], %[t2]\n\t"
        "v_cmp_eq_u32 vcc, -1, %[tm]\n\t"
        "s_cbranch_vccz .Lr3233_%=\n\t"
        "s_add_u32 %[st], %[zo], 9216\n\t"
        "buffer_load_dwordx4 v[88:91], %[zv], %[rz], %[st] offen sc1\n\t"
        "buffer_load_dwordx4 v[92:95], %[zu], %[rz], %[st] offen sc1\n\t"
        ".Lr3233_%=:\n\t"
        "v_max3_u32 %[tm], v96, v97, v98\n\t"
        "v_max3_u32 %[t2], v99, v100, v101\n\t"
        "v_max3_u32 %[tm], %[tm], v102, v103\n\t"
        "v_max_u32 %[tm], %[tm], %[t2]\n\t"
        "v_cmp_eq_u32 vcc, -1, %[tm]\n\t"
        "s_cbranch_vccz .Lr3234_%=\n\t"
        "s_add_u32 %[st], %[zo], 24576\n\t"
        "buffer_load_dwordx4 v[96:99], %[zv], %[rz], %[st] offen sc1\n\t"
        "buffer_load_dwordx4 v[100:103], %[zu], %[rz], %[st] offen sc1\n\t"
        ".Lr3234_%=:\n\t"
        "v_max3_u32 %[tm], v104, v105, v106\n\t"
        "v_max3_u32 %[t2], v107, v108, v109\n\t"
        "v_max3_u32 %[tm], %[tm], v110, v111\n\t"
        "v_max_u32 %[tm], %[tm], %[t2]\n\t"
        "v_cmp_eq_u32 vcc, -1, %[tm]\n\t"
        "s_cbranch_vccz .Lr3235_%=\n\t"
        "s_add_u32 %[st], %[zo], 25600\n\t"
        "buffer_load_dwordx4 v[104:107], %[zv], %[rz], %[st] offen sc1\n\t"
        "buffer_load_dwordx4 v[108:111], %[zu], %[rz], %[st] offen sc1\n\t"
        ".Lr3235_%=:\n\t"
        "s_waitcnt vmcnt(0)\n\t"
        "s_branch .Lz32_%=\n\t"
        ".Ly32_%=:\n\t"
        "s_waitcnt lgkmcnt(4)\n\t"
        "v_mfma_f32_32x32x16_bf16 v[0:15], v[64:67], v[80:83], v[0:15]\n\t"
        "v_mfma_f32_32x32x16_bf16 v[16:31], v[68:71], v[80:83], v[16:31]\n\t"
        "v_mfma_f32_32x32x16_bf16 v[32:47], v[64:67], v[84:87], v[32:47]\n\t"
        "v_mfma_f32_32x32x16_bf16 v[48:63], v[68:71], v[84:87], v[48:63]\n\t"
        "ds_read_b64_tr_b16 v[64:65], %[wb0] offset:4096\n\t"
        "ds_read_b64_tr_b16 v[66:67], %[wb0] offset:5120\n\t"
        "ds_read_b64_tr_b16 v[68:69], %[wb1] offset:4096\n\t"
        "ds_read_b64_tr_b16 v[70:71], %[wb1] offset:5120\n\t"
        "s_waitcnt vmcnt(4)\n\t"
        ".Lz33_%=:\n\t"
        "v_max3_u32 %[tm], v88, v89, v90\n\t"
        "v_max3_u32 %[t2], v91, v92, v93\n\t"
        "v_max3_u32 %[tm], %[tm], v94, v95\n\t"
        "v_max_u32 %[tm], %[tm], %[t2]\n\t"
        "v_cmp_eq_u32 vcc, -1, %[tm]\n\t"
        "s_cbranch_vccz .Ly33_%=\n\t"
        "s_sub_u32 %[sp], %[sp], 1\n\t"
        "s_cbranch_scc1 .Lzx_%=\n\t"
        "s_waitcnt vmcnt(0)\n\t"
        "s_sleep 1\n\t"
        "s_add_u32 %[st], %[zo], 9216\n\t"
        "buffer_load_dwordx4 v[88:91], %[zv], %[rz], %[st] offen sc1\n\t"
        "buffer_load_dwordx4 v[92:95], %[zu], %[rz], %[st] offen sc1\n\t"
        "v_max3_u32 %[tm], v96, v97, v98\n\t"
        "v_max3_u32 %[t2], v99, v100, v101\n\t"
        "v_max3_u32 %[tm], %[tm], v102, v103\n\t"
        "v_max_u32 %[tm], %[tm], %[t2]\n\t"
        "v_cmp_eq_u32 vcc, -1, %[tm]\n\t"
        "s_cbranch_vccz .Lr3334_%=\n\t"
        "s_add_u32 %[st], %[zo], 24576\n\t"
        "buffer_load_dwordx4 v[96:99], %[zv], %[rz], %[st] offen sc1\n\t"
        "buffer_load_dwordx4 v[100:103], %[zu], %[rz], %[st] offen sc1\n\t"
        ".Lr3334_%=:\n\t"
        "v_max3_u32 %[tm], v104, v105, v106\n\t"
        "v_max3_u32 %[t2], v107, v108, v109\n\t"
        "v_max3_u32 %[tm], %[tm], v110, v111\n\t"
        "v_max_u32 %[tm], %[tm], %[t2]\n\t"
        "v_cmp_eq_u32 vcc, -1, %[tm]\n\t"
        "s_cbranch_vccz .Lr3335_%=\n\t"
        "s_add_u32 %[st], %[zo], 25600\n\t"
        "buffer_load_dwordx4 v[104:107], %[zv], %[rz], %[st] offen sc1\n\t"
        "buffer_load_dwordx4 v[108:111], %[zu], %[rz], %[st] offen sc1\n\t"
        ".Lr3335_%=:\n\t"
        "s_waitcnt vmcnt(0)\n\t"
        "s_branch .Lz33_%=\n\t"
        ".Ly33_%=:\n\t"
        "s_waitcnt lgkmcnt(4)\n\t"
        "v_mfma_f32_32x32x16_bf16 v[0:15], v[72:75], v[88:91], v[0:15]\n\t"
        "v_mfma_f32_32x32x16_bf16 v[16:31], v[76:79], v[88:91], v[16:31]\n\t"
        "v_mfma_f32_32x32x16_bf16 v[32:47], v[72:75], v[92:95], v[32:47]\n\t"
        "v_mfma_f32_32x32x16_bf16 v[48:63], v[76:79], v[92:95], v[48:63]\n\t"
        "ds_read_b64_tr_b16 v[72:73], %[wb0] offset:6144\n\t"
        "ds_read_b64_tr_b16 v[74:75], %[wb0] offset:7168\n\t"
        "ds_read_b64_tr_b16 v[76:77], %[wb1] offset:6144\n\t"
        "ds_read_b64_tr_b16 v[78:79], %[wb1] offset:7168\n\t"
        "s_waitcnt vmcnt(2)\n\t"
        ".Lz34_%=:\n\t"
        "v_max3_u32 %[tm], v96, v97, v98\n\t"
        "v_max3_u32 %[t2], v99, v100, v101\n\t"
        "v_max3_u32 %[tm], %[tm], v102, v103\n\t"
        "v_max_u32 %[tm], %[tm], %[t2]\n\t"
        "v_cmp_eq_u32 vcc, -1, %[tm]\n\t"
        "s_cbranch_vccz .Ly34_%=\n\t"
        "s_sub_u32 %[sp], %[sp], 1\n\t"
        "s_cbranch_scc1 .Lzx_%=\n\t"
        "s_waitcnt vmcnt(0)\n\t"
        "s_sleep 1\n\t"
        "s_add_u32 %[st], %[zo], 24576\n\t"
        "buffer_load_dwordx4 v[96:99], %[zv], %[rz], %[st] offen sc1\n\t"
        "buffer_load_dwordx4 v[100:103], %[zu], %[rz], %[st] offen sc1\n\t"
        "v_max3_u32 %[tm], v104, v105, v106\n\t"
        "v_max3_u32 %[t2], v107, v108, v109\n\t"
        "v_max3_u32 %[tm], %[tm], v110, v111\n\t"
        "v_max_u32 %[tm], %[tm], %[t2]\n\t"
        "v_cmp_eq_u32 vcc, -1, %[tm]\n\t"
        "s_cbranch_vccz .Lr3435_%=\n\t"
        "s_add_u32 %[st], %[zo], 25600\n\t"
        "buffer_load_dwordx4 v[104:107], %[zv], %[rz], %[st] offen sc1\n\t"
        "buffer_load_dwordx4 v[108:111], %[zu], %[rz], %[st] offen sc1\n\t"
        ".Lr3435_%=:\n\t"
        "s_waitcnt vmcnt(0)\n\t"
        "s_branch .Lz34_%=\n\t"
        ".Ly34_%=:\n\t"
        "s_waitcnt lgkmcnt(4)\n\t"
        "v_mfma_f32_32x32x16_bf16 v[0:15], v[64:67], v[96:99], v[0:15]\n\t"
        "v_mfma_f32_32x32x16_bf16 v[16:31], v[68:71], v[96:99], v[16:31]\n\t"
        "v_mfma_f32_32x32x16_bf16 v[32:47], v[64:67], v[100:103], v[32:47]\n\t"
        "v_mfma_f32_32x32x16_bf16 v[48:63], v[68:71], v[100:103], v[48:63]\n\t"
        "s_waitcnt vmcnt(0)\n\t"
        ".Lz35_%=:\n\t"
        "v_max3_u32 %[tm], v104, v105, v106\n\t"
        "v_max3_u32 %[t2], v107, v108, v109\n\t"
        "v_max3_u32 %[tm], %[tm], v110, v111\n\t"
        "v_max_u32 %[tm], %[tm], %[t2]\n\t"
        "v_cmp_eq_u32 vcc, -1, %[tm]\n\t"
        "s_cbranch_vccz .Ly35_%=\n\t"
        "s_sub_u32 %[sp], %[sp], 1\n\t"
        "s_cbranch_scc1 .Lzx_%=\n\t"
        "s_waitcnt vmcnt(0)\n\t"
        "s_sleep 1\n\t"
        "s_add_u32 %[st], %[zo], 25600\n\t"
        "buffer_load_dwordx4 v[104:107], %[zv], %[rz], %[st] offen sc1\n\t"
        "buffer_load_dwordx4 v[108:111], %[zu], %[rz], %[st] offen sc1\n\t"
        "s_waitcnt vmcnt(0)\n\t"
        "s_branch .Lz35_%=\n\t"
        ".Ly35_%=:\n\t"
        "s_waitcnt lgkmcnt(0)\n\t"
        "v_mfma_f32_32x32x16_bf16 v[0:15], v[72:75], v[104:107], v[0:15]\n\t"
        "v_mfma_f32_32x32x16_bf16 v[16:31], v[76:79], v[104:107], v[16:31]\n\t"
        "v_mfma_f32_32x32x16_bf16 v[32:47], v[72:75], v[108:111], v[32:47]\n\t"
        "v_mfma_f32_32x32x16_bf16 v[48:63], v[76:79], v[108:111], v[48:63]\n\t"
        ".Lzx_%=:\n\t"
        "s_waitcnt vmcnt(0) lgkmcnt(0)\n\t"
        "s_nop 11\n\t"
            : "=&{v[0:15]}"(c0), "=&{v[16:31]}"(c1), "=&{v[32:47]}"(c2), "=&{v[48:63]}"(c3), [sv] "=&s"(sv), [st] "=&s"(st), [kw] "+s"(kw), [kz] "+s"(kz),
              [tm] "=&v"(tm), [t2] "=&v"(t2), [sp] "=&s"(sp)
            : [wv] "v"(wv), [wd] "s"(wd), [r2] "s"(r2), [ro] "s"(mk_rsrc(wo, DM * DM * 2)), [rz] "s"(rz), [s2] "s"(448 * 1024 * kq), [so] "s"(32 * 1024 * kq),
              [zv] "v"(zv), [zu] "v"(zv + ZT), [z2] "s"(28 * 1024 * kq), [zo] "s"((112 + 2 * kq) * 1024),
              [wb0] "v"(wbl + (kq >> 1) * 256), [wb1] "v"(wbl + ((kq >> 1) ^ 1) * 256),
              [hc] "v"(lds0 + L_CNT + (lane & 7) * 32), [lim] "s"(14 * 0xDDDD)
            : "memory", "scc", "vcc", "v64", "v65", "v66", "v67", "v68", "v69", "v70", "v71", "v72", "v73", "v74", "v75", "v76", "v77", "v78", "v79", "v80", "v81", "v82", "v83", "v84", "v85", "v86", "v87", "v88", "v89", "v90", "v91", "v92", "v93", "v94", "v95", "v96", "v97", "v98", "v99", "v100", "v101", "v102", "v103", "v104", "v105", "v106", "v107", "v108", "v109", "v110", "v111");
    }

    {
        const int ln = lane_id();
        const ldsp mine = L + L_F + kq * 16384 + ln * 16;
#pragma unroll
        for (int i = 0; i < 2; ++i) {
            *(LAS f32x4*)(mine + i * 1024) = f32x4{c0[8 + 4 * i], c0[9 + 4 * i], c0[10 + 4 * i], c0[11 + 4 * i]};
            *(LAS f32x4*)(mine + 2048 + i * 1024) = f32x4{c2[8 + 4 * i], c2[9 + 4 * i], c2[10 + 4 * i], c2[11 + 4 * i]};
        }
#pragma unroll
        for (int i = 0; i < 4; ++i) {
            *(LAS f32x4*)(mine + 4096 + (i >> 1) * 4096 + (i & 1) * 1024) = f32x4{c1[4 * i], c1[4 * i + 1], c1[4 * i + 2], c1[4 * i + 3]};
            *(LAS f32x4*)(mine + 6144 + (i >> 1) * 4096 + (i & 1) * 1024) = f32x4{c3[4 * i], c3[4 * i + 1], c3[4 * i + 2], c3[4 * i + 3]};
        }
        asm volatile("" ::: "memory");
        if (ln == 0) __hip_atomic_fetch_add((LAS unsigned*)(cw + C_SUM), 1u, __ATOMIC_RELAXED, __HIP_MEMORY_SCOPE_WORKGROUP);
        lds_wait_ge<0>(cw + C_SUM, 4u);

        f32x4 s[12];
#pragma unroll
        for (int i = 0; i < 12; ++i) s[i] = *(const LAS f32x4*)(L + L_F + (kq ^ ((i >> 2) + 1)) * 16384 + i * 1024 + ln * 16);
        __builtin_amdgcn_sched_barrier(0);
        s[0] += f32x4{c0[0], c0[1], c0[2], c0[3]}; s[1] += f32x4{c0[4], c0[5], c0[6], c0[7]};
        s[2] += f32x4{c2[0], c2[1], c2[2], c2[3]}; s[3] += f32x4{c2[4], c2[5], c2[6], c2[7]};
#pragma unroll
        for (int i = 4; i < 12; ++i) s[i & 3] += s[i];
#pragma unroll
        for (int t = 0; t < 2; ++t) {
            const auto r0 = __builtin_amdgcn_permlane32_swap(pk2(s[2 * t][0], s[2 * t][1]), pk2(s[2 * t + 1][0], s[2 * t + 1][1]), false, false);
            const auto r1 = __builtin_amdgcn_permlane32_swap(pk2(s[2 * t][2], s[2 * t][3]), pk2(s[2 * t + 1][2], s[2 * t + 1][3]), false, false);
            const u32x4 ov = {r0[0], r1[0], r0[1], r1[1]};
            *(GAS u32x4*)((GAS char*)out + (size_t)(rb64 * 64 + t * 32 + (ln & 31)) * 1024 + cb64 * 128 + kq * 32 + (ln >> 5) * 16) = ov;
        }
    }

}

__global__ __launch_bounds__(1024, 4) void fused(const void* x, const void* wqkv, const void* wo, const void* w1, const void* w2,
                                                 const void* gate_w, void* out, char* ws) {
    extern __shared__ __attribute__((aligned(16))) char smem[];
    const ldsp L = (ldsp)smem;
    const unsigned lds0 = (unsigned)(uintptr_t)L;

    Stage sg;
    {
        unsigned sv, st, s1, s2, wu, wp, t0, t1, t2;
        asm volatile(
        "s_load_dwordx2 s[40:41], %[ka], 0\n\t"
        "v_readfirstlane_b32 %[w], %[tid]\n\t"
        "s_load_dwordx2 s[48:49], %[ka], 40\n\t"
        "v_and_b32 %[t0], 63, %[tid]\n\t"
        "s_lshr_b32 %[w], %[w], 6\n\t"
        "s_cmp_lt_u32 %[w], 8\n\t"
        "s_cselect_b32 %[st], 8, 24\n\t"
        "s_load_dwordx2 s[44:45], %[ka], %[st]\n\t"
        "s_cselect_b32 %[wu], 3, 4\n\t"
        "s_cselect_b32 %[wp], 6, 7\n\t"
        "s_cselect_b32 %[s1], 15, 3\n\t"
        "s_cselect_b32 %[s2], 0, 4\n\t"
        "s_cselect_b32 %[wd], 0, 28\n\t"
        "s_lshl_b32 %[wu], %[wu], 11\n\t"
        "s_lshl_b32 s46, %[wp], 18\n\t"
        "s_lshl_b32 %[wp], %[wp], 9\n\t"
        "v_and_b32 %[t1], 0x30, %[t0]\n\t"
        "v_lshlrev_b32 %[xv], 11, %[t1]\n\t"
        "v_mul_u32_u24 %[t1], %[s2], %[t1]\n\t"
        "v_bfe_u32 %[t2], %[t0], 3, 1\n\t"
        "v_lshl_or_b32 %[xv], %[t2], 10, %[xv]\n\t"
        "v_and_b32 %[t2], 7, %[t0]\n\t"
        "v_lshl_or_b32 %[xv], %[t2], 4, %[xv]\n\t"
        "v_lshrrev_b32 %[t2], 2, %[t0]\n\t"
        "v_and_b32 %[t2], %[s1], %[t2]\n\t"
        "v_mad_u32_u24 %[wv], %[t2], %[wp], %[t1]\n\t"
        "s_and_b32 %[s1], %[w], 14\n\t"
        "s_cmp_eq_u32 %[s1], 2\n\t"
        "s_cselect_b32 %[s1], 2, 0\n\t"
        "v_and_b32 %[t2], 3, %[t0]\n\t"
        "v_xor_b32 %[t2], %[s1], %[t2]\n\t"
        "v_lshl_add_u32 %[wv], %[t2], 4, %[wv]\n\t"
        "v_lshlrev_b32 %[t1], 4, %[t0]\n\t"
        "v_lshlrev_b32 %[t0], 7, %[t0]\n\t"
        "s_and_b32 %[hp], %[a], 3\n\t"
        "s_lshl_b32 %[hp], %[hp], 2\n\t"
        "s_bfe_u32 %[st], %[a], 0x20003\n\t"
        "s_or_b32 %[hp], %[hp], %[st]\n\t"
        "s_lshl_b32 %[rb], %[a], 1\n\t"
        "s_and_b32 %[rb], %[rb], 8\n\t"
        "s_lshr_b32 %[st], %[a], 5\n\t"
        "s_or_b32 %[rb], %[rb], %[st]\n\t"
        "s_lshl_b32 %[xs], %[rb], 6\n\t"
        "s_add_u32 %[xs], %[xs], %[w]\n\t"
        "s_lshl_b32 %[xs], %[xs], 11\n\t"
        "s_mul_i32 %[xd], %[w], 1040\n\t"
        "s_add_u32 %[wd], %[wd], %[w]\n\t"
        "s_lshl_b32 %[wd], %[wd], 10\n\t"
        "s_add_u32 %[wd], %[wd], 66560\n\t"
        "s_mov_b32 s42, 2097152\n\t"
        "s_mov_b32 s43, 0x20000\n\t"
        "s_mov_b32 s47, 0x20000\n\t"
        "s_mov_b32 s50, 1024\n\t"
        "s_mov_b32 s51, 0x20000\n\t"
        "s_cmp_lt_u32 %[w], 8\n\t"
        "s_cbranch_scc0 .Lpf_%=\n\t"
        "s_and_b32 %[st], %[w], 1\n\t"
        "s_mul_i32 %[w0], %[st], 49152\n\t"
        "s_lshr_b32 %[st], %[w], 1\n\t"
        "s_lshl_b32 %[st], %[st], 10\n\t"
        "s_add_u32 %[w0], %[w0], %[st]\n\t"
        "s_lshl_b32 %[st], %[hp], 6\n\t"
        "s_add_u32 %[w0], %[w0], %[st]\n\t"
        "s_cmp_lt_u32 %[w], 6\n\t"
        "s_branch .Lpj_%=\n\t"
        ".Lpf_%=:\n\t"
        "s_mul_i32 %[w0], %[w], 14336\n\t"
        "s_add_u32 %[s1], %[hp], 8\n\t"
        "s_and_b32 %[s1], %[s1], 15\n\t"
        "s_lshl_b32 %[st], %[s1], 8\n\t"
        "s_add_u32 %[w0], %[w0], %[st]\n\t"
        "s_sub_u32 %[w0], %[w0], 114688\n\t"
        "s_cmp_lt_u32 %[s1], 14\n\t"
        ".Lpj_%=:\n\t"
        "s_cselect_b32 %[st], 0, 0x40000000\n\t"
        "s_cselect_b32 %[wd], %[wd], 0x30000\n\t"
        "v_add_u32 %[wv], %[st], %[wv]\n\t"
        "s_lshl_b32 %[wp], %[wp], 5\n\t"
        "s_bfe_u32 %[kx], %[a], 0x20003\n\t"
        "s_mul_i32 %[kx], %[kx], 2\n\t"
        "s_mul_i32 %[st], %[rb], 2\n\t"
        "s_add_u32 %[kx], %[kx], %[st]\n\t"
        "s_and_b32 %[kx], %[kx], 7\n\t"
        "s_lshl_b32 %[ku], %[kx], 1\n\t"
        "s_lshl_b32 %[kx], %[kx], 7\n\t"
        "v_add_u32 %[t1], %[kx], %[t1]\n\t"
        "v_and_b32 %[t1], 0x3ff, %[t1]\n\t"
        "s_mov_b32 %[sv], m0\n\t"
        "s_mov_b32 m0, 152576\n\t"
        "s_waitcnt lgkmcnt(0)\n\t"
        "s_cmp_eq_u32 %[w], 7\n\t"
        "s_cbranch_scc0 .Lng_%=\n\t"
        "buffer_load_dwordx4 %[t1], s[48:51], 0 offen lds\n\t"
        ".Lng_%=:\n\t"
        "s_cmp_eq_u32 %[w], 15\n\t"
        "s_cbranch_scc0 .Lnc_%=\n\t"
        "s_getpc_b64 s[48:49]\n\t"
        "s_and_b32 s48, s48, 0xffffff00\n\t"
        "s_mov_b32 s50, 40704\n\t"
        "s_and_b32 s49, s49, 0xffff\n\t"
        "s_lshr_b32 %[st], %[a], 3\n\t"
        "s_lshl_b32 %[st], %[st], 11\n\t"
        "v_add_u32 %[t2], %[st], %[t0]\n\t"
        "s_mov_b32 m0, 154624\n\t"
        "s_mov_b64 exec, 0xffff\n\t"
        "buffer_load_dword %[t2], s[48:51], 0 offen lds\n\t"
        "s_mov_b64 exec, -1\n\t"
        ".Lnc_%=:\n\t"
        "s_add_u32 m0, %[xd], 0\n\t"
        "s_add_u32 %[st], %[xs], %[kx]\n\t"
        "buffer_load_dwordx4 %[xv], s[40:43], %[st] offen lds\n\t"
        "s_add_u32 %[kx], %[kx], 128\n\t"
        "s_and_b32 %[kx], %[kx], 0x3ff\n\t"
        "s_mov_b32 m0, %[wd]\n\t"
        "s_mov_b32 %[s1], %[wd]\n\t"
        "s_mul_i32 %[st], %[ku], %[wp]\n\t"
        "s_add_u32 %[st], %[st], %[w0]\n\t"
        "buffer_load_dwordx4 %[wv], s[44:47], %[st] offen lds\n\t"
        "s_add_u32 %[ku], %[ku], 1\n\t"
        "s_and_b32 %[ku], %[ku], 15\n\t"
        "s_barrier\n\t"
        "s_add_u32 %[s1], %[s1], %[wu]\n\t"
        "s_mov_b32 m0, %[s1]\n\t"
        "s_mul_i32 %[st], %[ku], %[wp]\n\t"
        "s_add_u32 %[st], %[st], %[w0]\n\t"
        "buffer_load_dwordx4 %[wv], s[44:47], %[st] offen lds\n\t"
        "s_add_u32 %[ku], %[ku], 1\n\t"
        "s_and_b32 %[ku], %[ku], 15\n\t"
        "s_add_u32 m0, %[xd], 16640\n\t"
        "s_add_u32 %[st], %[xs], %[kx]\n\t"
        "buffer_load_dwordx4 %[xv], s[40:43], %[st] offen lds\n\t"
        "s_add_u32 %[kx], %[kx], 128\n\t"
        "s_and_b32 %[kx], %[kx], 0x3ff\n\t"
        "s_add_u32 %[s1], %[s1], %[wu]\n\t"
        "s_mov_b32 m0, %[s1]\n\t"
        "s_mul_i32 %[st], %[ku], %[wp]\n\t"
        "s_add_u32 %[st], %[st], %[w0]\n\t"
        "buffer_load_dwordx4 %[wv], s[44:47], %[st] offen lds\n\t"
        "s_add_u32 %[ku], %[ku], 1\n\t"
        "s_and_b32 %[ku], %[ku], 15\n\t"
        "s_mov_b32 m0, %[sv]\n\t"
            : "=&{s[40:43]}"(sg.rx), "=&{s[44:47]}"(sg.rw), [sv] "=&s"(sv), [st] "=&s"(st), [s1] "=&s"(s1), [s2] "=&s"(s2), [wu] "=&s"(wu), [wp] "=&s"(wp),
              [xs] "=&s"(sg.xs), [xd] "=&s"(sg.xd), [wd] "=&s"(sg.wd), [w0] "=&s"(sg.w0), [kx] "=&s"(sg.kx), [ku] "=&s"(sg.ku), [w] "=&s"(sg.w16), [hp] "=&s"(sg.hp), [rb] "=&s"(sg.rb),
              [xv] "=&v"(sg.xv), [wv] "=&v"(sg.wv), [t0] "=&v"(t0), [t1] "=&v"(t1), [t2] "=&v"(t2)
            : [ka] "s"(__builtin_amdgcn_kernarg_segment_ptr()), [tid] "v"(threadIdx.x), [a] "s"(blockIdx.x)
            : "memory", "scc", "vcc", "s48", "s49", "s50", "s51");
    }
    const bool isA = sg.w16 < 8;
    __builtin_amdgcn_sched_barrier(0);
    if (__builtin_expect(!isA, 0)) {
        if (sg.w16 < 12) role_f(sg, wo, w2, out, ws, L, lds0);
        else role_kv(sg, ws, L, lds0);
    } else role_a(sg, ws, L, lds0);
}
}

extern "C" void kernel_launch(void* const* d_in, const int* in_sizes, int n_in, void* d_out, int out_size, void* d_ws, size_t ws_size,
                              hipStream_t stream) {
    (void)in_sizes; (void)n_in; (void)out_size; (void)ws_size;
    (void)hipFuncSetAttribute((const void*)gm::fused, hipFuncAttributeMaxDynamicSharedMemorySize, gm::LDS_BYTES);
    gm::fused<<<256, 1024, gm::LDS_BYTES, stream>>>(d_in[0], d_in[1], d_in[2], d_in[4], d_in[5], d_in[6], d_out, (char*)d_ws);
}
```
